```python
import torch

def _softmax(x, axis=-1):
    return torch.softmax(x, dim=axis)
def _gelu(x):
    return torch.nn.functional.gelu(x, approximate="tanh")
def _silu(x):
    return torch.nn.functional.silu(x)
def _sigmoid(x):
    return torch.sigmoid(x)
def _layernorm(x, axis=-1, eps=1e-5):
    m = x.mean(dim=axis, keepdim=True)
    v = x.var(dim=axis, keepdim=True, unbiased=False)
    return (x - m) / torch.sqrt(v + eps)
def _scatter_add(tgt, idx, src, axis=0):
    return tgt.clone().index_add_(axis, idx, src)
def _segment_sum(src, seg, n):
    out = torch.zeros((n,) + src.shape[1:], dtype=src.dtype)
    return out.index_add_(0, seg, src)
def _pad(x, pw):
    flat = tuple(v for pair in reversed(pw) for v in pair)
    return torch.nn.functional.pad(x, flat)

kernel_name = 'sequential_preLN__softmax_full_mha__glu_gated_bilinear__prompt'

def setup_inputs():

    _w = (lambda a: (lambda t: t.to(torch.bfloat16) if t.is_floating_point() else t)(torch.as_tensor(a)))
    def _A_setup():
        B, S, D = 1, 2048, 576
        return {
            "x": _w((lambda *s: torch.randn(*s, dtype=torch.bfloat16) * 0.05)(B, S, D)),
            "wqkv": _w((lambda *s: torch.randn(*s, dtype=torch.bfloat16) * 0.05)(D, 3 * D)),
            "wo": _w((lambda *s: torch.randn(*s, dtype=torch.bfloat16) * 0.05)(D, D)),
        }
    _A_in = _A_setup()

    def _B_setup():
        B, S, D, F = 1, 2048, 576, 4608
        return {
            "x": _w((lambda *s: torch.randn(*s, dtype=torch.bfloat16) * 0.05)(B, S, D)),
            "w_gate": _w((lambda *s: torch.randn(*s, dtype=torch.bfloat16) * 0.4)(D, F) * (D ** -0.5)),
            "w_up": _w((lambda *s: torch.randn(*s, dtype=torch.bfloat16) * 0.4)(D, F) * (D ** -0.5)),
            "w_down": _w((lambda *s: torch.randn(*s, dtype=torch.bfloat16) * 0.4)(F, D) * (F ** -0.5)),
        }
    _B_in = _B_setup()

    return {
        "A_x": _A_in["x"],
        "A_wqkv": _A_in["wqkv"],
        "A_wo": _A_in["wo"],
        "B_x": _B_in["x"],
        "B_w_gate": _B_in["w_gate"],
        "B_w_up": _B_in["w_up"],
        "B_w_down": _B_in["w_down"],
    }

def reference(A_x, A_wqkv, A_wo, B_x, B_w_gate, B_w_up, B_w_down):
    def _A_ref(x, wqkv, wo):
        B, S, D = x.shape
        H = 32; Dh = D // H
        qkv = (lambda x, *d: x.permute(*d))((x @ wqkv).reshape(B, S, 3, H, Dh), 2, 0, 3, 1, 4)
        q, k, v = qkv[0], qkv[1], qkv[2]
        scale = Dh ** -0.5
        scores = torch.einsum("bhqd,bhkd->bhqk", q, k) * scale
        p = _softmax(scores, axis=-1)
        o = torch.einsum("bhqk,bhkd->bhqd", p, v)
        return (lambda x, *d: x.permute(*d))(o, 0, 2, 1, 3).reshape(B, S, D) @ wo

    def _B_ref(x, w_gate, w_up, w_down):
        g = torch.einsum("bsd,df->bsf", x, w_gate)
        u = torch.einsum("bsd,df->bsf", x, w_up)
        h = g * u
        return torch.einsum("bsf,fd->bsd", h, w_down)

    x = A_x
    h = x + _A_ref(_layernorm(x, axis=-1), A_wqkv, A_wo)
    h2 = h + _B_ref(_layernorm(h, axis=-1), B_w_gate, B_w_up, B_w_down)
    return h2
```

```cpp
#include <hip/hip_runtime.h>
#include <hip/hip_bf16.h>
#include <stdint.h>
typedef unsigned short u16;
__device__ __forceinline__ float b2f(u16 v){ return __uint_as_float(((unsigned)v)<<16); }
__device__ __forceinline__ u16 f2b(float f){ __hip_bfloat16 h=__float2bfloat16(f); return *(u16*)&h; }
constexpr int S=2048,D=576,H=32,DH=18,F=4608;

__global__ void ln_k(const u16* x, u16* y){
  int r=blockIdx.x; int t=threadIdx.x;
  float v[9]; float s=0;
  for(int i=0;i<9;i++){ v[i]=b2f(x[r*D+t+64*i]); s+=v[i]; }
  for(int o=32;o;o>>=1) s+=__shfl_xor(s,o);
  float m=s/D; float q=0;
  for(int i=0;i<9;i++){ float d=v[i]-m; q+=d*d; }
  for(int o=32;o;o>>=1) q+=__shfl_xor(q,o);
  float rs=1.0f/sqrtf(q/D+1e-5f);
  for(int i=0;i<9;i++) y[r*D+t+64*i]=f2b((v[i]-m)*rs);
}
template<int MODE>
__global__ void gemm_k(const u16* A,const u16* B,const u16* B2,const u16* res,u16* C,int M,int N,int K){
  __shared__ float As[16][65], Bs[16][65], Bs2[16][65];
  int tx=threadIdx.x&15, ty=threadIdx.x>>4; int bm=blockIdx.y*64,bn=blockIdx.x*64;
  float acc[4][4]={},acc2[4][4]={};
  for(int k0=0;k0<K;k0+=16){
    for(int i=threadIdx.x;i<1024;i+=256){ int m=i>>4,k=i&15; As[k][m]=b2f(A[(size_t)(bm+m)*K+k0+k]); }
    for(int i=threadIdx.x;i<1024;i+=256){ int k=i>>6,n=i&63; Bs[k][n]=b2f(B[(size_t)(k0+k)*N+bn+n]); if(MODE==2) Bs2[k][n]=b2f(B2[(size_t)(k0+k)*N+bn+n]); }
    __syncthreads();
    for(int k=0;k<16;k++){ for(int i=0;i<4;i++) for(int j=0;j<4;j++){ acc[i][j]+=As[k][ty*4+i]*Bs[k][tx*4+j]; if(MODE==2) acc2[i][j]+=As[k][ty*4+i]*Bs2[k][tx*4+j]; } }
    __syncthreads();
  }
  for(int i=0;i<4;i++) for(int j=0;j<4;j++){ size_t o=(size_t)(bm+ty*4+i)*N+bn+tx*4+j; float v=acc[i][j]; if(MODE==1) v+=b2f(res[o]); if(MODE==2) v*=acc2[i][j]; C[o]=f2b(v);}
}
__global__ void attn_k(const u16* qkv,u16* o){
  int h=blockIdx.y; int q=blockIdx.x*256+threadIdx.x;
  __shared__ float Ks[64][DH], Vs[64][DH];
  float qv[DH],acc[DH]; for(int d=0;d<DH;d++){ qv[d]=b2f(qkv[(size_t)q*3*D+h*DH+d])*0.23570226f; acc[d]=0; }
  float m=-1e30f,l=0;
  for(int k0=0;k0<S;k0+=64){
    for(int i=threadIdx.x;i<64*DH;i+=256){ int k=i/DH,d=i%DH; Ks[k][d]=b2f(qkv[(size_t)(k0+k)*3*D+D+h*DH+d]); Vs[k][d]=b2f(qkv[(size_t)(k0+k)*3*D+2*D+h*DH+d]); }
    __syncthreads();
    for(int k=0;k<64;k++){ float s=0; for(int d=0;d<DH;d++) s+=qv[d]*Ks[k][d]; float mn=fmaxf(m,s); float a=__expf(m-mn),p=__expf(s-mn); l=l*a+p; for(int d=0;d<DH;d++) acc[d]=acc[d]*a+p*Vs[k][d]; m=mn; }
    __syncthreads();
  }
  for(int d=0;d<DH;d++) o[(size_t)q*D+h*DH+d]=f2b(acc[d]/l);
}
extern "C" void kernel_launch(void* const* d_in, const int* in_sizes, int n_in, void* d_out, int out_size, void* d_ws, size_t ws_size, hipStream_t stream){
  const u16* x=(const u16*)d_in[0]; const u16* wqkv=(const u16*)d_in[1]; const u16* wo=(const u16*)d_in[2];
  const u16* wg=(const u16*)d_in[4]; const u16* wu=(const u16*)d_in[5]; const u16* wd=(const u16*)d_in[6];
  u16* ws=(u16*)d_ws; u16* xn=ws; u16* qkv=xn+S*D; u16* o=qkv+S*3*D; u16* h=o+S*D; u16* hn=h+S*D; u16* ff=hn+S*D;
  ln_k<<<S,64,0,stream>>>(x,xn);
  gemm_k<0><<<dim3(3*D/64,S/64),256,0,stream>>>(xn,wqkv,nullptr,nullptr,qkv,S,3*D,D);
  attn_k<<<dim3(S/256,H),256,0,stream>>>(qkv,o);
  gemm_k<1><<<dim3(D/64,S/64),256,0,stream>>>(o,wo,nullptr,x,h,S,D,D);
  ln_k<<<S,64,0,stream>>>(h,hn);
  gemm_k<2><<<dim3(F/64,S/64),256,0,stream>>>(hn,wg,wu,nullptr,ff,S,F,D);
  gemm_k<1><<<dim3(D/64,S/64),256,0,stream>>>(ff,wd,nullptr,h,(u16*)d_out,S,D,F);
}
```

```cpp
#include <hip/hip_runtime.h>
#include <hip/hip_bf16.h>

extern "C" __global__ __launch_bounds__(512, 2) __attribute__((aligned(4096))) void k_main(char* ws, const char* x, const char* wg, const char* wu, const char* wd, char* out, const char* wqkv, const char* wo) {
  __shared__ __attribute__((aligned(16))) char lds[163840];
  unsigned tid = threadIdx.x + (unsigned)(size_t)lds;
  asm volatile(
    "s_load_dwordx4 s[4:7], s[0:1], 0\n"
    "s_load_dwordx4 s[56:59], s[0:1], 48\n"
    "s_load_dwordx8 s[8:15], s[0:1], 16\n"
    "v_readfirstlane_b32 s3, v0\n"
    "v_and_b32 v0, 63, v0\n"
    "v_lshlrev_b32 v1, 4, v0\n"
    "s_lshr_b32 s3, s3, 6\n"
    "s_mov_b32 s18, 54906880\n"
    "s_mov_b32 s19, 0x20000\n"
    "s_mov_b32 s31, 0x20000\n"
    "s_cmp_ge_u32 s3, 2\n"
    "s_cbranch_scc1 kb_pl1_%=\n"
    "s_and_b32 s43, s2, 7\n"
    "s_lshl_b32 s39, s3, 8\n"
    "s_and_b32 s51, s2, 0xe0\n"
    "s_add_u32 s39, s39, s51\n"
    "s_lshl2_add_u32 s43, s43, s39\n"
    "s_bfe_u32 s39, s2, 0x20003\n"
    "s_add_u32 s43, s43, s39\n"
    "s_lshr_b32 s44, s43, 2\n"
    "s_mul_i32 s44, s44, 0x6c00\n"
    "s_and_b32 s39, s43, 3\n"
    "s_mul_i32 s39, s39, 0x120\n"
    "s_add_u32 s44, s44, s39\n"
    "s_lshl_b32 s45, s3, 13\n"
    "s_add_u32 s45, s45, 0x24000\n"
    "v_mul_u32_u24 v2, 0xe39, v0\n"
    "v_lshrrev_b32 v2, 16, v2\n"
    "v_mul_u32_u24 v2, 0x360, v2\n"
    "v_add_u32 v2, v2, v1\n"
    "s_mov_b32 m0, s45\n"
    "s_cmp_lt_u32 s43, 288\n"
    "s_cselect_b32 s30, 1990656, 0\n"
    "s_mov_b32 exec_hi, 0x3fffff\n"
    "s_waitcnt lgkmcnt(0)\n"
    "s_mov_b32 s28, s56\n"
    "s_and_b32 s29, s57, 0xffff\n"
    "buffer_load_dwordx4 v2, s[28:31], s44 offen nt lds\n"
    "s_add_u32 m0, m0, 864\n"
    "s_add_u32 s44, s44, 3456\n"
    "buffer_load_dwordx4 v2, s[28:31], s44 offen nt lds\n"
    "s_add_u32 m0, m0, 864\n"
    "s_add_u32 s44, s44, 3456\n"
    "buffer_load_dwordx4 v2, s[28:31], s44 offen nt lds\n"
    "s_add_u32 m0, m0, 864\n"
    "s_add_u32 s44, s44, 3456\n"
    "buffer_load_dwordx4 v2, s[28:31], s44 offen nt lds\n"
    "s_add_u32 m0, m0, 864\n"
    "s_add_u32 s44, s44, 3456\n"
    "buffer_load_dwordx4 v2, s[28:31], s44 offen nt lds\n"
    "s_add_u32 m0, m0, 864\n"
    "s_add_u32 s44, s44, 3456\n"
    "buffer_load_dwordx4 v2, s[28:31], s44 offen nt lds\n"
    "s_add_u32 m0, m0, 864\n"
    "s_add_u32 s44, s44, 3456\n"
    "buffer_load_dwordx4 v2, s[28:31], s44 offen nt lds\n"
    "s_add_u32 m0, m0, 864\n"
    "s_add_u32 s44, s44, 3456\n"
    "buffer_load_dwordx4 v2, s[28:31], s44 offen nt lds\n"
    "s_mov_b32 exec_hi, -1\n"
    "s_branch kb_pw2_%=\n"
    "kb_pl1_%=:\n"
    "s_cmp_ge_u32 s3, 5\n"
    "s_cbranch_scc1 kb_po3_%=\n"
    "s_mul_i32 s39, s2, 0x2400\n"
    "s_sub_u32 s51, s3, 2\n"
    "s_mul_i32 s51, s51, 0x180\n"
    "s_add_u32 s39, s39, s51\n"
    "v_lshrrev_b32 v2, 3, v0\n"
    "v_mul_u32_u24 v2, 0x480, v2\n"
    "v_add_u32 v2, s39, v2\n"
    "v_and_b32 v12, 7, v0\n"
    "v_lshl_add_u32 v2, v12, 4, v2\n"
    "s_mov_b32 s30, 2359296\n"
    "s_waitcnt lgkmcnt(0)\n"
    "s_mov_b32 s28, s6\n"
    "s_and_b32 s29, s7, 0xffff\n"
    "buffer_load_dwordx4 v[16:19], v2, s[28:31], 0 offen offset:0 nt\n"
    "buffer_load_dwordx4 v[20:23], v2, s[28:31], 0 offen offset:128 nt\n"
    "buffer_load_dwordx4 v[24:27], v2, s[28:31], 0 offen offset:256 nt\n"
    "s_mov_b32 s16, s4\n"
    "s_and_b32 s17, s5, 0xffff\n"
    "s_load_dword s60, s[4:5], 229376\n"
    "s_lshr_b32 s39, s2, 1\n"
    "s_mul_i32 s39, s39, 18\n"
    "s_sub_u32 s51, s3, 2\n"
    "s_mul_i32 s51, s51, 6\n"
    "s_add_u32 s39, s39, s51\n"
    "s_lshl_b32 s39, s39, 10\n"
    "s_and_b32 s51, s2, 1\n"
    "s_lshl_b32 s51, s51, 7\n"
    "s_add_u32 s39, s39, s51\n"
    "s_add_u32 s39, s39, 0x40000\n"
    "v_and_b32 v5, 7, v0\n"
    "v_lshrrev_b32 v5, 2, v5\n"
    "v_lshl_add_u32 v5, v5, 10, s39\n"
    "v_and_b32 v12, 7, v0\n"
    "v_and_b32 v12, 3, v12\n"
    "v_lshl_add_u32 v5, v12, 8, v5\n"
    "v_lshrrev_b32 v12, 3, v0\n"
    "v_lshl_add_u32 v5, v12, 4, v5\n"
    "v_lshrrev_b32 v4, 3, v0\n"
    "v_lshlrev_b32 v4, 3, v4\n"
    "s_sub_u32 s39, s3, 2\n"
    "s_lshl_b32 s39, s39, 6\n"
    "v_add_u32 v3, s39, v4\n"
    "s_lshl_b32 s39, s2, 7\n"
    "s_add_u32 s39, s39, 0x2015000\n"
    "v_lshrrev_b32 v6, 3, v0\n"
    "v_lshl_add_u32 v6, v6, 4, s39\n"
    "v_and_b32 v12, 7, v0\n"
    "v_lshl_add_u32 v6, v12, 2, v6\n"
    "v_mov_b32 v12, 0x80000000\n"
    "s_sub_u32 s39, s3, 2\n"
    "s_lshl_b32 s39, s39, 3\n"
    "v_and_b32 v11, 7, v0\n"
    "v_add_u32 v11, s39, v11\n"
    "v_cmp_gt_u32 vcc, 4, v11\n"
    "s_nop 1\n"
    "v_cndmask_b32 v6, v12, v6, vcc\n"
    "s_mov_b32 s40, 0x47800000\n"
    "s_mov_b32 s41, 0x47800000\n"
    "s_mov_b32 s42, 0x3f803f80\n"
    "s_mov_b32 s45, 0x3f80\n"
    "s_mov_b32 s46, 0x3f800000\n"
    "s_mov_b32 s47, 4096\n"
    "v_mov_b64 v[52:53], 0\n"
    "s_waitcnt vmcnt(2)\n"
    "v_dot2c_f32_bf16 v52, s42, v16\n"
    "v_dot2c_f32_bf16 v53, v16, v16\n"
    "v_dot2c_f32_bf16 v52, s42, v17\n"
    "v_dot2c_f32_bf16 v53, v17, v17\n"
    "v_dot2c_f32_bf16 v52, s42, v18\n"
    "v_dot2c_f32_bf16 v53, v18, v18\n"
    "v_dot2c_f32_bf16 v52, s42, v19\n"
    "v_dot2c_f32_bf16 v53, v19, v19\n"
    "s_waitcnt vmcnt(1)\n"
    "v_dot2c_f32_bf16 v52, s42, v20\n"
    "v_dot2c_f32_bf16 v53, v20, v20\n"
    "v_dot2c_f32_bf16 v52, s42, v21\n"
    "v_dot2c_f32_bf16 v53, v21, v21\n"
    "v_dot2c_f32_bf16 v52, s42, v22\n"
    "v_dot2c_f32_bf16 v53, v22, v22\n"
    "v_dot2c_f32_bf16 v52, s42, v23\n"
    "v_dot2c_f32_bf16 v53, v23, v23\n"
    "s_waitcnt vmcnt(0)\n"
    "v_dot2c_f32_bf16 v52, s42, v24\n"
    "v_dot2c_f32_bf16 v53, v24, v24\n"
    "v_dot2c_f32_bf16 v52, s42, v25\n"
    "v_dot2c_f32_bf16 v53, v25, v25\n"
    "v_dot2c_f32_bf16 v52, s42, v26\n"
    "v_dot2c_f32_bf16 v53, v26, v26\n"
    "v_dot2c_f32_bf16 v52, s42, v27\n"
    "v_dot2c_f32_bf16 v53, v27, v27\n"
    "s_nop 1\n"
    "v_add_f32_dpp v52, v52, v52 quad_perm:[1,0,3,2] row_mask:0xf bank_mask:0xf\n"
    "v_add_f32_dpp v53, v53, v53 quad_perm:[1,0,3,2] row_mask:0xf bank_mask:0xf\n"
    "s_nop 0\n"
    "v_add_f32_dpp v52, v52, v52 quad_perm:[2,3,0,1] row_mask:0xf bank_mask:0xf\n"
    "v_add_f32_dpp v53, v53, v53 quad_perm:[2,3,0,1] row_mask:0xf bank_mask:0xf\n"
    "s_nop 0\n"
    "v_add_f32_dpp v52, v52, v52 row_half_mirror row_mask:0xf bank_mask:0xf\n"
    "v_add_f32_dpp v53, v53, v53 row_half_mirror row_mask:0xf bank_mask:0xf\n"
    "ds_write_b64 v3, v[52:53]\n"
    "s_waitcnt lgkmcnt(0)\n"
    "s_barrier\n"
    "ds_read_b64 v[52:53], v4 offset:0\n"
    "ds_read_b64 v[54:55], v4 offset:64\n"
    "ds_read_b64 v[56:57], v4 offset:128\n"
    "s_waitcnt lgkmcnt(0)\n"
    "v_pk_add_f32 v[52:53], v[52:53], v[54:55]\n"
    "v_pk_add_f32 v[52:53], v[52:53], v[56:57]\n"
    "v_mul_f32 v52, 0x3ae38e39, v52\n"
    "v_mul_f32 v53, 0x3ae38e39, v53\n"
    "v_cvt_pk_bf16_f32 v58, 0, -v52\n"
    "v_fma_f32 v53, -v52, v52, v53\n"
    "v_max_f32 v53, 0, v53\n"
    "v_cvt_pk_bf16_f32 v53, 0, v53\n"
    "v_add_f32 v53, 0x3727c5ac, v53\n"
    "v_cvt_pk_bf16_f32 v53, 0, v53\n"
    "v_sqrt_f32 v53, v53\n"
    "s_nop 0\n"
    "v_cvt_pk_bf16_f32 v53, 0, v53\n"
    "v_rcp_f32 v30, v53\n"
    "v_dot2_f32_bf16 v60, v16, s45, v58\n"
    "v_dot2_f32_bf16 v61, v16, s46, v58\n"
    "v_dot2_f32_bf16 v62, v17, s45, v58\n"
    "v_dot2_f32_bf16 v63, v17, s46, v58\n"
    "v_dot2_f32_bf16 v64, v18, s45, v58\n"
    "v_dot2_f32_bf16 v65, v18, s46, v58\n"
    "v_dot2_f32_bf16 v66, v19, s45, v58\n"
    "v_dot2_f32_bf16 v67, v19, s46, v58\n"
    "v_pk_fma_f32 v[68:69], v[60:61], s[40:41], v[60:61]\n"
    "v_pk_fma_f32 v[70:71], v[62:63], s[40:41], v[62:63]\n"
    "v_pk_fma_f32 v[72:73], v[64:65], s[40:41], v[64:65]\n"
    "v_pk_fma_f32 v[74:75], v[66:67], s[40:41], v[66:67]\n"
    "v_pk_fma_f32 v[60:61], v[60:61], s[40:41], v[68:69] neg_lo:[1,0,0] neg_hi:[1,0,0]\n"
    "v_pk_fma_f32 v[62:63], v[62:63], s[40:41], v[70:71] neg_lo:[1,0,0] neg_hi:[1,0,0]\n"
    "v_pk_fma_f32 v[64:65], v[64:65], s[40:41], v[72:73] neg_lo:[1,0,0] neg_hi:[1,0,0]\n"
    "v_pk_fma_f32 v[66:67], v[66:67], s[40:41], v[74:75] neg_lo:[1,0,0] neg_hi:[1,0,0]\n"
    "v_pk_mul_f32 v[60:61], v[60:61], v[30:31] op_sel_hi:[1,0]\n"
    "v_pk_mul_f32 v[62:63], v[62:63], v[30:31] op_sel_hi:[1,0]\n"
    "v_pk_mul_f32 v[64:65], v[64:65], v[30:31] op_sel_hi:[1,0]\n"
    "v_pk_mul_f32 v[66:67], v[66:67], v[30:31] op_sel_hi:[1,0]\n"
    "v_cvt_pk_bf16_f32 v16, v60, v62\n"
    "v_cvt_pk_bf16_f32 v17, v64, v66\n"
    "v_cvt_pk_bf16_f32 v18, v61, v63\n"
    "v_cvt_pk_bf16_f32 v19, v65, v67\n"
    "buffer_store_dwordx4 v[16:19], v5, s[16:19], 0 offen offset:0 sc1\n"
    "v_dot2_f32_bf16 v60, v20, s45, v58\n"
    "v_dot2_f32_bf16 v61, v20, s46, v58\n"
    "v_dot2_f32_bf16 v62, v21, s45, v58\n"
    "v_dot2_f32_bf16 v63, v21, s46, v58\n"
    "v_dot2_f32_bf16 v64, v22, s45, v58\n"
    "v_dot2_f32_bf16 v65, v22, s46, v58\n"
    "v_dot2_f32_bf16 v66, v23, s45, v58\n"
    "v_dot2_f32_bf16 v67, v23, s46, v58\n"
    "v_pk_fma_f32 v[68:69], v[60:61], s[40:41], v[60:61]\n"
    "v_pk_fma_f32 v[70:71], v[62:63], s[40:41], v[62:63]\n"
    "v_pk_fma_f32 v[72:73], v[64:65], s[40:41], v[64:65]\n"
    "v_pk_fma_f32 v[74:75], v[66:67], s[40:41], v[66:67]\n"
    "v_pk_fma_f32 v[60:61], v[60:61], s[40:41], v[68:69] neg_lo:[1,0,0] neg_hi:[1,0,0]\n"
    "v_pk_fma_f32 v[62:63], v[62:63], s[40:41], v[70:71] neg_lo:[1,0,0] neg_hi:[1,0,0]\n"
    "v_pk_fma_f32 v[64:65], v[64:65], s[40:41], v[72:73] neg_lo:[1,0,0] neg_hi:[1,0,0]\n"
    "v_pk_fma_f32 v[66:67], v[66:67], s[40:41], v[74:75] neg_lo:[1,0,0] neg_hi:[1,0,0]\n"
    "v_pk_mul_f32 v[60:61], v[60:61], v[30:31] op_sel_hi:[1,0]\n"
    "v_pk_mul_f32 v[62:63], v[62:63], v[30:31] op_sel_hi:[1,0]\n"
    "v_pk_mul_f32 v[64:65], v[64:65], v[30:31] op_sel_hi:[1,0]\n"
    "v_pk_mul_f32 v[66:67], v[66:67], v[30:31] op_sel_hi:[1,0]\n"
    "v_cvt_pk_bf16_f32 v20, v60, v62\n"
    "v_cvt_pk_bf16_f32 v21, v64, v66\n"
    "v_cvt_pk_bf16_f32 v22, v61, v63\n"
    "v_cvt_pk_bf16_f32 v23, v65, v67\n"
    "buffer_store_dwordx4 v[20:23], v5, s[16:19], 0 offen offset:2048 sc1\n"
    "v_dot2_f32_bf16 v60, v24, s45, v58\n"
    "v_dot2_f32_bf16 v61, v24, s46, v58\n"
    "v_dot2_f32_bf16 v62, v25, s45, v58\n"
    "v_dot2_f32_bf16 v63, v25, s46, v58\n"
    "v_dot2_f32_bf16 v64, v26, s45, v58\n"
    "v_dot2_f32_bf16 v65, v26, s46, v58\n"
    "v_dot2_f32_bf16 v66, v27, s45, v58\n"
    "v_dot2_f32_bf16 v67, v27, s46, v58\n"
    "v_pk_fma_f32 v[68:69], v[60:61], s[40:41], v[60:61]\n"
    "v_pk_fma_f32 v[70:71], v[62:63], s[40:41], v[62:63]\n"
    "v_pk_fma_f32 v[72:73], v[64:65], s[40:41], v[64:65]\n"
    "v_pk_fma_f32 v[74:75], v[66:67], s[40:41], v[66:67]\n"
    "v_pk_fma_f32 v[60:61], v[60:61], s[40:41], v[68:69] neg_lo:[1,0,0] neg_hi:[1,0,0]\n"
    "v_pk_fma_f32 v[62:63], v[62:63], s[40:41], v[70:71] neg_lo:[1,0,0] neg_hi:[1,0,0]\n"
    "v_pk_fma_f32 v[64:65], v[64:65], s[40:41], v[72:73] neg_lo:[1,0,0] neg_hi:[1,0,0]\n"
    "v_pk_fma_f32 v[66:67], v[66:67], s[40:41], v[74:75] neg_lo:[1,0,0] neg_hi:[1,0,0]\n"
    "v_pk_mul_f32 v[60:61], v[60:61], v[30:31] op_sel_hi:[1,0]\n"
    "v_pk_mul_f32 v[62:63], v[62:63], v[30:31] op_sel_hi:[1,0]\n"
    "v_pk_mul_f32 v[64:65], v[64:65], v[30:31] op_sel_hi:[1,0]\n"
    "v_pk_mul_f32 v[66:67], v[66:67], v[30:31] op_sel_hi:[1,0]\n"
    "v_cvt_pk_bf16_f32 v24, v60, v62\n"
    "v_cvt_pk_bf16_f32 v25, v64, v66\n"
    "v_cvt_pk_bf16_f32 v26, v61, v63\n"
    "v_cvt_pk_bf16_f32 v27, v65, v67\n"
    "buffer_store_dwordx4 v[24:27], v5, s[16:19], s47 offen offset:0 sc1\n"
    "buffer_store_dword v52, v6, s[16:19], 0 offen sc1\n"
    "s_branch kb_pj4_%=\n"
    "kb_pw2_%=:\n"
    "s_mov_b32 s16, s4\n"
    "s_and_b32 s17, s5, 0xffff\n"
    "s_load_dword s60, s[4:5], 229376\n"
    "s_lshl_b32 s45, s3, 13\n"
    "s_add_u32 s45, s45, 0x24000\n"
    "v_bfe_u32 v2, v0, 2, 2\n"
    "v_mul_u32_u24 v2, 0x6c0, v2\n"
    "v_add_u32 v2, s45, v2\n"
    "v_lshrrev_b32 v11, 4, v0\n"
    "v_mul_u32_u24 v11, 0x48, v11\n"
    "v_add_u32 v2, v2, v11\n"
    "v_and_b32 v3, 3, v0\n"
    "v_lshrrev_b32 v3, 1, v3\n"
    "v_and_b32 v11, 3, v0\n"
    "v_lshl_add_u32 v3, v11, 1, v3\n"
    "v_lshl_add_u32 v3, v3, 3, v2\n"
    "v_and_b32 v4, 3, v0\n"
    "v_lshl_add_u32 v4, v4, 3, v2\n"
    "v_mov_b32 v5, 0x280\n"
    "v_mov_b32 v11, 0x140\n"
    "v_and_b32 v12, 3, v0\n"
    "v_cmp_eq_u32 vcc, 2, v12\n"
    "s_nop 1\n"
    "v_cndmask_b32 v11, 32, v11, vcc\n"
    "v_and_b32 v12, 3, v0\n"
    "v_cmp_eq_u32 vcc, 1, v12\n"
    "s_nop 1\n"
    "v_cndmask_b32 v5, v11, v5, vcc\n"
    "v_add_u32 v5, v5, v2\n"
    "s_cmp_lt_u32 s43, 0x120\n"
    "s_cselect_b32 s46, 0, 0x80000000\n"
    "s_and_b32 s39, s43, 3\n"
    "s_mul_i32 s39, s39, 0x7e000\n"
    "s_lshr_b32 s51, s43, 4\n"
    "s_mul_i32 s51, s51, 0x1c00\n"
    "s_add_u32 s39, s39, s51\n"
    "s_bfe_u32 s51, s43, 0x20002\n"
    "s_lshl_b32 s51, s51, 8\n"
    "s_add_u32 s39, s39, s51\n"
    "s_add_u32 s39, s39, s46\n"
    "s_add_u32 s39, s39, 0x280000\n"
    "v_lshrrev_b32 v6, 4, v0\n"
    "v_mul_u32_u24 v6, 0x1f800, v6\n"
    "v_add_u32 v6, s39, v6\n"
    "v_and_b32 v12, 15, v0\n"
    "v_lshl_add_u32 v6, v12, 4, v6\n"
    "s_mov_b32 s47, 4096\n"
    "s_barrier\n"
    "s_waitcnt vmcnt(0) lgkmcnt(0)\n"
    "ds_read_b64_tr_b16 v[16:17], v3 offset:0\n"
    "ds_read_b64_tr_b16 v[18:19], v3 offset:864\n"
    "ds_read_b64_tr_b16 v[20:21], v3 offset:8\n"
    "ds_read_b64_tr_b16 v[22:23], v3 offset:872\n"
    "ds_read_b64_tr_b16 v[24:25], v3 offset:288\n"
    "ds_read_b64_tr_b16 v[26:27], v3 offset:1152\n"
    "ds_read_b64_tr_b16 v[28:29], v3 offset:296\n"
    "ds_read_b64_tr_b16 v[30:31], v3 offset:1160\n"
    "ds_read_b64_tr_b16 v[32:33], v4 offset:576\n"
    "ds_read_b64_tr_b16 v[34:35], v4 offset:1440\n"
    "ds_read_b64_tr_b16 v[36:37], v4 offset:608\n"
    "ds_read_b64_tr_b16 v[38:39], v4 offset:1472\n"
    "ds_read_b64_tr_b16 v[40:41], v5 offset:0\n"
    "ds_read_b64_tr_b16 v[42:43], v5 offset:864\n"
    "s_waitcnt lgkmcnt(12)\n"
    "buffer_store_dwordx4 v[16:19], v6, s[16:19], 0 offen offset:0 sc1\n"
    "s_waitcnt lgkmcnt(10)\n"
    "buffer_store_dwordx4 v[20:23], v6, s[16:19], 0 offen offset:1024 sc1\n"
    "s_waitcnt lgkmcnt(8)\n"
    "buffer_store_dwordx4 v[24:27], v6, s[16:19], 0 offen offset:2048 sc1\n"
    "s_waitcnt lgkmcnt(6)\n"
    "buffer_store_dwordx4 v[28:31], v6, s[16:19], 0 offen offset:3072 sc1\n"
    "s_waitcnt lgkmcnt(4)\n"
    "buffer_store_dwordx4 v[32:35], v6, s[16:19], s47 offen offset:0 sc1\n"
    "s_waitcnt lgkmcnt(2)\n"
    "buffer_store_dwordx4 v[36:39], v6, s[16:19], s47 offen offset:1024 sc1\n"
    "s_waitcnt lgkmcnt(0)\n"
    "buffer_store_dwordx4 v[40:43], v6, s[16:19], s47 offen offset:2048 sc1\n"
    "s_branch kb_pj4_%=\n"
    "kb_po3_%=:\n"
    "s_sub_u32 s39, s3, 5\n"
    "s_lshr_b32 s51, s2, 3\n"
    "s_mul_i32 s51, s51, 12\n"
    "s_lshl2_add_u32 s39, s39, s51\n"
    "v_and_b32 v2, 3, v0\n"
    "v_add_u32 v2, s39, v2\n"
    "v_lshlrev_b32 v2, 7, v2\n"
    "v_min_u32 v2, kb_code_end-k_main, v2\n"
    "s_getpc_b64 s[36:37]\n"
    "s_add_u32 s36, s36, k_main@rel32@lo+4\n"
    "s_addc_u32 s37, s37, k_main@rel32@hi+12\n"
    "global_load_dword v2, v2, s[36:37]\n"
    "s_waitcnt lgkmcnt(0)\n"
    "s_mov_b32 s16, s4\n"
    "s_and_b32 s17, s5, 0xffff\n"
    "s_load_dword s60, s[4:5], 229376\n"
    "s_mul_i32 s39, s2, 7\n"
    "v_add_u32 v3, s39, v0\n"
    "v_lshlrev_b32 v3, 7, v3\n"
    "v_mov_b32 v12, 0x80000000\n"
    "s_xor_b32 s39, s3, 5\n"
    "s_lshl_b32 s39, s39, 3\n"
    "v_add_u32 v11, s39, v0\n"
    "v_cmp_gt_u32 vcc, 7, v11\n"
    "s_nop 1\n"
    "v_cndmask_b32 v3, v12, v3, vcc\n"
    "buffer_load_dword v3, v3, s[16:19], 0 offen\n"
    "s_barrier\n"
    "s_sub_u32 s70, s2, 0xb8\n"
    "s_lshr_b32 s64, s70, 3\n"
    "s_mul_i32 s39, s2, 0x1c72\n"
    "s_lshr_b32 s39, s39, 16\n"
    "s_cmp_eq_u32 s3, 7\n"
    "s_cselect_b32 s64, s64, s39\n"
    "s_mul_i32 s65, s64, 9\n"
    "s_sub_u32 s65, s2, s65\n"
    "s_mov_b32 s66, 0x51000\n"
    "s_cmp_lt_u32 s64, 8\n"
    "s_cselect_b32 s66, 0xa200, s66\n"
    "s_mov_b32 s67, 0x28800\n"
    "s_cmp_lt_u32 s64, 8\n"
    "s_cselect_b32 s67, 0x5100, s67\n"
    "s_mov_b32 s68, 0x14400\n"
    "s_cmp_lt_u32 s64, 8\n"
    "s_cselect_b32 s68, 0x1200, s68\n"
    "s_mov_b32 s45, 0xa200\n"
    "s_cmp_lt_u32 s64, 8\n"
    "s_cselect_b32 s45, 0x2400, s45\n"
    "s_mov_b32 s46, 0x5100\n"
    "s_cmp_lt_u32 s64, 8\n"
    "s_cselect_b32 s46, 0x900, s46\n"
    "s_and_b32 s47, s70, 1\n"
    "s_lshr_b32 s39, s70, 1\n"
    "s_mul_i32 s39, s39, 18\n"
    "s_lshl2_add_u32 s47, s47, s39\n"
    "s_sub_u32 s39, s70, 64\n"
    "s_mul_i32 s39, s39, 0x48\n"
    "s_add_u32 s39, s39, 16\n"
    "s_cmp_lt_u32 s64, 8\n"
    "s_cselect_b32 s47, s47, s39\n"
    "s_mul_i32 s47, s47, 0x480\n"
    "s_mul_i32 s39, s64, 0x14400\n"
    "s_cmp_lt_u32 s64, 8\n"
    "s_cselect_b32 s39, s39, 0x4800\n"
    "s_lshl_b32 s51, s65, 7\n"
    "s_add_u32 s39, s39, s51\n"
    "s_cmp_eq_u32 s3, 7\n"
    "s_cselect_b32 s47, s47, s39\n"
    "v_lshrrev_b32 v100, 5, v0\n"
    "v_mul_u32_u24 v100, s45, v100\n"
    "v_add_u32 v100, s47, v100\n"
    "v_bfe_u32 v12, v0, 4, 1\n"
    "v_mul_u32_u24 v12, s46, v12\n"
    "v_add_u32 v100, v100, v12\n"
    "v_bfe_u32 v12, v0, 3, 1\n"
    "v_mul_u32_u24 v12, 0x480, v12\n"
    "v_add_u32 v100, v100, v12\n"
    "v_and_b32 v12, 7, v0\n"
    "v_lshl_add_u32 v100, v12, 4, v100\n"
    "s_mov_b32 s39, 0x24000\n"
    "v_lshrrev_b32 v101, 4, v0\n"
    "v_lshl_add_u32 v101, v101, 10, s39\n"
    "v_bfe_u32 v12, v0, 2, 2\n"
    "v_lshl_add_u32 v101, v12, 7, v101\n"
    "v_and_b32 v12, 3, v0\n"
    "v_lshl_add_u32 v101, v12, 3, v101\n"
    "s_mul_i32 s69, s64, 0x48\n"
    "s_lshl2_add_u32 s69, s65, s69\n"
    "s_lshl_b32 s69, s69, 10\n"
    "s_add_u32 s69, s69, 0x1f70000\n"
    "s_lshl_b32 s39, s70, 7\n"
    "s_add_u32 s39, s39, 0x2012000\n"
    "v_and_b32 v102, 7, v0\n"
    "v_lshl_add_u32 v102, v102, 4, s39\n"
    "v_lshrrev_b32 v12, 3, v0\n"
    "v_lshl_add_u32 v102, v12, 1, v102\n"
    "s_mov_b32 s28, s58\n"
    "s_and_b32 s29, s59, 0xffff\n"
    "s_mov_b32 s30, 663552\n"
    "kb_pj4_%=:\n"
    "s_and_b32 s76, s2, 1\n"
    "s_bfe_u32 s39, s2, 0x30003\n"
    "s_lshl3_add_u32 s76, s76, s39\n"
    "s_bfe_u32 s77, s2, 0x20001\n"
    "s_lshr_b32 s39, s2, 6\n"
    "s_lshl_b32 s39, s39, 2\n"
    "s_lshl4_add_u32 s77, s77, s39\n"
    "s_add_u32 s77, s77, s3\n"
    "s_mul_i32 s42, s77, 0x9000\n"
    "s_add_u32 s42, s42, 0x40000\n"
    "s_add_u32 s43, s42, 0x4800\n"
    "s_mul_i32 s44, s76, 0x1f800\n"
    "s_add_u32 s44, s44, 0x280000\n"
    "s_and_b32 s45, s3, 1\n"
    "s_mul_i32 s45, s45, 0x1c00\n"
    "s_add_u32 s24, s4, s44\n"
    "s_addc_u32 s25, s17, 0\n"
    "s_mov_b32 s26, 129024\n"
    "s_mov_b32 s27, 0x20000\n"
    "s_mov_b32 m0, s45\n"
    "s_cmp_lg_u32 s3, 7\n"
    "s_cbranch_scc1 kb_n110_%=\n"
    "s_lshr_b32 s39, s76, 2\n"
    "s_lshl_b32 s39, s39, 13\n"
    "s_add_u32 s39, s39, 0x28000\n"
    "v_lshl_add_u32 v4, v0, 7, s39\n"
    "s_sub_u32 s39, s77, s3\n"
    "s_lshl_b32 s39, s39, 2\n"
    "v_and_b32 v5, 15, v0\n"
    "v_add_u32 v5, s39, v5\n"
    "s_mov_b32 s39, 0x28000\n"
    "v_and_b32 v12, 7, v5\n"
    "v_bfe_u32 v11, v5, 3, 2\n"
    "v_lshl_add_u32 v12, v11, 6, v12\n"
    "v_lshrrev_b32 v11, 5, v5\n"
    "v_lshl_add_u32 v12, v11, 3, v12\n"
    "v_lshl_add_u32 v5, v12, 7, s39\n"
    "s_lshr_b32 s39, s2, 5\n"
    "s_bfe_u32 s51, s2, 0x20003\n"
    "s_lshl_b32 s51, s51, 6\n"
    "s_lshl3_add_u32 s39, s39, s51\n"
    "s_and_b32 s51, s2, 7\n"
    "s_add_u32 s39, s39, s51\n"
    "s_lshl_b32 s39, s39, 7\n"
    "s_add_u32 s39, s39, 0x28000\n"
    "v_mov_b32 v7, s39\n"
    "v_mov_b32 v12, 0x80000000\n"
    "v_cmp_eq_u32 vcc, 0, v0\n"
    "s_nop 1\n"
    "v_cndmask_b32 v7, v12, v7, vcc\n"
    "kb_n110_%=:\n"
    "s_waitcnt vmcnt(0)\n"
    "kb_n110_%=w:\n"
    "s_waitcnt lgkmcnt(0)\n"
    "s_add_u32 s60, s60, 1\n"
    "s_barrier\n"
    "s_cmp_eq_u32 s3, 7\n"
    "s_cbranch_scc1 kb_n110_%=p\n"
    "v_bfe_u32 v2, v0, 4, 1\n"
    "v_lshlrev_b32 v2, 9, v2\n"
    "v_and_b32 v12, 15, v0\n"
    "v_lshl_add_u32 v2, v12, 4, v2\n"
    "s_mul_i32 s39, s76, 0x48000\n"
    "s_mul_i32 s51, s77, 0x900\n"
    "s_add_u32 s39, s39, s51\n"
    "s_add_u32 s39, s39, 0x700000\n"
    "v_lshrrev_b32 v3, 5, v0\n"
    "v_mul_u32_u24 v3, 0x24000, v3\n"
    "v_add_u32 v3, s39, v3\n"
    "v_add_u32 v3, v3, v2\n"
    "s_mul_i32 s39, s76, 0x24000\n"
    "s_mul_i32 s51, s77, 0x480\n"
    "s_add_u32 s39, s39, s51\n"
    "s_add_u32 s39, s39, 0x478000\n"
    "v_lshrrev_b32 v12, 5, v0\n"
    "v_mul_u32_u24 v12, 0x12000, v12\n"
    "v_add_u32 v12, s39, v12\n"
    "v_add_u32 v2, v12, v2\n"
    "s_mul_i32 s39, s76, 0x24000\n"
    "s_mul_i32 s51, s77, 0x480\n"
    "s_add_u32 s39, s39, s51\n"
    "s_add_u32 s39, s39, 0x478400\n"
    "v_mov_b32 v4, s39\n"
    "s_mul_i32 s39, s76, 0x48000\n"
    "s_mul_i32 s51, s77, 0x900\n"
    "s_add_u32 s39, s39, s51\n"
    "s_add_u32 s39, s39, 0x700880\n"
    "v_mov_b32 v12, s39\n"
    "v_cmp_gt_u32 vcc, 32, v0\n"
    "s_nop 1\n"
    "v_cndmask_b32 v4, v12, v4, vcc\n"
    "v_and_b32 v12, 31, v0\n"
    "v_lshl_add_u32 v4, v12, 2, v4\n"
    "v_mov_b32 v5, 0x12000\n"
    "v_mov_b32 v12, 0x24000\n"
    "v_cmp_gt_u32 vcc, 32, v0\n"
    "s_nop 1\n"
    "v_cndmask_b32 v5, v12, v5, vcc\n"
    "v_add_u32 v5, v5, v4\n"
    "s_mul_i32 s46, s76, 0x48000\n"
    "s_mul_i32 s39, s77, 0x900\n"
    "s_add_u32 s46, s46, s39\n"
    "s_add_u32 s46, s46, 0x700400\n"
    "v_lshrrev_b32 v9, 5, v0\n"
    "v_bfe_u32 v12, v0, 4, 1\n"
    "v_lshl_add_u32 v9, v12, 1, v9\n"
    "v_and_b32 v8, 15, v0\n"
    "v_add_u32 v8, 16, v8\n"
    "v_and_b32 v12, 15, v0\n"
    "v_subrev_u32 v12, 2, v12\n"
    "v_and_b32 v11, 15, v0\n"
    "v_cmp_gt_u32 vcc, 2, v11\n"
    "s_nop 1\n"
    "v_cndmask_b32 v8, v12, v8, vcc\n"
    "v_mov_b32 v7, 0\n"
    "v_mov_b32 v11, 0x24000\n"
    "v_and_b32 v12, 15, v0\n"
    "v_cmp_gt_u32 vcc, 2, v12\n"
    "s_nop 1\n"
    "v_cndmask_b32 v7, v11, v7, vcc\n"
    "v_add_u32 v7, s46, v7\n"
    "v_lshl_add_u32 v7, v8, 6, v7\n"
    "v_bfe_u32 v12, v8, 2, 2\n"
    "v_xor_b32 v12, v9, v12\n"
    "v_lshl_add_u32 v7, v12, 4, v7\n"
    "v_and_b32 v6, 15, v0\n"
    "v_add_u32 v6, 10, v6\n"
    "s_add_u32 s39, s46, 0x24000\n"
    "v_lshl_add_u32 v8, v6, 6, s39\n"
    "v_bfe_u32 v12, v6, 2, 2\n"
    "v_xor_b32 v12, v9, v12\n"
    "v_lshl_add_u32 v8, v12, 4, v8\n"
    "v_mov_b32 v12, 0x80000000\n"
    "v_and_b32 v11, 15, v0\n"
    "v_lshrrev_b32 v11, 2, v11\n"
    "v_cmp_eq_u32 vcc, 1, v11\n"
    "s_nop 1\n"
    "v_cndmask_b32 v8, v12, v8, vcc\n"
    "v_and_b32 v6, 15, v0\n"
    "v_lshl_add_u32 v6, v6, 6, s46\n"
    "v_and_b32 v11, 15, v0\n"
    "v_bfe_u32 v11, v11, 2, 2\n"
    "v_xor_b32 v11, v9, v11\n"
    "v_lshl_add_u32 v6, v11, 4, v6\n"
    "v_mov_b32 v9, 0x3eae1a93\n"
    "v_mov_b32 v11, 0x3f800000\n"
    "v_cmp_gt_u32 vcc, 32, v0\n"
    "s_nop 1\n"
    "v_cndmask_b32 v9, v11, v9, vcc\n"
    "s_branch kb_n08_%=\n"
    "kb_n110_%=p:\n"
    "v_mov_b32 v8, s60\n"
    "s_mov_b32 s48, 0\n"
    "buffer_store_dword v8, v7, s[16:19], 0 offen sc1\n"
    "s_sleep 16\n"
    "kb_s09_%=:\n"
    "buffer_load_dword v8, v4, s[16:19], 0 offen sc1\n"
    "buffer_load_dword v9, v5, s[16:19], 0 offen sc1\n"
    "s_add_u32 s48, s48, 1\n"
    "s_waitcnt vmcnt(0)\n"
    "v_cmp_ne_u32 vcc, s60, v8\n"
    "v_cmp_ne_u32 s[46:47], s60, v9\n"
    "s_or_b64 vcc, vcc, s[46:47]\n"
    "s_cbranch_vccz kb_n08_%=\n"
    "s_cmp_lt_u32 s48, 131072\n"
    "s_cbranch_scc1 kb_s09_%=\n"
    "kb_n08_%=:\n"
    "s_barrier\n"
    "s_cmp_lt_u32 s3, 4\n"
    "s_cbranch_scc1 kb_qh5_%=\n"
    "s_cmp_ge_u32 s3, 6\n"
    "s_cbranch_scc1 kb_qa6_%=\n"
    "s_setprio 3\n"
    "s_mov_b32 s45, 0\n"
    "s_branch kb_qn12_%=\n"
    "kb_ql11_%=:\n"
    "s_waitcnt vmcnt(7)\n"
    "s_barrier\n"
    "kb_qn12_%=:\n"
    "buffer_load_dwordx4 v1, s[24:27], m0 offen offset:0 sc1 lds\n"
    "buffer_load_dwordx4 v1, s[24:27], m0 offen offset:1024 sc1 lds\n"
    "buffer_load_dwordx4 v1, s[24:27], m0 offen offset:2048 sc1 lds\n"
    "buffer_load_dwordx4 v1, s[24:27], m0 offen offset:3072 sc1 lds\n"
    "s_add_u32 m0, m0, 4096\n"
    "s_nop 0\n"
    "buffer_load_dwordx4 v1, s[24:27], m0 offen offset:0 sc1 lds\n"
    "buffer_load_dwordx4 v1, s[24:27], m0 offen offset:1024 sc1 lds\n"
    "buffer_load_dwordx4 v1, s[24:27], m0 offen offset:2048 sc1 lds\n"
    "s_add_u32 m0, m0, 10240\n"
    "s_add_u32 s45, s45, 1\n"
    "s_cmp_lt_u32 s45, 2\n"
    "s_cbranch_scc1 kb_qn12_%=\n"
    "s_cmp_lt_u32 s45, 9\n"
    "s_cbranch_scc1 kb_ql11_%=\n"
    "s_waitcnt vmcnt(7)\n"
    "s_barrier\n"
    "s_waitcnt vmcnt(0)\n"
    "s_barrier\n"
    "s_setprio 0\n"
    "s_branch kb_qj7_%=\n"
    "kb_qa6_%=:\n"
    "s_waitcnt vmcnt(0)\n"
    "s_barrier\n"
    "s_barrier\n"
    "s_barrier\n"
    "s_barrier\n"
    "s_barrier\n"
    "s_cmp_eq_u32 s3, 7\n"
    "s_cbranch_scc1 kb_qb13_%=\n"
    "s_cmp_ge_u32 s2, 81\n"
    "s_cbranch_scc1 kb_n614_%=\n"
    "s_mov_b32 m0, 147456\n"
    "s_nop 0\n"
    "buffer_load_dwordx4 v100, s[28:31], 0 offen nt lds\n"
    "s_mov_b32 m0, 148480\n"
    "s_nop 0\n"
    "buffer_load_dwordx4 v100, s[28:31], s68 offen nt lds\n"
    "s_mov_b32 m0, 149504\n"
    "s_nop 0\n"
    "buffer_load_dwordx4 v100, s[28:31], s67 offen nt lds\n"
    "s_mov_b32 m0, 150528\n"
    "s_add_u32 s45, s67, s68\n"
    "buffer_load_dwordx4 v100, s[28:31], s45 offen nt lds\n"
    "s_mov_b32 m0, 151552\n"
    "s_nop 0\n"
    "buffer_load_dwordx4 v100, s[28:31], s66 offen nt lds\n"
    "s_mov_b32 m0, 152576\n"
    "s_add_u32 s45, s66, s68\n"
    "buffer_load_dwordx4 v100, s[28:31], s45 offen nt lds\n"
    "s_mov_b32 m0, 153600\n"
    "s_add_u32 s45, s66, s67\n"
    "buffer_load_dwordx4 v100, s[28:31], s45 offen nt lds\n"
    "s_mov_b32 m0, 154624\n"
    "s_add_u32 s45, s66, s67\n"
    "s_add_u32 s45, s45, s68\n"
    "buffer_load_dwordx4 v100, s[28:31], s45 offen nt lds\n"
    "kb_n614_%=:\n"
    "s_barrier\n"
    "s_barrier\n"
    "s_barrier\n"
    "s_barrier\n"
    "s_cmp_ge_u32 s2, 81\n"
    "s_cbranch_scc1 kb_qj7_%=\n"
    "s_add_u32 s41, s69, 36864\n"
    "s_waitcnt vmcnt(0)\n"
    "ds_read_b64_tr_b16 v[16:17], v101 offset:0\n"
    "ds_read_b64_tr_b16 v[18:19], v101 offset:512\n"
    "ds_read_b64_tr_b16 v[20:21], v101 offset:32\n"
    "ds_read_b64_tr_b16 v[22:23], v101 offset:544\n"
    "ds_read_b64_tr_b16 v[24:25], v101 offset:64\n"
    "ds_read_b64_tr_b16 v[26:27], v101 offset:576\n"
    "ds_read_b64_tr_b16 v[28:29], v101 offset:96\n"
    "ds_read_b64_tr_b16 v[30:31], v101 offset:608\n"
    "ds_read_b64_tr_b16 v[32:33], v101 offset:4096\n"
    "ds_read_b64_tr_b16 v[34:35], v101 offset:4608\n"
    "ds_read_b64_tr_b16 v[36:37], v101 offset:4128\n"
    "ds_read_b64_tr_b16 v[38:39], v101 offset:4640\n"
    "ds_read_b64_tr_b16 v[40:41], v101 offset:4160\n"
    "ds_read_b64_tr_b16 v[42:43], v101 offset:4672\n"
    "ds_read_b64_tr_b16 v[44:45], v101 offset:4192\n"
    "ds_read_b64_tr_b16 v[46:47], v101 offset:4704\n"
    "s_waitcnt lgkmcnt(0)\n"
    "buffer_store_dwordx4 v[16:19], v1, s[16:19], s69 offen offset:0 sc1\n"
    "buffer_store_dwordx4 v[20:23], v1, s[16:19], s69 offen offset:1024 sc1\n"
    "buffer_store_dwordx4 v[24:27], v1, s[16:19], s69 offen offset:2048 sc1\n"
    "buffer_store_dwordx4 v[28:31], v1, s[16:19], s69 offen offset:3072 sc1\n"
    "buffer_store_dwordx4 v[32:35], v1, s[16:19], s41 offen offset:0 sc1\n"
    "buffer_store_dwordx4 v[36:39], v1, s[16:19], s41 offen offset:1024 sc1\n"
    "buffer_store_dwordx4 v[40:43], v1, s[16:19], s41 offen offset:2048 sc1\n"
    "buffer_store_dwordx4 v[44:47], v1, s[16:19], s41 offen offset:3072 sc1\n"
    "s_branch kb_qj7_%=\n"
    "kb_qb13_%=:\n"
    "s_cmp_lt_u32 s2, 184\n"
    "s_cbranch_scc1 kb_n715_%=\n"
    "buffer_load_dwordx4 v[52:55], v100, s[28:31], 0 offen offset:0 nt\n"
    "buffer_load_dwordx4 v[56:59], v100, s[28:31], 0 offen offset:128 nt\n"
    "buffer_load_dwordx4 v[60:63], v100, s[28:31], 0 offen offset:256 nt\n"
    "buffer_load_dwordx4 v[64:67], v100, s[28:31], 0 offen offset:384 nt\n"
    "buffer_load_dwordx4 v[68:71], v100, s[28:31], 0 offen offset:512 nt\n"
    "buffer_load_dwordx4 v[72:75], v100, s[28:31], 0 offen offset:640 nt\n"
    "buffer_load_dwordx4 v[76:79], v100, s[28:31], 0 offen offset:768 nt\n"
    "buffer_load_dwordx4 v[80:83], v100, s[28:31], 0 offen offset:896 nt\n"
    "buffer_load_dwordx4 v[84:87], v100, s[28:31], 0 offen offset:1024 nt\n"
    "kb_n715_%=:\n"
    "s_barrier\n"
    "s_barrier\n"
    "s_barrier\n"
    "s_barrier\n"
    "s_cmp_lt_u32 s2, 184\n"
    "s_cbranch_scc1 kb_qj7_%=\n"
    "v_mov_b32 v44, 0\n"
    "s_mov_b32 s45, 0x3f803f80\n"
    "s_waitcnt vmcnt(0)\n"
    "v_dot2c_f32_bf16 v44, s45, v52\n"
    "v_dot2c_f32_bf16 v44, s45, v53\n"
    "v_dot2c_f32_bf16 v44, s45, v54\n"
    "v_dot2c_f32_bf16 v44, s45, v55\n"
    "v_dot2c_f32_bf16 v44, s45, v56\n"
    "v_dot2c_f32_bf16 v44, s45, v57\n"
    "v_dot2c_f32_bf16 v44, s45, v58\n"
    "v_dot2c_f32_bf16 v44, s45, v59\n"
    "v_dot2c_f32_bf16 v44, s45, v60\n"
    "v_dot2c_f32_bf16 v44, s45, v61\n"
    "v_dot2c_f32_bf16 v44, s45, v62\n"
    "v_dot2c_f32_bf16 v44, s45, v63\n"
    "v_dot2c_f32_bf16 v44, s45, v64\n"
    "v_dot2c_f32_bf16 v44, s45, v65\n"
    "v_dot2c_f32_bf16 v44, s45, v66\n"
    "v_dot2c_f32_bf16 v44, s45, v67\n"
    "v_dot2c_f32_bf16 v44, s45, v68\n"
    "v_dot2c_f32_bf16 v44, s45, v69\n"
    "v_dot2c_f32_bf16 v44, s45, v70\n"
    "v_dot2c_f32_bf16 v44, s45, v71\n"
    "v_dot2c_f32_bf16 v44, s45, v72\n"
    "v_dot2c_f32_bf16 v44, s45, v73\n"
    "v_dot2c_f32_bf16 v44, s45, v74\n"
    "v_dot2c_f32_bf16 v44, s45, v75\n"
    "v_dot2c_f32_bf16 v44, s45, v76\n"
    "v_dot2c_f32_bf16 v44, s45, v77\n"
    "v_dot2c_f32_bf16 v44, s45, v78\n"
    "v_dot2c_f32_bf16 v44, s45, v79\n"
    "v_dot2c_f32_bf16 v44, s45, v80\n"
    "v_dot2c_f32_bf16 v44, s45, v81\n"
    "v_dot2c_f32_bf16 v44, s45, v82\n"
    "v_dot2c_f32_bf16 v44, s45, v83\n"
    "v_dot2c_f32_bf16 v44, s45, v84\n"
    "v_dot2c_f32_bf16 v44, s45, v85\n"
    "v_dot2c_f32_bf16 v44, s45, v86\n"
    "v_dot2c_f32_bf16 v44, s45, v87\n"
    "s_nop 2\n"
    "v_add_f32_dpp v44, v44, v44 quad_perm:[1,0,3,2] row_mask:0xf bank_mask:0xf\n"
    "s_nop 1\n"
    "v_add_f32_dpp v44, v44, v44 quad_perm:[2,3,0,1] row_mask:0xf bank_mask:0xf\n"
    "s_nop 1\n"
    "v_add_f32_dpp v44, v44, v44 row_half_mirror row_mask:0xf bank_mask:0xf\n"
    "v_mul_f32 v44, 0x3ae38e39, v44\n"
    "v_cvt_pk_bf16_f32 v45, 0, v44\n"
    "v_sub_f32 v44, v44, v45\n"
    "v_cvt_pk_bf16_f32 v44, v45, v44\n"
    "v_and_b32 v46, 7, v0\n"
    "v_lshlrev_b32 v45, 4, v46\n"
    "v_lshrrev_b32 v44, v45, v44\n"
    "v_cmp_gt_u32 vcc, 2, v46\n"
    "s_nop 1\n"
    "v_cndmask_b32 v44, 0, v44, vcc\n"
    "buffer_store_short v44, v102, s[16:19], 0 offen sc1\n"
    "s_branch kb_qj7_%=\n"
    "kb_qh5_%=:\n"
    "buffer_load_dwordx4 v[16:19], v1, s[16:19], s42 offen offset:0 sc1\n"
    "buffer_load_dwordx4 v[20:23], v1, s[16:19], s43 offen offset:0 sc1\n"
    "buffer_load_dwordx4 v[24:27], v1, s[16:19], s42 offen offset:1024 sc1\n"
    "buffer_load_dwordx4 v[28:31], v1, s[16:19], s43 offen offset:1024 sc1\n"
    "buffer_load_dwordx4 v[32:35], v1, s[16:19], s42 offen offset:2048 sc1\n"
    "buffer_load_dwordx4 v[36:39], v1, s[16:19], s43 offen offset:2048 sc1\n"
    "buffer_load_dwordx4 v[40:43], v1, s[16:19], s42 offen offset:3072 sc1\n"
    "buffer_load_dwordx4 v[44:47], v1, s[16:19], s43 offen offset:3072 sc1\n"
    "v_mov_b32 v13, v1\n"
    "s_barrier\n"
    "ds_read_b128 v[224:227], v13 offset:0\n"
    "ds_read_b128 v[228:231], v13 offset:7168\n"
    "ds_read_b128 v[232:235], v13 offset:1024\n"
    "ds_read_b128 v[236:239], v13 offset:8192\n"
    "ds_read_b128 v[240:243], v13 offset:2048\n"
    "ds_read_b128 v[244:247], v13 offset:9216\n"
    "s_waitcnt vmcnt(4) lgkmcnt(4)\n"
    "v_mfma_f32_16x16x32_bf16 v[160:163], v[224:227], v[16:19], 0\n"
    "ds_read_b128 v[248:251], v13 offset:3072\n"
    "v_mfma_f32_16x16x32_bf16 v[160:163], v[228:231], v[24:27], v[160:163]\n"
    "ds_read_b128 v[252:255], v13 offset:10240\n"
    "v_mfma_f32_16x16x32_bf16 v[164:167], v[224:227], v[20:23], 0\n"
    "s_add_u32 s42, s42, 4096\n"
    "s_add_u32 s43, s43, 4096\n"
    "buffer_load_dwordx4 v[48:51], v1, s[16:19], s42 offen offset:0 sc1\n"
    "v_mfma_f32_16x16x32_bf16 v[164:167], v[228:231], v[28:31], v[164:167]\n"
    "s_waitcnt lgkmcnt(4)\n"
    "v_mfma_f32_16x16x32_bf16 v[168:171], v[232:235], v[16:19], 0\n"
    "ds_read_b128 v[224:227], v13 offset:4096\n"
    "v_mfma_f32_16x16x32_bf16 v[168:171], v[236:239], v[24:27], v[168:171]\n"
    "ds_read_b128 v[228:231], v13 offset:11264\n"
    "v_mfma_f32_16x16x32_bf16 v[172:175], v[232:235], v[20:23], 0\n"
    "buffer_load_dwordx4 v[52:55], v1, s[16:19], s43 offen offset:0 sc1\n"
    "v_mfma_f32_16x16x32_bf16 v[172:175], v[236:239], v[28:31], v[172:175]\n"
    "s_waitcnt lgkmcnt(4)\n"
    "v_mfma_f32_16x16x32_bf16 v[176:179], v[240:243], v[16:19], 0\n"
    "ds_read_b128 v[232:235], v13 offset:5120\n"
    "v_mfma_f32_16x16x32_bf16 v[176:179], v[244:247], v[24:27], v[176:179]\n"
    "ds_read_b128 v[236:239], v13 offset:12288\n"
    "v_mfma_f32_16x16x32_bf16 v[180:183], v[240:243], v[20:23], 0\n"
    "buffer_load_dwordx4 v[56:59], v1, s[16:19], s42 offen offset:1024 sc1\n"
    "v_mfma_f32_16x16x32_bf16 v[180:183], v[244:247], v[28:31], v[180:183]\n"
    "s_waitcnt lgkmcnt(4)\n"
    "v_mfma_f32_16x16x32_bf16 v[184:187], v[248:251], v[16:19], 0\n"
    "ds_read_b128 v[240:243], v13 offset:6144\n"
    "v_mfma_f32_16x16x32_bf16 v[184:187], v[252:255], v[24:27], v[184:187]\n"
    "ds_read_b128 v[244:247], v13 offset:13312\n"
    "v_mfma_f32_16x16x32_bf16 v[188:191], v[248:251], v[20:23], 0\n"
    "buffer_load_dwordx4 v[60:63], v1, s[16:19], s43 offen offset:1024 sc1\n"
    "v_mfma_f32_16x16x32_bf16 v[188:191], v[252:255], v[28:31], v[188:191]\n"
    "s_waitcnt lgkmcnt(4)\n"
    "v_mfma_f32_16x16x32_bf16 v[192:195], v[16:19], v[224:227], 0\n"
    "s_barrier\n"
    "ds_read_b128 v[248:251], v13 offset:14336\n"
    "v_mfma_f32_16x16x32_bf16 v[192:195], v[24:27], v[228:231], v[192:195]\n"
    "ds_read_b128 v[252:255], v13 offset:21504\n"
    "v_mfma_f32_16x16x32_bf16 v[196:199], v[20:23], v[224:227], 0\n"
    "v_mfma_f32_16x16x32_bf16 v[196:199], v[28:31], v[228:231], v[196:199]\n"
    "s_waitcnt lgkmcnt(4)\n"
    "v_mfma_f32_16x16x32_bf16 v[200:203], v[16:19], v[232:235], 0\n"
    "ds_read_b128 v[224:227], v13 offset:15360\n"
    "v_mfma_f32_16x16x32_bf16 v[200:203], v[24:27], v[236:239], v[200:203]\n"
    "ds_read_b128 v[228:231], v13 offset:22528\n"
    "v_mfma_f32_16x16x32_bf16 v[204:207], v[20:23], v[232:235], 0\n"
    "v_mfma_f32_16x16x32_bf16 v[204:207], v[28:31], v[236:239], v[204:207]\n"
    "s_waitcnt lgkmcnt(4)\n"
    "v_mfma_f32_16x16x32_bf16 v[208:211], v[240:243], v[16:19], 0\n"
    "ds_read_b128 v[232:235], v13 offset:16384\n"
    "v_mfma_f32_16x16x32_bf16 v[208:211], v[244:247], v[24:27], v[208:211]\n"
    "ds_read_b128 v[236:239], v13 offset:23552\n"
    "v_mfma_f32_16x16x32_bf16 v[212:215], v[240:243], v[20:23], 0\n"
    "v_mfma_f32_16x16x32_bf16 v[212:215], v[244:247], v[28:31], v[212:215]\n"
    "s_waitcnt vmcnt(4) lgkmcnt(4)\n"
    "v_mfma_f32_16x16x32_bf16 v[160:163], v[248:251], v[32:35], v[160:163]\n"
    "ds_read_b128 v[240:243], v13 offset:17408\n"
    "v_mfma_f32_16x16x32_bf16 v[160:163], v[252:255], v[40:43], v[160:163]\n"
    "ds_read_b128 v[244:247], v13 offset:24576\n"
    "v_mfma_f32_16x16x32_bf16 v[164:167], v[248:251], v[36:39], v[164:167]\n"
    "buffer_load_dwordx4 v[64:67], v1, s[16:19], s42 offen offset:2048 sc1\n"
    "v_mfma_f32_16x16x32_bf16 v[164:167], v[252:255], v[44:47], v[164:167]\n"
    "s_waitcnt lgkmcnt(4)\n"
    "v_mfma_f32_16x16x32_bf16 v[168:171], v[224:227], v[32:35], v[168:171]\n"
    "ds_read_b128 v[248:251], v13 offset:18432\n"
    "v_mfma_f32_16x16x32_bf16 v[168:171], v[228:231], v[40:43], v[168:171]\n"
    "ds_read_b128 v[252:255], v13 offset:25600\n"
    "v_mfma_f32_16x16x32_bf16 v[172:175], v[224:227], v[36:39], v[172:175]\n"
    "buffer_load_dwordx4 v[68:71], v1, s[16:19], s43 offen offset:2048 sc1\n"
    "v_mfma_f32_16x16x32_bf16 v[172:175], v[228:231], v[44:47], v[172:175]\n"
    "s_waitcnt lgkmcnt(4)\n"
    "v_mfma_f32_16x16x32_bf16 v[176:179], v[232:235], v[32:35], v[176:179]\n"
    "ds_read_b128 v[224:227], v13 offset:19456\n"
    "v_mfma_f32_16x16x32_bf16 v[176:179], v[236:239], v[40:43], v[176:179]\n"
    "ds_read_b128 v[228:231], v13 offset:26624\n"
    "v_mfma_f32_16x16x32_bf16 v[180:183], v[232:235], v[36:39], v[180:183]\n"
    "buffer_load_dwordx4 v[72:75], v1, s[16:19], s42 offen offset:3072 sc1\n"
    "v_mfma_f32_16x16x32_bf16 v[180:183], v[236:239], v[44:47], v[180:183]\n"
    "s_waitcnt lgkmcnt(4)\n"
    "v_mfma_f32_16x16x32_bf16 v[184:187], v[240:243], v[32:35], v[184:187]\n"
    "ds_read_b128 v[232:235], v13 offset:20480\n"
    "v_mfma_f32_16x16x32_bf16 v[184:187], v[244:247], v[40:43], v[184:187]\n"
    "ds_read_b128 v[236:239], v13 offset:27648\n"
    "v_mfma_f32_16x16x32_bf16 v[188:191], v[240:243], v[36:39], v[188:191]\n"
    "buffer_load_dwordx4 v[76:79], v1, s[16:19], s43 offen offset:3072 sc1\n"
    "v_mfma_f32_16x16x32_bf16 v[188:191], v[244:247], v[44:47], v[188:191]\n"
    "s_waitcnt lgkmcnt(4)\n"
    "v_mfma_f32_16x16x32_bf16 v[192:195], v[32:35], v[248:251], v[192:195]\n"
    "s_barrier\n"
    "ds_read_b128 v[240:243], v13 offset:28672\n"
    "v_mfma_f32_16x16x32_bf16 v[192:195], v[40:43], v[252:255], v[192:195]\n"
    "ds_read_b128 v[244:247], v13 offset:35840\n"
    "v_mfma_f32_16x16x32_bf16 v[196:199], v[36:39], v[248:251], v[196:199]\n"
    "v_mfma_f32_16x16x32_bf16 v[196:199], v[44:47], v[252:255], v[196:199]\n"
    "s_waitcnt lgkmcnt(4)\n"
    "v_mfma_f32_16x16x32_bf16 v[200:203], v[32:35], v[224:227], v[200:203]\n"
    "ds_read_b128 v[248:251], v13 offset:29696\n"
    "v_mfma_f32_16x16x32_bf16 v[200:203], v[40:43], v[228:231], v[200:203]\n"
    "ds_read_b128 v[252:255], v13 offset:36864\n"
    "v_mfma_f32_16x16x32_bf16 v[204:207], v[36:39], v[224:227], v[204:207]\n"
    "v_mfma_f32_16x16x32_bf16 v[204:207], v[44:47], v[228:231], v[204:207]\n"
    "s_waitcnt lgkmcnt(4)\n"
    "v_mfma_f32_16x16x32_bf16 v[208:211], v[232:235], v[32:35], v[208:211]\n"
    "ds_read_b128 v[224:227], v13 offset:30720\n"
    "v_mfma_f32_16x16x32_bf16 v[208:211], v[236:239], v[40:43], v[208:211]\n"
    "ds_read_b128 v[228:231], v13 offset:37888\n"
    "v_mfma_f32_16x16x32_bf16 v[212:215], v[232:235], v[36:39], v[212:215]\n"
    "v_mfma_f32_16x16x32_bf16 v[212:215], v[236:239], v[44:47], v[212:215]\n"
    "s_waitcnt vmcnt(4) lgkmcnt(4)\n"
    "v_mfma_f32_16x16x32_bf16 v[160:163], v[240:243], v[48:51], v[160:163]\n"
    "ds_read_b128 v[232:235], v13 offset:31744\n"
    "v_mfma_f32_16x16x32_bf16 v[160:163], v[244:247], v[56:59], v[160:163]\n"
    "ds_read_b128 v[236:239], v13 offset:38912\n"
    "v_mfma_f32_16x16x32_bf16 v[164:167], v[240:243], v[52:55], v[164:167]\n"
    "s_add_u32 s42, s42, 4096\n"
    "s_add_u32 s43, s43, 4096\n"
    "buffer_load_dwordx4 v[80:83], v1, s[16:19], s42 offen offset:0 sc1\n"
    "v_mfma_f32_16x16x32_bf16 v[164:167], v[244:247], v[60:63], v[164:167]\n"
    "s_waitcnt lgkmcnt(4)\n"
    "v_mfma_f32_16x16x32_bf16 v[168:171], v[248:251], v[48:51], v[168:171]\n"
    "ds_read_b128 v[240:243], v13 offset:32768\n"
    "v_mfma_f32_16x16x32_bf16 v[168:171], v[252:255], v[56:59], v[168:171]\n"
    "ds_read_b128 v[244:247], v13 offset:39936\n"
    "v_mfma_f32_16x16x32_bf16 v[172:175], v[248:251], v[52:55], v[172:175]\n"
    "buffer_load_dwordx4 v[84:87], v1, s[16:19], s43 offen offset:0 sc1\n"
    "v_mfma_f32_16x16x32_bf16 v[172:175], v[252:255], v[60:63], v[172:175]\n"
    "s_waitcnt lgkmcnt(4)\n"
    "v_mfma_f32_16x16x32_bf16 v[176:179], v[224:227], v[48:51], v[176:179]\n"
    "ds_read_b128 v[248:251], v13 offset:33792\n"
    "v_mfma_f32_16x16x32_bf16 v[176:179], v[228:231], v[56:59], v[176:179]\n"
    "ds_read_b128 v[252:255], v13 offset:40960\n"
    "v_mfma_f32_16x16x32_bf16 v[180:183], v[224:227], v[52:55], v[180:183]\n"
    "buffer_load_dwordx4 v[88:91], v1, s[16:19], s42 offen offset:1024 sc1\n"
    "v_mfma_f32_16x16x32_bf16 v[180:183], v[228:231], v[60:63], v[180:183]\n"
    "s_waitcnt lgkmcnt(4)\n"
    "v_mfma_f32_16x16x32_bf16 v[184:187], v[232:235], v[48:51], v[184:187]\n"
    "ds_read_b128 v[224:227], v13 offset:34816\n"
    "v_mfma_f32_16x16x32_bf16 v[184:187], v[236:239], v[56:59], v[184:187]\n"
    "ds_read_b128 v[228:231], v13 offset:41984\n"
    "v_mfma_f32_16x16x32_bf16 v[188:191], v[232:235], v[52:55], v[188:191]\n"
    "buffer_load_dwordx4 v[92:95], v1, s[16:19], s43 offen offset:1024 sc1\n"
    "v_mfma_f32_16x16x32_bf16 v[188:191], v[236:239], v[60:63], v[188:191]\n"
    "s_waitcnt lgkmcnt(4)\n"
    "v_mfma_f32_16x16x32_bf16 v[192:195], v[48:51], v[240:243], v[192:195]\n"
    "s_barrier\n"
    "ds_read_b128 v[232:235], v13 offset:43008\n"
    "v_mfma_f32_16x16x32_bf16 v[192:195], v[56:59], v[244:247], v[192:195]\n"
    "ds_read_b128 v[236:239], v13 offset:50176\n"
    "v_mfma_f32_16x16x32_bf16 v[196:199], v[52:55], v[240:243], v[196:199]\n"
    "v_mfma_f32_16x16x32_bf16 v[196:199], v[60:63], v[244:247], v[196:199]\n"
    "s_waitcnt lgkmcnt(4)\n"
    "v_mfma_f32_16x16x32_bf16 v[200:203], v[48:51], v[248:251], v[200:203]\n"
    "ds_read_b128 v[240:243], v13 offset:44032\n"
    "v_mfma_f32_16x16x32_bf16 v[200:203], v[56:59], v[252:255], v[200:203]\n"
    "ds_read_b128 v[244:247], v13 offset:51200\n"
    "v_mfma_f32_16x16x32_bf16 v[204:207], v[52:55], v[248:251], v[204:207]\n"
    "v_mfma_f32_16x16x32_bf16 v[204:207], v[60:63], v[252:255], v[204:207]\n"
    "s_waitcnt lgkmcnt(4)\n"
    "v_mfma_f32_16x16x32_bf16 v[208:211], v[224:227], v[48:51], v[208:211]\n"
    "ds_read_b128 v[248:251], v13 offset:45056\n"
    "v_mfma_f32_16x16x32_bf16 v[208:211], v[228:231], v[56:59], v[208:211]\n"
    "ds_read_b128 v[252:255], v13 offset:52224\n"
    "v_mfma_f32_16x16x32_bf16 v[212:215], v[224:227], v[52:55], v[212:215]\n"
    "v_mfma_f32_16x16x32_bf16 v[212:215], v[228:231], v[60:63], v[212:215]\n"
    "s_waitcnt vmcnt(4) lgkmcnt(4)\n"
    "v_mfma_f32_16x16x32_bf16 v[160:163], v[232:235], v[64:67], v[160:163]\n"
    "ds_read_b128 v[224:227], v13 offset:46080\n"
    "v_mfma_f32_16x16x32_bf16 v[160:163], v[236:239], v[72:75], v[160:163]\n"
    "ds_read_b128 v[228:231], v13 offset:53248\n"
    "v_mfma_f32_16x16x32_bf16 v[164:167], v[232:235], v[68:71], v[164:167]\n"
    "buffer_load_dwordx4 v[96:99], v1, s[16:19], s42 offen offset:2048 sc1\n"
    "v_mfma_f32_16x16x32_bf16 v[164:167], v[236:239], v[76:79], v[164:167]\n"
    "s_waitcnt lgkmcnt(4)\n"
    "v_mfma_f32_16x16x32_bf16 v[168:171], v[240:243], v[64:67], v[168:171]\n"
    "ds_read_b128 v[232:235], v13 offset:47104\n"
    "v_mfma_f32_16x16x32_bf16 v[168:171], v[244:247], v[72:75], v[168:171]\n"
    "ds_read_b128 v[236:239], v13 offset:54272\n"
    "v_mfma_f32_16x16x32_bf16 v[172:175], v[240:243], v[68:71], v[172:175]\n"
    "buffer_load_dwordx4 v[100:103], v1, s[16:19], s43 offen offset:2048 sc1\n"
    "v_mfma_f32_16x16x32_bf16 v[172:175], v[244:247], v[76:79], v[172:175]\n"
    "s_waitcnt lgkmcnt(4)\n"
    "v_mfma_f32_16x16x32_bf16 v[176:179], v[248:251], v[64:67], v[176:179]\n"
    "ds_read_b128 v[240:243], v13 offset:48128\n"
    "v_mfma_f32_16x16x32_bf16 v[176:179], v[252:255], v[72:75], v[176:179]\n"
    "ds_read_b128 v[244:247], v13 offset:55296\n"
    "v_mfma_f32_16x16x32_bf16 v[180:183], v[248:251], v[68:71], v[180:183]\n"
    "buffer_load_dwordx4 v[104:107], v1, s[16:19], s42 offen offset:3072 sc1\n"
    "v_mfma_f32_16x16x32_bf16 v[180:183], v[252:255], v[76:79], v[180:183]\n"
    "s_waitcnt lgkmcnt(4)\n"
    "v_mfma_f32_16x16x32_bf16 v[184:187], v[224:227], v[64:67], v[184:187]\n"
    "ds_read_b128 v[248:251], v13 offset:49152\n"
    "v_mfma_f32_16x16x32_bf16 v[184:187], v[228:231], v[72:75], v[184:187]\n"
    "ds_read_b128 v[252:255], v13 offset:56320\n"
    "v_mfma_f32_16x16x32_bf16 v[188:191], v[224:227], v[68:71], v[188:191]\n"
    "buffer_load_dwordx4 v[108:111], v1, s[16:19], s43 offen offset:3072 sc1\n"
    "v_mfma_f32_16x16x32_bf16 v[188:191], v[228:231], v[76:79], v[188:191]\n"
    "s_waitcnt lgkmcnt(4)\n"
    "v_mfma_f32_16x16x32_bf16 v[192:195], v[64:67], v[232:235], v[192:195]\n"
    "s_barrier\n"
    "v_add_u32 v13, 57344, v13\n"
    "ds_read_b128 v[224:227], v13 offset:0\n"
    "v_mfma_f32_16x16x32_bf16 v[192:195], v[72:75], v[236:239], v[192:195]\n"
    "ds_read_b128 v[228:231], v13 offset:7168\n"
    "v_mfma_f32_16x16x32_bf16 v[196:199], v[68:71], v[232:235], v[196:199]\n"
    "v_mfma_f32_16x16x32_bf16 v[196:199], v[76:79], v[236:239], v[196:199]\n"
    "s_waitcnt lgkmcnt(4)\n"
    "v_mfma_f32_16x16x32_bf16 v[200:203], v[64:67], v[240:243], v[200:203]\n"
    "ds_read_b128 v[232:235], v13 offset:1024\n"
    "v_mfma_f32_16x16x32_bf16 v[200:203], v[72:75], v[244:247], v[200:203]\n"
    "ds_read_b128 v[236:239], v13 offset:8192\n"
    "v_mfma_f32_16x16x32_bf16 v[204:207], v[68:71], v[240:243], v[204:207]\n"
    "v_mfma_f32_16x16x32_bf16 v[204:207], v[76:79], v[244:247], v[204:207]\n"
    "s_waitcnt lgkmcnt(4)\n"
    "v_mfma_f32_16x16x32_bf16 v[208:211], v[248:251], v[64:67], v[208:211]\n"
    "ds_read_b128 v[240:243], v13 offset:2048\n"
    "v_mfma_f32_16x16x32_bf16 v[208:211], v[252:255], v[72:75], v[208:211]\n"
    "ds_read_b128 v[244:247], v13 offset:9216\n"
    "v_mfma_f32_16x16x32_bf16 v[212:215], v[248:251], v[68:71], v[212:215]\n"
    "v_mfma_f32_16x16x32_bf16 v[212:215], v[252:255], v[76:79], v[212:215]\n"
    "s_waitcnt vmcnt(4) lgkmcnt(4)\n"
    "v_mfma_f32_16x16x32_bf16 v[160:163], v[224:227], v[80:83], v[160:163]\n"
    "ds_read_b128 v[248:251], v13 offset:3072\n"
    "v_mfma_f32_16x16x32_bf16 v[160:163], v[228:231], v[88:91], v[160:163]\n"
    "ds_read_b128 v[252:255], v13 offset:10240\n"
    "v_mfma_f32_16x16x32_bf16 v[164:167], v[224:227], v[84:87], v[164:167]\n"
    "s_add_u32 s42, s42, 4096\n"
    "s_add_u32 s43, s43, 4096\n"
    "buffer_load_dwordx4 v[112:115], v1, s[16:19], s42 offen offset:0 sc1\n"
    "v_mfma_f32_16x16x32_bf16 v[164:167], v[228:231], v[92:95], v[164:167]\n"
    "s_waitcnt lgkmcnt(4)\n"
    "v_mfma_f32_16x16x32_bf16 v[168:171], v[232:235], v[80:83], v[168:171]\n"
    "ds_read_b128 v[224:227], v13 offset:4096\n"
    "v_mfma_f32_16x16x32_bf16 v[168:171], v[236:239], v[88:91], v[168:171]\n"
    "ds_read_b128 v[228:231], v13 offset:11264\n"
    "v_mfma_f32_16x16x32_bf16 v[172:175], v[232:235], v[84:87], v[172:175]\n"
    "buffer_load_dwordx4 v[116:119], v1, s[16:19], s43 offen offset:0 sc1\n"
    "v_mfma_f32_16x16x32_bf16 v[172:175], v[236:239], v[92:95], v[172:175]\n"
    "s_waitcnt lgkmcnt(4)\n"
    "v_mfma_f32_16x16x32_bf16 v[176:179], v[240:243], v[80:83], v[176:179]\n"
    "ds_read_b128 v[232:235], v13 offset:5120\n"
    "v_mfma_f32_16x16x32_bf16 v[176:179], v[244:247], v[88:91], v[176:179]\n"
    "ds_read_b128 v[236:239], v13 offset:12288\n"
    "v_mfma_f32_16x16x32_bf16 v[180:183], v[240:243], v[84:87], v[180:183]\n"
    "buffer_load_dwordx4 v[120:123], v1, s[16:19], s42 offen offset:1024 sc1\n"
    "v_mfma_f32_16x16x32_bf16 v[180:183], v[244:247], v[92:95], v[180:183]\n"
    "s_waitcnt lgkmcnt(4)\n"
    "v_mfma_f32_16x16x32_bf16 v[184:187], v[248:251], v[80:83], v[184:187]\n"
    "ds_read_b128 v[240:243], v13 offset:6144\n"
    "v_mfma_f32_16x16x32_bf16 v[184:187], v[252:255], v[88:91], v[184:187]\n"
    "ds_read_b128 v[244:247], v13 offset:13312\n"
    "v_mfma_f32_16x16x32_bf16 v[188:191], v[248:251], v[84:87], v[188:191]\n"
    "buffer_load_dwordx4 v[124:127], v1, s[16:19], s43 offen offset:1024 sc1\n"
    "v_mfma_f32_16x16x32_bf16 v[188:191], v[252:255], v[92:95], v[188:191]\n"
    "s_waitcnt lgkmcnt(4)\n"
    "v_mfma_f32_16x16x32_bf16 v[192:195], v[80:83], v[224:227], v[192:195]\n"
    "s_barrier\n"
    "ds_read_b128 v[248:251], v13 offset:14336\n"
    "v_mfma_f32_16x16x32_bf16 v[192:195], v[88:91], v[228:231], v[192:195]\n"
    "ds_read_b128 v[252:255], v13 offset:21504\n"
    "v_mfma_f32_16x16x32_bf16 v[196:199], v[84:87], v[224:227], v[196:199]\n"
    "v_mfma_f32_16x16x32_bf16 v[196:199], v[92:95], v[228:231], v[196:199]\n"
    "s_waitcnt lgkmcnt(4)\n"
    "v_mfma_f32_16x16x32_bf16 v[200:203], v[80:83], v[232:235], v[200:203]\n"
    "ds_read_b128 v[224:227], v13 offset:15360\n"
    "v_mfma_f32_16x16x32_bf16 v[200:203], v[88:91], v[236:239], v[200:203]\n"
    "ds_read_b128 v[228:231], v13 offset:22528\n"
    "v_mfma_f32_16x16x32_bf16 v[204:207], v[84:87], v[232:235], v[204:207]\n"
    "v_mfma_f32_16x16x32_bf16 v[204:207], v[92:95], v[236:239], v[204:207]\n"
    "s_waitcnt lgkmcnt(4)\n"
    "v_mfma_f32_16x16x32_bf16 v[208:211], v[240:243], v[80:83], v[208:211]\n"
    "ds_read_b128 v[232:235], v13 offset:16384\n"
    "v_mfma_f32_16x16x32_bf16 v[208:211], v[244:247], v[88:91], v[208:211]\n"
    "ds_read_b128 v[236:239], v13 offset:23552\n"
    "v_mfma_f32_16x16x32_bf16 v[212:215], v[240:243], v[84:87], v[212:215]\n"
    "v_mfma_f32_16x16x32_bf16 v[212:215], v[244:247], v[92:95], v[212:215]\n"
    "s_waitcnt vmcnt(4) lgkmcnt(4)\n"
    "v_mfma_f32_16x16x32_bf16 v[160:163], v[248:251], v[96:99], v[160:163]\n"
    "ds_read_b128 v[240:243], v13 offset:17408\n"
    "v_mfma_f32_16x16x32_bf16 v[160:163], v[252:255], v[104:107], v[160:163]\n"
    "ds_read_b128 v[244:247], v13 offset:24576\n"
    "v_mfma_f32_16x16x32_bf16 v[164:167], v[248:251], v[100:103], v[164:167]\n"
    "buffer_load_dwordx4 v[128:131], v1, s[16:19], s42 offen offset:2048 sc1\n"
    "v_mfma_f32_16x16x32_bf16 v[164:167], v[252:255], v[108:111], v[164:167]\n"
    "v_lshrrev_b32 v74, 4, v0\n"
    "s_waitcnt lgkmcnt(4)\n"
    "v_mfma_f32_16x16x32_bf16 v[168:171], v[224:227], v[96:99], v[168:171]\n"
    "ds_read_b128 v[248:251], v13 offset:18432\n"
    "v_mfma_f32_16x16x32_bf16 v[168:171], v[228:231], v[104:107], v[168:171]\n"
    "ds_read_b128 v[252:255], v13 offset:25600\n"
    "v_mfma_f32_16x16x32_bf16 v[172:175], v[224:227], v[100:103], v[172:175]\n"
    "buffer_load_dwordx4 v[132:135], v1, s[16:19], s43 offen offset:2048 sc1\n"
    "v_mfma_f32_16x16x32_bf16 v[172:175], v[228:231], v[108:111], v[172:175]\n"
    "v_mul_u32_u24 v74, 20, v74\n"
    "s_waitcnt lgkmcnt(4)\n"
    "v_mfma_f32_16x16x32_bf16 v[176:179], v[232:235], v[96:99], v[176:179]\n"
    "ds_read_b128 v[224:227], v13 offset:19456\n"
    "v_mfma_f32_16x16x32_bf16 v[176:179], v[236:239], v[104:107], v[176:179]\n"
    "ds_read_b128 v[228:231], v13 offset:26624\n"
    "v_mfma_f32_16x16x32_bf16 v[180:183], v[232:235], v[100:103], v[180:183]\n"
    "buffer_load_dwordx4 v[136:139], v1, s[16:19], s42 offen offset:3072 sc1\n"
    "v_mfma_f32_16x16x32_bf16 v[180:183], v[236:239], v[108:111], v[180:183]\n"
    "v_sub_u32 v74, v0, v74\n"
    "s_waitcnt lgkmcnt(4)\n"
    "v_mfma_f32_16x16x32_bf16 v[184:187], v[240:243], v[96:99], v[184:187]\n"
    "ds_read_b128 v[232:235], v13 offset:20480\n"
    "v_mfma_f32_16x16x32_bf16 v[184:187], v[244:247], v[104:107], v[184:187]\n"
    "ds_read_b128 v[236:239], v13 offset:27648\n"
    "v_mfma_f32_16x16x32_bf16 v[188:191], v[240:243], v[100:103], v[188:191]\n"
    "buffer_load_dwordx4 v[140:143], v1, s[16:19], s43 offen offset:3072 sc1\n"
    "v_mfma_f32_16x16x32_bf16 v[188:191], v[244:247], v[108:111], v[188:191]\n"
    "v_mov_b32 v72, 0x3f80\n"
    "s_waitcnt lgkmcnt(4)\n"
    "v_mfma_f32_16x16x32_bf16 v[192:195], v[96:99], v[248:251], v[192:195]\n"
    "s_barrier\n"
    "ds_read_b128 v[240:243], v13 offset:28672\n"
    "v_mfma_f32_16x16x32_bf16 v[192:195], v[104:107], v[252:255], v[192:195]\n"
    "ds_read_b128 v[244:247], v13 offset:35840\n"
    "v_mfma_f32_16x16x32_bf16 v[196:199], v[100:103], v[248:251], v[196:199]\n"
    "v_lshlrev_b32 v11, 4, v74\n"
    "v_mfma_f32_16x16x32_bf16 v[196:199], v[108:111], v[252:255], v[196:199]\n"
    "v_lshlrev_b32 v72, v11, v72\n"
    "s_waitcnt lgkmcnt(4)\n"
    "v_mfma_f32_16x16x32_bf16 v[200:203], v[96:99], v[224:227], v[200:203]\n"
    "ds_read_b128 v[248:251], v13 offset:29696\n"
    "v_mfma_f32_16x16x32_bf16 v[200:203], v[104:107], v[228:231], v[200:203]\n"
    "ds_read_b128 v[252:255], v13 offset:36864\n"
    "v_mfma_f32_16x16x32_bf16 v[204:207], v[100:103], v[224:227], v[204:207]\n"
    "v_cmp_gt_u32 vcc, 2, v74\n"
    "s_nop 1\n"
    "v_cndmask_b32 v72, 0, v72, vcc\n"
    "v_mfma_f32_16x16x32_bf16 v[204:207], v[108:111], v[228:231], v[204:207]\n"
    "v_subrev_u32 v74, 2, v74\n"
    "s_waitcnt lgkmcnt(4)\n"
    "v_mfma_f32_16x16x32_bf16 v[208:211], v[232:235], v[96:99], v[208:211]\n"
    "ds_read_b128 v[224:227], v13 offset:30720\n"
    "v_mfma_f32_16x16x32_bf16 v[208:211], v[236:239], v[104:107], v[208:211]\n"
    "ds_read_b128 v[228:231], v13 offset:37888\n"
    "v_mfma_f32_16x16x32_bf16 v[212:215], v[232:235], v[100:103], v[212:215]\n"
    "v_mov_b32 v73, 0x3f80\n"
    "v_mfma_f32_16x16x32_bf16 v[212:215], v[236:239], v[108:111], v[212:215]\n"
    "v_lshlrev_b32 v11, 4, v74\n"
    "s_waitcnt vmcnt(4) lgkmcnt(4)\n"
    "v_mfma_f32_16x16x32_bf16 v[160:163], v[240:243], v[112:115], v[160:163]\n"
    "ds_read_b128 v[232:235], v13 offset:31744\n"
    "v_mfma_f32_16x16x32_bf16 v[160:163], v[244:247], v[120:123], v[160:163]\n"
    "ds_read_b128 v[236:239], v13 offset:38912\n"
    "v_mfma_f32_16x16x32_bf16 v[164:167], v[240:243], v[116:119], v[164:167]\n"
    "s_add_u32 s42, s42, 4096\n"
    "s_add_u32 s43, s43, 4096\n"
    "buffer_load_dwordx4 v[144:147], v1, s[16:19], s42 offen offset:0 sc1\n"
    "v_mfma_f32_16x16x32_bf16 v[164:167], v[244:247], v[124:127], v[164:167]\n"
    "v_lshlrev_b32 v73, v11, v73\n"
    "s_waitcnt lgkmcnt(4)\n"
    "v_mfma_f32_16x16x32_bf16 v[168:171], v[248:251], v[112:115], v[168:171]\n"
    "ds_read_b128 v[240:243], v13 offset:32768\n"
    "v_mfma_f32_16x16x32_bf16 v[168:171], v[252:255], v[120:123], v[168:171]\n"
    "ds_read_b128 v[244:247], v13 offset:39936\n"
    "v_mfma_f32_16x16x32_bf16 v[172:175], v[248:251], v[116:119], v[172:175]\n"
    "buffer_load_dwordx4 v[148:151], v1, s[16:19], s43 offen offset:0 sc1\n"
    "v_mfma_f32_16x16x32_bf16 v[172:175], v[252:255], v[124:127], v[172:175]\n"
    "v_cmp_gt_u32 vcc, 2, v74\n"
    "s_nop 1\n"
    "v_cndmask_b32 v73, 0, v73, vcc\n"
    "s_waitcnt lgkmcnt(4)\n"
    "v_mfma_f32_16x16x32_bf16 v[176:179], v[224:227], v[112:115], v[176:179]\n"
    "ds_read_b128 v[248:251], v13 offset:33792\n"
    "v_mfma_f32_16x16x32_bf16 v[176:179], v[228:231], v[120:123], v[176:179]\n"
    "ds_read_b128 v[252:255], v13 offset:40960\n"
    "v_mfma_f32_16x16x32_bf16 v[180:183], v[224:227], v[116:119], v[180:183]\n"
    "buffer_load_dwordx4 v[152:155], v1, s[16:19], s42 offen offset:1024 sc1\n"
    "v_mfma_f32_16x16x32_bf16 v[180:183], v[228:231], v[124:127], v[180:183]\n"
    "v_mov_b32 v74, 0\n"
    "s_waitcnt lgkmcnt(4)\n"
    "v_mfma_f32_16x16x32_bf16 v[184:187], v[232:235], v[112:115], v[184:187]\n"
    "ds_read_b128 v[224:227], v13 offset:34816\n"
    "v_mfma_f32_16x16x32_bf16 v[184:187], v[236:239], v[120:123], v[184:187]\n"
    "ds_read_b128 v[228:231], v13 offset:41984\n"
    "v_mfma_f32_16x16x32_bf16 v[188:191], v[232:235], v[116:119], v[188:191]\n"
    "buffer_load_dwordx4 v[156:159], v1, s[16:19], s43 offen offset:1024 sc1\n"
    "v_mfma_f32_16x16x32_bf16 v[188:191], v[236:239], v[124:127], v[188:191]\n"
    "v_mov_b32 v75, 0\n"
    "s_waitcnt lgkmcnt(4)\n"
    "v_mfma_f32_16x16x32_bf16 v[192:195], v[112:115], v[240:243], v[192:195]\n"
    "s_barrier\n"
    "ds_read_b128 v[232:235], v13 offset:43008\n"
    "v_mfma_f32_16x16x32_bf16 v[192:195], v[120:123], v[244:247], v[192:195]\n"
    "ds_read_b128 v[236:239], v13 offset:50176\n"
    "v_mfma_f32_16x16x32_bf16 v[196:199], v[116:119], v[240:243], v[196:199]\n"
    "v_mov_b32 v66, 0\n"
    "v_mfma_f32_16x16x32_bf16 v[196:199], v[124:127], v[244:247], v[196:199]\n"
    "v_mov_b32 v67, 0\n"
    "s_waitcnt lgkmcnt(4)\n"
    "v_mfma_f32_16x16x32_bf16 v[200:203], v[112:115], v[248:251], v[200:203]\n"
    "ds_read_b128 v[240:243], v13 offset:44032\n"
    "v_mfma_f32_16x16x32_bf16 v[200:203], v[120:123], v[252:255], v[200:203]\n"
    "ds_read_b128 v[244:247], v13 offset:51200\n"
    "v_mfma_f32_16x16x32_bf16 v[204:207], v[116:119], v[248:251], v[204:207]\n"
    "v_mov_b32 v70, 0\n"
    "v_mfma_f32_16x16x32_bf16 v[204:207], v[124:127], v[252:255], v[204:207]\n"
    "v_mov_b32 v71, 0\n"
    "s_waitcnt lgkmcnt(4)\n"
    "v_mfma_f32_16x16x32_bf16 v[208:211], v[224:227], v[112:115], v[208:211]\n"
    "ds_read_b128 v[248:251], v13 offset:45056\n"
    "v_mfma_f32_16x16x32_bf16 v[208:211], v[228:231], v[120:123], v[208:211]\n"
    "ds_read_b128 v[252:255], v13 offset:52224\n"
    "v_mfma_f32_16x16x32_bf16 v[212:215], v[224:227], v[116:119], v[212:215]\n"
    "v_mfma_f32_16x16x32_bf16 v[212:215], v[228:231], v[124:127], v[212:215]\n"
    "s_waitcnt vmcnt(4) lgkmcnt(4)\n"
    "v_mfma_f32_16x16x32_bf16 v[160:163], v[232:235], v[128:131], v[160:163]\n"
    "ds_read_b128 v[224:227], v13 offset:46080\n"
    "v_mfma_f32_16x16x32_bf16 v[160:163], v[236:239], v[136:139], v[160:163]\n"
    "ds_read_b128 v[228:231], v13 offset:53248\n"
    "v_mfma_f32_16x16x32_bf16 v[164:167], v[232:235], v[132:135], v[164:167]\n"
    "v_mfma_f32_16x16x32_bf16 v[164:167], v[236:239], v[140:143], v[164:167]\n"
    "s_waitcnt lgkmcnt(4)\n"
    "v_mfma_f32_16x16x32_bf16 v[168:171], v[240:243], v[128:131], v[168:171]\n"
    "ds_read_b128 v[232:235], v13 offset:47104\n"
    "v_mfma_f32_16x16x32_bf16 v[168:171], v[244:247], v[136:139], v[168:171]\n"
    "ds_read_b128 v[236:239], v13 offset:54272\n"
    "v_mfma_f32_16x16x32_bf16 v[172:175], v[240:243], v[132:135], v[172:175]\n"
    "v_mfma_f32_16x16x32_bf16 v[172:175], v[244:247], v[140:143], v[172:175]\n"
    "s_waitcnt lgkmcnt(4)\n"
    "v_mfma_f32_16x16x32_bf16 v[176:179], v[248:251], v[128:131], v[176:179]\n"
    "ds_read_b128 v[240:243], v13 offset:48128\n"
    "v_mfma_f32_16x16x32_bf16 v[176:179], v[252:255], v[136:139], v[176:179]\n"
    "ds_read_b128 v[244:247], v13 offset:55296\n"
    "v_mfma_f32_16x16x32_bf16 v[180:183], v[248:251], v[132:135], v[180:183]\n"
    "v_mfma_f32_16x16x32_bf16 v[180:183], v[252:255], v[140:143], v[180:183]\n"
    "s_waitcnt lgkmcnt(4)\n"
    "v_mfma_f32_16x16x32_bf16 v[184:187], v[224:227], v[128:131], v[184:187]\n"
    "ds_read_b128 v[248:251], v13 offset:49152\n"
    "v_mfma_f32_16x16x32_bf16 v[184:187], v[228:231], v[136:139], v[184:187]\n"
    "ds_read_b128 v[252:255], v13 offset:56320\n"
    "v_mfma_f32_16x16x32_bf16 v[188:191], v[224:227], v[132:135], v[188:191]\n"
    "v_mfma_f32_16x16x32_bf16 v[188:191], v[228:231], v[140:143], v[188:191]\n"
    "s_waitcnt lgkmcnt(4)\n"
    "v_mfma_f32_16x16x32_bf16 v[192:195], v[128:131], v[232:235], v[192:195]\n"
    "s_barrier\n"
    "v_add_u32 v13, 57344, v13\n"
    "ds_read_b128 v[224:227], v13 offset:0\n"
    "v_mfma_f32_16x16x32_bf16 v[192:195], v[136:139], v[236:239], v[192:195]\n"
    "ds_read_b128 v[228:231], v13 offset:7168\n"
    "v_mfma_f32_16x16x32_bf16 v[196:199], v[132:135], v[232:235], v[196:199]\n"
    "v_mfma_f32_16x16x32_bf16 v[196:199], v[140:143], v[236:239], v[196:199]\n"
    "s_waitcnt lgkmcnt(4)\n"
    "v_mfma_f32_16x16x32_bf16 v[200:203], v[128:131], v[240:243], v[200:203]\n"
    "ds_read_b128 v[232:235], v13 offset:1024\n"
    "v_mfma_f32_16x16x32_bf16 v[200:203], v[136:139], v[244:247], v[200:203]\n"
    "ds_read_b128 v[236:239], v13 offset:8192\n"
    "v_mfma_f32_16x16x32_bf16 v[204:207], v[132:135], v[240:243], v[204:207]\n"
    "v_mfma_f32_16x16x32_bf16 v[204:207], v[140:143], v[244:247], v[204:207]\n"
    "s_waitcnt lgkmcnt(4)\n"
    "v_mfma_f32_16x16x32_bf16 v[208:211], v[248:251], v[128:131], v[208:211]\n"
    "ds_read_b128 v[240:243], v13 offset:2048\n"
    "v_mfma_f32_16x16x32_bf16 v[208:211], v[252:255], v[136:139], v[208:211]\n"
    "ds_read_b128 v[244:247], v13 offset:9216\n"
    "v_mfma_f32_16x16x32_bf16 v[212:215], v[248:251], v[132:135], v[212:215]\n"
    "v_mfma_f32_16x16x32_bf16 v[212:215], v[252:255], v[140:143], v[212:215]\n"
    "s_waitcnt vmcnt(0) lgkmcnt(4)\n"
    "v_mfma_f32_16x16x32_bf16 v[160:163], v[224:227], v[144:147], v[160:163]\n"
    "ds_read_b128 v[248:251], v13 offset:3072\n"
    "v_mfma_f32_16x16x32_bf16 v[160:163], v[228:231], v[152:155], v[160:163]\n"
    "ds_read_b128 v[252:255], v13 offset:10240\n"
    "v_mfma_f32_16x16x32_bf16 v[164:167], v[224:227], v[148:151], v[164:167]\n"
    "v_mfma_f32_16x16x32_bf16 v[164:167], v[228:231], v[156:159], v[164:167]\n"
    "s_waitcnt lgkmcnt(4)\n"
    "v_mfma_f32_16x16x32_bf16 v[168:171], v[232:235], v[144:147], v[168:171]\n"
    "ds_read_b128 v[224:227], v13 offset:4096\n"
    "v_mfma_f32_16x16x32_bf16 v[168:171], v[236:239], v[152:155], v[168:171]\n"
    "ds_read_b128 v[228:231], v13 offset:11264\n"
    "v_mfma_f32_16x16x32_bf16 v[172:175], v[232:235], v[148:151], v[172:175]\n"
    "v_mfma_f32_16x16x32_bf16 v[172:175], v[236:239], v[156:159], v[172:175]\n"
    "s_waitcnt lgkmcnt(4)\n"
    "v_mfma_f32_16x16x32_bf16 v[176:179], v[240:243], v[144:147], v[176:179]\n"
    "ds_read_b128 v[232:235], v13 offset:5120\n"
    "v_mfma_f32_16x16x32_bf16 v[176:179], v[244:247], v[152:155], v[176:179]\n"
    "ds_read_b128 v[236:239], v13 offset:12288\n"
    "v_mfma_f32_16x16x32_bf16 v[180:183], v[240:243], v[148:151], v[180:183]\n"
    "v_mfma_f32_16x16x32_bf16 v[180:183], v[244:247], v[156:159], v[180:183]\n"
    "s_waitcnt lgkmcnt(4)\n"
    "v_mfma_f32_16x16x32_bf16 v[184:187], v[248:251], v[144:147], v[184:187]\n"
    "ds_read_b128 v[240:243], v13 offset:6144\n"
    "v_mfma_f32_16x16x32_bf16 v[184:187], v[252:255], v[152:155], v[184:187]\n"
    "ds_read_b128 v[244:247], v13 offset:13312\n"
    "v_mfma_f32_16x16x32_bf16 v[188:191], v[248:251], v[148:151], v[188:191]\n"
    "v_mfma_f32_16x16x32_bf16 v[188:191], v[252:255], v[156:159], v[188:191]\n"
    "s_waitcnt lgkmcnt(4)\n"
    "v_mfma_f32_16x16x32_bf16 v[192:195], v[144:147], v[224:227], v[192:195]\n"
    "v_mfma_f32_16x16x32_bf16 v[192:195], v[152:155], v[228:231], v[192:195]\n"
    "v_mfma_f32_16x16x32_bf16 v[196:199], v[148:151], v[224:227], v[196:199]\n"
    "v_mfma_f32_16x16x32_bf16 v[196:199], v[156:159], v[228:231], v[196:199]\n"
    "s_waitcnt lgkmcnt(2)\n"
    "v_mfma_f32_16x16x32_bf16 v[200:203], v[144:147], v[232:235], v[200:203]\n"
    "v_mfma_f32_16x16x32_bf16 v[200:203], v[152:155], v[236:239], v[200:203]\n"
    "v_mfma_f32_16x16x32_bf16 v[204:207], v[148:151], v[232:235], v[204:207]\n"
    "v_mfma_f32_16x16x32_bf16 v[204:207], v[156:159], v[236:239], v[204:207]\n"
    "s_waitcnt lgkmcnt(0)\n"
    "v_mfma_f32_16x16x32_bf16 v[208:211], v[240:243], v[144:147], v[208:211]\n"
    "v_mfma_f32_16x16x32_bf16 v[208:211], v[244:247], v[152:155], v[208:211]\n"
    "v_mfma_f32_16x16x32_bf16 v[212:215], v[240:243], v[148:151], v[212:215]\n"
    "v_mfma_f32_16x16x32_bf16 v[212:215], v[244:247], v[156:159], v[212:215]\n"
    "s_mov_b32 s44, 0x3eae1a93\n"
    "s_mov_b32 s45, 0x3eae1a93\n"
    "v_cvt_pk_bf16_f32 v32, v192, v193\n"
    "v_cvt_pk_bf16_f32 v33, v194, v195\n"
    "v_cvt_pk_bf16_f32 v34, v196, v197\n"
    "v_cvt_pk_bf16_f32 v35, v198, v199\n"
    "s_nop 0\n"
    "v_permlane16_swap_b32 v32, v34\n"
    "v_permlane16_swap_b32 v33, v35\n"
    "buffer_store_dwordx4 v[32:35], v6, s[16:19], 0 offen sc1\n"
    "v_cvt_pk_bf16_f32 v36, v200, v201\n"
    "v_cvt_pk_bf16_f32 v37, v202, v203\n"
    "v_cvt_pk_bf16_f32 v38, v204, v205\n"
    "v_cvt_pk_bf16_f32 v39, v206, v207\n"
    "s_nop 0\n"
    "v_permlane16_swap_b32 v36, v38\n"
    "v_permlane16_swap_b32 v37, v39\n"
    "buffer_store_dwordx4 v[36:39], v7, s[16:19], 0 offen sc1\n"
    "v_cvt_pk_bf16_f32 v64, v208, v209\n"
    "v_cvt_pk_bf16_f32 v65, v210, v211\n"
    "v_cvt_pk_bf16_f32 v68, v212, v213\n"
    "v_cvt_pk_bf16_f32 v69, v214, v215\n"
    "v_mfma_f32_16x16x32_bf16 v[216:219], v[64:67], v[72:75], 0\n"
    "s_nop 0\n"
    "v_mfma_f32_16x16x32_bf16 v[220:223], v[68:71], v[72:75], 0\n"
    "v_cvt_pk_bf16_f32 v16, v176, v177\n"
    "v_cvt_pk_bf16_f32 v17, v178, v179\n"
    "v_cvt_pk_bf16_f32 v18, v184, v185\n"
    "v_cvt_pk_bf16_f32 v19, v186, v187\n"
    "buffer_store_dwordx4 v[16:19], v3, s[16:19], 0 offen offset:0 sc1\n"
    "v_cvt_pk_bf16_f32 v20, v180, v181\n"
    "v_cvt_pk_bf16_f32 v21, v182, v183\n"
    "v_cvt_pk_bf16_f32 v22, v188, v189\n"
    "v_cvt_pk_bf16_f32 v23, v190, v191\n"
    "buffer_store_dwordx4 v[20:23], v3, s[16:19], 0 offen offset:256 sc1\n"
    "v_cvt_pk_bf16_f32 v44, v216, v217\n"
    "v_cvt_pk_bf16_f32 v45, v218, v219\n"
    "v_cvt_pk_bf16_f32 v46, v220, v221\n"
    "v_cvt_pk_bf16_f32 v47, v222, v223\n"
    "s_nop 0\n"
    "v_permlane16_swap_b32 v44, v46\n"
    "v_permlane16_swap_b32 v45, v47\n"
    "buffer_store_dwordx4 v[44:47], v8, s[16:19], 0 offen sc1\n"
    "v_mul_f32 v208, v9, v208\n"
    "v_mul_f32 v209, v9, v209\n"
    "v_mul_f32 v210, v9, v210\n"
    "v_mul_f32 v211, v9, v211\n"
    "v_mul_f32 v212, v9, v212\n"
    "v_mul_f32 v213, v9, v213\n"
    "v_mul_f32 v214, v9, v214\n"
    "v_mul_f32 v215, v9, v215\n"
    "v_cvt_pk_bf16_f32 v40, v208, v209\n"
    "v_cvt_pk_bf16_f32 v41, v210, v211\n"
    "v_cvt_pk_bf16_f32 v42, v212, v213\n"
    "v_cvt_pk_bf16_f32 v43, v214, v215\n"
    "s_nop 0\n"
    "v_permlane16_swap_b32 v40, v42\n"
    "v_permlane16_swap_b32 v41, v43\n"
    "buffer_store_dword v40, v4, s[16:19], 0 offen sc1\n"
    "buffer_store_dword v41, v5, s[16:19], 0 offen sc1\n"
    "v_pk_mul_f32 v[160:161], v[160:161], s[44:45]\n"
    "v_pk_mul_f32 v[162:163], v[162:163], s[44:45]\n"
    "v_pk_mul_f32 v[164:165], v[164:165], s[44:45]\n"
    "v_pk_mul_f32 v[166:167], v[166:167], s[44:45]\n"
    "v_pk_mul_f32 v[168:169], v[168:169], s[44:45]\n"
    "v_pk_mul_f32 v[170:171], v[170:171], s[44:45]\n"
    "v_pk_mul_f32 v[172:173], v[172:173], s[44:45]\n"
    "v_pk_mul_f32 v[174:175], v[174:175], s[44:45]\n"
    "v_cvt_pk_bf16_f32 v24, v160, v161\n"
    "v_cvt_pk_bf16_f32 v25, v162, v163\n"
    "v_cvt_pk_bf16_f32 v26, v168, v169\n"
    "v_cvt_pk_bf16_f32 v27, v170, v171\n"
    "buffer_store_dwordx4 v[24:27], v2, s[16:19], 0 offen offset:0 sc1\n"
    "v_cvt_pk_bf16_f32 v28, v164, v165\n"
    "v_cvt_pk_bf16_f32 v29, v166, v167\n"
    "v_cvt_pk_bf16_f32 v30, v172, v173\n"
    "v_cvt_pk_bf16_f32 v31, v174, v175\n"
    "buffer_store_dwordx4 v[28:31], v2, s[16:19], 0 offen offset:256 sc1\n"
    "kb_qj7_%=:\n"
    "s_and_b32 s72, s2, 7\n"
    "s_bfe_u32 s39, s2, 0x20003\n"
    "s_lshl2_add_u32 s72, s72, s39\n"
    "s_lshr_b32 s73, s2, 5\n"
    "s_and_b32 s39, s3, 3\n"
    "s_lshl_b32 s39, s39, 1\n"
    "s_lshl3_add_u32 s73, s73, s39\n"
    "s_lshr_b32 s39, s3, 2\n"
    "s_add_u32 s73, s73, s39\n"
    "s_lshl_b32 s75, s3, 10\n"
    "s_mul_i32 s61, s72, 0x12000\n"
    "s_add_u32 s61, s61, 0x478000\n"
    "s_mul_i32 s62, s73, 0x480\n"
    "s_add_u32 s62, s62, s61\n"
    "s_xor_b32 s63, s73, 1\n"
    "s_mul_i32 s63, s63, 0x480\n"
    "s_add_u32 s63, s63, s61\n"
    "v_and_b32 v48, 31, v0\n"
    "v_lshlrev_b32 v48, 2, v48\n"
    "v_lshrrev_b32 v148, 5, v0\n"
    "v_xor_b32 v148, s73, v148\n"
    "v_mul_u32_u24 v148, 0x480, v148\n"
    "v_add_u32 v148, v148, v48\n"
    "s_sub_u32 s39, s77, s3\n"
    "s_lshr_b32 s39, s39, 2\n"
    "s_lshl4_add_u32 s39, s76, s39\n"
    "s_lshl_b32 s39, s39, 7\n"
    "s_add_u32 s39, s39, 0x20000\n"
    "v_mov_b32 v149, s39\n"
    "s_lshl2_add_u32 s39, s2, 0x34000\n"
    "v_mov_b32 v11, s39\n"
    "v_mov_b32 v12, 0x80000000\n"
    "v_cmp_eq_u32 vcc, 1, v0\n"
    "s_nop 1\n"
    "v_cndmask_b32 v11, v12, v11, vcc\n"
    "v_cmp_eq_u32 vcc, 0, v0\n"
    "s_nop 1\n"
    "v_cndmask_b32 v149, v11, v149, vcc\n"
    "s_lshr_b32 s39, s72, 1\n"
    "s_lshl_b32 s39, s39, 11\n"
    "s_add_u32 s39, s39, 0x20000\n"
    "v_and_b32 v150, 15, v0\n"
    "v_lshl_add_u32 v150, v150, 7, s39\n"
    "s_mul_i32 s38, s72, 0x24000\n"
    "s_add_u32 s38, s38, 0x700000\n"
    "s_add_u32 s20, s4, s38\n"
    "s_addc_u32 s21, s5, 0\n"
    "s_and_b32 s21, s21, 0xffff\n"
    "s_mov_b32 s22, 147456\n"
    "s_mov_b32 s23, 0x20000\n"
    "s_mov_b32 s41, 0\n"
    "kb_start16_%=:\n"
    "s_lshr_b32 s74, s3, 2\n"
    "s_mul_i32 s74, s74, 0x900\n"
    "v_add_u32 v137, s74, v1\n"
    "v_add_u32 v139, s74, v48\n"
    "s_add_u32 s39, s74, 0x400\n"
    "v_lshl_add_u32 v138, v48, 4, s39\n"
    "v_lshrrev_b32 v11, 5, v0\n"
    "v_bfe_u32 v12, v0, 2, 2\n"
    "v_xor_b32 v11, v11, v12\n"
    "v_lshl_add_u32 v138, v11, 4, v138\n"
    "v_xor_b32 v2, 32, v138\n"
    "s_mov_b32 s42, 0x3ffff\n"
    "s_mov_b32 s43, 0x3ffff\n"
    "v_mov_b32 v136, 0xdddddddd\n"
    "v_mov_b32 v127, 0x3f803f80\n"
    "v_mov_b32 v121, 0\n"
    "v_mov_b64 v[80:81], 0\n"
    "v_mov_b64 v[82:83], 0\n"
    "v_mov_b64 v[84:85], 0\n"
    "v_mov_b64 v[86:87], 0\n"
    "v_mov_b64 v[88:89], 0\n"
    "v_mov_b64 v[90:91], 0\n"
    "v_mov_b64 v[92:93], 0\n"
    "v_mov_b64 v[94:95], 0\n"
    "v_mov_b64 v[96:97], 0\n"
    "v_mov_b64 v[98:99], 0\n"
    "v_mov_b64 v[100:101], 0\n"
    "v_mov_b64 v[102:103], 0\n"
    "v_mov_b64 v[104:105], 0\n"
    "v_mov_b64 v[106:107], 0\n"
    "v_mov_b64 v[108:109], 0\n"
    "v_mov_b64 v[110:111], 0\n"
    "v_mov_b64 v[128:129], 0\n"
    "v_mov_b64 v[130:131], 0\n"
    "v_mov_b64 v[132:133], 0\n"
    "v_mov_b64 v[134:135], 0\n"
    "s_mov_b32 exec_lo, 0x40000\n"
    "s_mov_b32 exec_hi, 0x40000\n"
    "v_mov_b32 v128, 0x3f803f80\n"
    "v_mov_b32 v129, 0x3f803f80\n"
    "v_mov_b64 v[130:131], v[128:129]\n"
    "v_mov_b64 v[132:133], v[128:129]\n"
    "v_mov_b64 v[134:135], v[128:129]\n"
    "s_mov_b64 exec, -1\n"
    "s_cmp_lg_u32 s41, 0\n"
    "s_cbranch_scc1 kb_slow18_%=\n"
    "v_mov_b32 v151, s60\n"
    "s_mov_b32 m0, s75\n"
    "s_lshl3_add_u32 s99, s2, s3\n"
    "s_mul_i32 s99, s99, 0x900\n"
    "s_add_u32 s99, s99, 0xb80000\n"
    "v_mov_b32 v240, -1\n"
    "v_mov_b32 v241, -1\n"
    "v_mov_b32 v242, -1\n"
    "v_mov_b32 v243, -1\n"
    "s_lshl3_add_u32 s39, s2, s3\n"
    "s_lshl2_add_u32 s39, s39, 0xc000\n"
    "v_mov_b32 v13, s39\n"
    "v_mov_b32 v11, 0x80000000\n"
    "v_cmp_eq_u32 vcc, 0, v0\n"
    "s_nop 1\n"
    "v_cndmask_b32 v13, v11, v13, vcc\n"
    "s_mul_i32 s76, s2, 0xe39\n"
    "s_lshr_b32 s76, s76, 16\n"
    "s_sub_u32 s77, s2, 0xa2\n"
    "s_mul_i32 s78, s77, 0x1c72\n"
    "s_lshr_b32 s78, s78, 16\n"
    "s_lshl3_add_u32 s79, s78, s3\n"
    "s_cmp_lt_u32 s2, 0xa2\n"
    "s_cselect_b32 s79, s76, s79\n"
    "s_mul_i32 s80, s76, 18\n"
    "s_sub_u32 s80, s2, s80\n"
    "s_lshr_b32 s80, s80, 1\n"
    "s_lshl3_add_u32 s80, s80, s3\n"
    "s_mul_i32 s39, s78, 9\n"
    "s_sub_u32 s39, s77, s39\n"
    "s_cmp_lt_u32 s2, 0xa2\n"
    "s_cselect_b32 s80, s80, s39\n"
    "s_mov_b32 s81, 0x480\n"
    "s_cmp_lt_u32 s2, 0xa2\n"
    "s_cselect_b32 s81, 0x2400, s81\n"
    "s_lshl_b32 s82, s81, 5\n"
    "s_mov_b32 s84, 0x1200\n"
    "s_cmp_lt_u32 s2, 0xa2\n"
    "s_cselect_b32 s84, 0x12000, s84\n"
    "s_lshl_b32 s83, s84, 1\n"
    "s_mul_i32 s76, s79, s81\n"
    "s_lshl_b32 s76, s76, 6\n"
    "s_lshl_b32 s39, s80, 7\n"
    "s_add_u32 s76, s76, s39\n"
    "s_cmp_lt_u32 s2, 0xf3\n"
    "s_cselect_b32 s76, s76, 0x80000000\n"
    "s_mov_b32 s77, 0x4800\n"
    "s_cmp_lt_u32 s2, 0xa2\n"
    "s_cselect_b32 s77, 0x9000, s77\n"
    "v_lshrrev_b32 v4, 5, v0\n"
    "v_mul_u32_u24 v4, s77, v4\n"
    "v_add_u32 v4, s76, v4\n"
    "v_bfe_u32 v11, v0, 3, 2\n"
    "v_mul_u32_u24 v11, s81, v11\n"
    "v_add_u32 v4, v4, v11\n"
    "v_and_b32 v11, 7, v0\n"
    "v_lshl_add_u32 v4, v11, 4, v4\n"
    "s_bitcmp1_b32 s2, 0\n"
    "s_cselect_b64 s[24:25], s[10:11], s[8:9]\n"
    "s_cmp_lt_u32 s2, 162\n"
    "s_cselect_b64 s[24:25], s[24:25], s[12:13]\n"
    "s_and_b32 s25, s25, 0xffff\n"
    "s_mov_b32 s26, 5308416\n"
    "s_mov_b32 s27, 0x20000\n"
    "s_and_b32 s45, s3, 3\n"
    "s_lshl_b32 s45, s45, 12\n"
    "s_lshr_b32 s39, s3, 2\n"
    "s_mul_i32 s39, s39, 0x24000\n"
    "s_add_u32 s45, s45, s39\n"
    "v_add_u32 v3, s45, v1\n"
    "v_lshrrev_b32 v5, 4, v0\n"
    "v_lshl_add_u32 v5, v5, 10, s45\n"
    "v_bfe_u32 v11, v0, 2, 2\n"
    "v_lshl_add_u32 v5, v11, 7, v5\n"
    "v_and_b32 v11, 3, v0\n"
    "v_lshl_add_u32 v5, v11, 3, v5\n"
    "s_mov_b32 s85, 0x400\n"
    "s_cmp_lt_u32 s2, 0xa2\n"
    "s_cselect_b32 s85, 0x9000, s85\n"
    "s_mov_b32 s86, 0x9000\n"
    "s_cmp_lt_u32 s2, 0xa2\n"
    "s_cselect_b32 s86, 0x400, s86\n"
    "s_and_b32 s87, s2, 1\n"
    "s_lshl3_add_u32 s87, s80, s87\n"
    "s_mul_i32 s87, s87, 18\n"
    "s_lshl1_add_u32 s87, s79, s87\n"
    "s_lshl_b32 s87, s87, 10\n"
    "s_add_u32 s87, s87, 0x1040000\n"
    "s_mul_i32 s39, s79, 0x48\n"
    "s_lshl2_add_u32 s39, s80, s39\n"
    "s_lshl_b32 s39, s39, 10\n"
    "s_add_u32 s39, s39, 0x1a60000\n"
    "s_cmp_lt_u32 s2, 0xa2\n"
    "s_cselect_b32 s87, s87, s39\n"
    "s_cmp_lt_u32 s2, 0xf3\n"
    "s_cselect_b32 s39, 0, 0x80000000\n"
    "v_add_u32 v6, s39, v1\n"
    "s_bfe_u32 s88, s2, 0x20003\n"
    "s_and_b32 s89, s2, 7\n"
    "s_lshr_b32 s39, s2, 5\n"
    "s_lshl3_add_u32 s89, s89, s39\n"
    "s_and_b32 s90, s3, 1\n"
    "s_lshr_b32 s91, s3, 1\n"
    "s_mul_i32 s92, s88, 9\n"
    "s_lshl1_add_u32 s92, s91, s92\n"
    "s_min_u32 s39, s91, 2\n"
    "s_add_u32 s92, s92, s39\n"
    "s_mov_b32 s93, 0x9000\n"
    "s_cmp_eq_u32 s91, 3\n"
    "s_cselect_b32 s93, 0x200, s93\n"
    "s_mov_b32 s46, 0\n"
    "s_mov_b32 s47, 0\n"
    "s_mul_i32 s39, s90, 0x144\n"
    "s_add_u32 s39, s39, s92\n"
    "s_lshl_b32 s39, s39, 10\n"
    "s_add_u32 s39, s39, 0x1f70000\n"
    "v_add_u32 v7, s39, v1\n"
    "s_mul_i32 s39, s90, 0x1200\n"
    "s_add_u32 s39, s39, 0x2012000\n"
    "v_and_b32 v8, 3, v0\n"
    "v_lshl_add_u32 v8, v8, 4, s39\n"
    "v_lshrrev_b32 v11, 4, v0\n"
    "v_lshl_add_u32 v8, v11, 7, v8\n"
    "v_add_u32 v11, 0x400, v7\n"
    "s_cmp_eq_u32 s91, 3\n"
    "s_cselect_b64 vcc, -1, 0\n"
    "v_cndmask_b32 v8, v11, v8, vcc\n"
    "s_cmp_lt_u32 s91, 2\n"
    "s_cselect_b32 s39, 0, 0x80000000\n"
    "v_add_u32 v9, s39, v7\n"
    "s_mov_b32 s28, s6\n"
    "s_and_b32 s29, s7, 0xffff\n"
    "s_mov_b32 s30, 2359296\n"
    "s_mov_b32 s31, 0x20000\n"
    "s_waitcnt vmcnt(0)\n"
    "s_barrier\n"
    "s_cmp_lg_u32 s3, 5\n"
    "s_cbranch_scc1 kb_np19_%=\n"
    "s_mov_b32 s48, 0\n"
    "buffer_store_dword v151, v149, s[16:19], 0 offen sc1\n"
    "s_sleep 12\n"
    "kb_sp20_%=:\n"
    "buffer_load_dword v151, v150, s[16:19], 0 offen sc1\n"
    "s_add_u32 s48, s48, 1\n"
    "s_waitcnt vmcnt(0)\n"
    "v_cmp_ne_u32 vcc, s60, v151\n"
    "s_cbranch_vccz kb_np19_%=\n"
    "s_cmp_lt_u32 s48, 131072\n"
    "s_cbranch_scc1 kb_sp20_%=\n"
    "kb_np19_%=:\n"
    "s_barrier\n"
    "buffer_load_dwordx4 v1, s[20:23], m0 offen sc1 lds\n"
    "s_add_u32 m0, m0, 8192\n"
    "buffer_load_dwordx4 v[112:115], v1, s[16:19], s62 offen sc1\n"
    "buffer_load_dwordx4 v[116:119], v1, s[16:19], s63 offen sc1\n"
    "buffer_load_dword v120, v148, s[16:19], s61 offen offset:1024 sc1\n"
    "buffer_load_dwordx4 v1, s[20:23], m0 offen sc1 lds\n"
    "s_add_u32 m0, m0, 8192\n"
    "s_nop 0\n"
    "buffer_load_dwordx4 v1, s[20:23], m0 offen sc1 lds\n"
    "s_add_u32 m0, m0, 8192\n"
    "s_nop 0\n"
    "buffer_load_dwordx4 v1, s[20:23], m0 offen sc1 lds\n"
    "s_add_u32 m0, m0, 8192\n"
    "s_nop 0\n"
    "buffer_load_dwordx4 v1, s[20:23], m0 offen sc1 lds\n"
    "s_add_u32 m0, m0, 8192\n"
    "s_nop 0\n"
    "buffer_load_dwordx4 v1, s[20:23], m0 offen sc1 lds\n"
    "s_add_u32 m0, m0, 8192\n"
    "s_waitcnt vmcnt(8)\n"
    "s_barrier\n"
    "ds_read_b32 v126, v48 offset:2176\n"
    "ds_read_b128 v[122:125], v1 offset:0\n"
    "s_waitcnt vmcnt(5) lgkmcnt(1)\n"
    "v_mfma_f32_32x32x4_2b_bf16 v[48:79], v[126:127], v[120:121], 0\n"
    "ds_read_b32 v126, v139 offset:2176\n"
    "s_waitcnt lgkmcnt(1)\n"
    "s_nop 15\n"
    "v_mfma_f32_32x32x16_bf16 v[48:63], v[122:125], v[112:115], v[48:63]\n"
    "v_mfma_f32_32x32x16_bf16 v[64:79], v[122:125], v[116:119], v[64:79]\n"
    "ds_read_b128 v[122:125], v137 offset:0\n"
    "s_nop 9\n"
    "v_max3_f32 v140, v48, v49, v50\n"
    "v_max3_f32 v140, v51, v52, v140\n"
    "v_max3_f32 v140, v53, v54, v140\n"
    "v_max3_f32 v140, v55, v56, v140\n"
    "v_max3_f32 v140, v57, v58, v140\n"
    "v_max3_f32 v140, v59, v60, v140\n"
    "v_max3_f32 v140, v61, v62, v140\n"
    "v_max_f32 v140, v63, v140\n"
    "v_max3_f32 v141, v64, v65, v66\n"
    "v_max3_f32 v141, v67, v68, v141\n"
    "v_max3_f32 v141, v69, v70, v141\n"
    "v_max3_f32 v141, v71, v72, v141\n"
    "v_max3_f32 v141, v73, v74, v141\n"
    "v_max3_f32 v141, v75, v76, v141\n"
    "v_max3_f32 v141, v77, v78, v141\n"
    "v_max_f32 v141, v79, v141\n"
    "s_nop 1\n"
    "v_permlane32_swap_b32 v140, v141\n"
    "v_max_f32 v140, v140, v141\n"
    "v_cvt_pk_bf16_f32 v121, -v140, 0\n"
    "s_waitcnt lgkmcnt(1)\n"
    "s_nop 0\n"
    "v_mfma_f32_32x32x4_2b_bf16 v[16:47], v[126:127], v[120:121], 0\n"
    "s_waitcnt vmcnt(0) lgkmcnt(0)\n"
    "s_barrier\n"
    "ds_read_b32 v126, v139 offset:6784\n"
    "s_mov_b64 exec, s[42:43]\n"
    "ds_read_b128 v[128:131], v138 offset:0\n"
    "s_mov_b64 exec, -1\n"
    "s_nop 11\n"
    "v_mfma_f32_32x32x16_bf16 v[16:31], v[122:125], v[112:115], v[16:31]\n"
    "v_mfma_f32_32x32x16_bf16 v[32:47], v[122:125], v[116:119], v[32:47]\n"
    "ds_read_b128 v[122:125], v137 offset:4608\n"
    "s_nop 9\n"
    "v_exp_f32 v16, v16\n"
    "v_exp_f32 v17, v17\n"
    "s_mov_b64 exec, s[42:43]\n"
    "ds_read_b128 v[132:135], v2 offset:0\n"
    "s_mov_b64 exec, -1\n"
    "s_setprio 3\n"
    "v_exp_f32 v18, v18\n"
    "v_exp_f32 v19, v19\n"
    "v_exp_f32 v20, v20\n"
    "s_waitcnt lgkmcnt(3)\n"
    "v_mfma_f32_32x32x4_2b_bf16 v[48:79], v[126:127], v[120:121], 0\n"
    "ds_read_b32 v126, v139 offset:11392\n"
    "v_exp_f32 v21, v21\n"
    "v_exp_f32 v22, v22\n"
    "v_exp_f32 v23, v23\n"
    "v_exp_f32 v24, v24\n"
    "v_exp_f32 v25, v25\n"
    "v_exp_f32 v26, v26\n"
    "v_exp_f32 v27, v27\n"
    "v_exp_f32 v28, v28\n"
    "s_waitcnt lgkmcnt(3)\n"
    "v_smfmac_f32_32x32x32_bf16 v[80:95], v[128:131], v[16:23], v136\n"
    "buffer_load_dwordx4 v1, s[20:23], m0 offen sc1 lds\n"
    "s_add_u32 m0, m0, 8192\n"
    "v_exp_f32 v29, v29\n"
    "v_exp_f32 v30, v30\n"
    "v_exp_f32 v31, v31\n"
    "v_exp_f32 v32, v32\n"
    "v_exp_f32 v33, v33\n"
    "s_waitcnt lgkmcnt(1)\n"
    "v_smfmac_f32_32x32x32_bf16 v[80:95], v[132:135], v[24:31], v136\n"
    "buffer_load_dwordx4 v1, s[20:23], m0 offen sc1 lds\n"
    "s_add_u32 m0, m0, 8192\n"
    "v_exp_f32 v34, v34\n"
    "v_exp_f32 v35, v35\n"
    "v_exp_f32 v36, v36\n"
    "v_mfma_f32_32x32x16_bf16 v[48:63], v[122:125], v[112:115], v[48:63]\n"
    "buffer_load_dwordx4 v1, s[20:23], m0 offen sc1 lds\n"
    "s_add_u32 m0, m0, 8192\n"
    "v_exp_f32 v37, v37\n"
    "v_exp_f32 v38, v38\n"
    "v_exp_f32 v39, v39\n"
    "v_exp_f32 v40, v40\n"
    "v_exp_f32 v41, v41\n"
    "v_smfmac_f32_32x32x32_bf16 v[96:111], v[128:131], v[32:39], v136\n"
    "s_mov_b64 exec, s[42:43]\n"
    "ds_read_b128 v[128:131], v138 offset:4608\n"
    "s_mov_b64 exec, -1\n"
    "v_exp_f32 v42, v42\n"
    "v_exp_f32 v43, v43\n"
    "v_exp_f32 v44, v44\n"
    "v_exp_f32 v45, v45\n"
    "v_mfma_f32_32x32x16_bf16 v[64:79], v[122:125], v[116:119], v[64:79]\n"
    "ds_read_b128 v[122:125], v137 offset:9216\n"
    "v_exp_f32 v46, v46\n"
    "v_exp_f32 v47, v47\n"
    "v_exp_f32 v48, v48\n"
    "v_exp_f32 v49, v49\n"
    "v_smfmac_f32_32x32x32_bf16 v[96:111], v[132:135], v[40:47], v136\n"
    "s_mov_b64 exec, s[42:43]\n"
    "ds_read_b128 v[132:135], v2 offset:4608\n"
    "s_mov_b64 exec, -1\n"
    "v_exp_f32 v50, v50\n"
    "v_exp_f32 v51, v51\n"
    "v_exp_f32 v52, v52\n"
    "s_waitcnt lgkmcnt(3)\n"
    "v_mfma_f32_32x32x4_2b_bf16 v[16:47], v[126:127], v[120:121], 0\n"
    "ds_read_b32 v126, v139 offset:16000\n"
    "v_exp_f32 v53, v53\n"
    "v_exp_f32 v54, v54\n"
    "v_exp_f32 v55, v55\n"
    "v_exp_f32 v56, v56\n"
    "v_exp_f32 v57, v57\n"
    "v_exp_f32 v58, v58\n"
    "v_exp_f32 v59, v59\n"
    "v_exp_f32 v60, v60\n"
    "s_waitcnt lgkmcnt(3)\n"
    "v_smfmac_f32_32x32x32_bf16 v[80:95], v[128:131], v[48:55], v136\n"
    "buffer_load_dwordx4 v1, s[20:23], m0 offen sc1 lds\n"
    "s_add_u32 m0, m0, 8192\n"
    "v_exp_f32 v61, v61\n"
    "v_exp_f32 v62, v62\n"
    "v_exp_f32 v63, v63\n"
    "v_exp_f32 v64, v64\n"
    "v_exp_f32 v65, v65\n"
    "s_waitcnt lgkmcnt(1)\n"
    "v_smfmac_f32_32x32x32_bf16 v[80:95], v[132:135], v[56:63], v136\n"
    "buffer_load_dwordx4 v1, s[20:23], m0 offen sc1 lds\n"
    "s_add_u32 m0, m0, 8192\n"
    "v_exp_f32 v66, v66\n"
    "v_exp_f32 v67, v67\n"
    "v_exp_f32 v68, v68\n"
    "v_mfma_f32_32x32x16_bf16 v[16:31], v[122:125], v[112:115], v[16:31]\n"
    "buffer_load_dwordx4 v1, s[20:23], m0 offen sc1 lds\n"
    "s_add_u32 m0, m0, 8192\n"
    "buffer_store_dwordx4 v[240:243], v1, s[16:19], s99 offen offset:0 sc1\n"
    "v_exp_f32 v69, v69\n"
    "v_exp_f32 v70, v70\n"
    "v_exp_f32 v71, v71\n"
    "v_exp_f32 v72, v72\n"
    "v_exp_f32 v73, v73\n"
    "v_smfmac_f32_32x32x32_bf16 v[96:111], v[128:131], v[64:71], v136\n"
    "s_mov_b64 exec, s[42:43]\n"
    "ds_read_b128 v[128:131], v138 offset:9216\n"
    "s_mov_b64 exec, -1\n"
    "v_exp_f32 v74, v74\n"
    "v_exp_f32 v75, v75\n"
    "v_exp_f32 v76, v76\n"
    "v_exp_f32 v77, v77\n"
    "v_mfma_f32_32x32x16_bf16 v[32:47], v[122:125], v[116:119], v[32:47]\n"
    "ds_read_b128 v[122:125], v137 offset:13824\n"
    "v_exp_f32 v78, v78\n"
    "v_exp_f32 v79, v79\n"
    "v_exp_f32 v16, v16\n"
    "v_exp_f32 v17, v17\n"
    "v_smfmac_f32_32x32x32_bf16 v[96:111], v[132:135], v[72:79], v136\n"
    "s_mov_b64 exec, s[42:43]\n"
    "ds_read_b128 v[132:135], v2 offset:9216\n"
    "s_mov_b64 exec, -1\n"
    "v_exp_f32 v18, v18\n"
    "v_exp_f32 v19, v19\n"
    "v_exp_f32 v20, v20\n"
    "s_waitcnt lgkmcnt(3)\n"
    "v_mfma_f32_32x32x4_2b_bf16 v[48:79], v[126:127], v[120:121], 0\n"
    "ds_read_b32 v126, v139 offset:20608\n"
    "v_exp_f32 v21, v21\n"
    "v_exp_f32 v22, v22\n"
    "v_exp_f32 v23, v23\n"
    "v_exp_f32 v24, v24\n"
    "v_exp_f32 v25, v25\n"
    "v_exp_f32 v26, v26\n"
    "v_exp_f32 v27, v27\n"
    "v_exp_f32 v28, v28\n"
    "s_waitcnt lgkmcnt(3)\n"
    "v_smfmac_f32_32x32x32_bf16 v[80:95], v[128:131], v[16:23], v136\n"
    "buffer_load_dwordx4 v1, s[20:23], m0 offen sc1 lds\n"
    "s_add_u32 m0, m0, 8192\n"
    "v_exp_f32 v29, v29\n"
    "v_exp_f32 v30, v30\n"
    "v_exp_f32 v31, v31\n"
    "v_exp_f32 v32, v32\n"
    "v_exp_f32 v33, v33\n"
    "s_waitcnt lgkmcnt(1)\n"
    "v_smfmac_f32_32x32x32_bf16 v[80:95], v[132:135], v[24:31], v136\n"
    "buffer_load_dwordx4 v1, s[20:23], m0 offen sc1 lds\n"
    "s_add_u32 m0, m0, 8192\n"
    "v_exp_f32 v34, v34\n"
    "v_exp_f32 v35, v35\n"
    "v_exp_f32 v36, v36\n"
    "v_mfma_f32_32x32x16_bf16 v[48:63], v[122:125], v[112:115], v[48:63]\n"
    "buffer_load_dwordx4 v1, s[20:23], m0 offen sc1 lds\n"
    "s_add_u32 m0, m0, 8192\n"
    "buffer_store_dwordx4 v[240:243], v1, s[16:19], s99 offen offset:1024 sc1\n"
    "v_exp_f32 v37, v37\n"
    "v_exp_f32 v38, v38\n"
    "v_exp_f32 v39, v39\n"
    "v_exp_f32 v40, v40\n"
    "v_exp_f32 v41, v41\n"
    "v_smfmac_f32_32x32x32_bf16 v[96:111], v[128:131], v[32:39], v136\n"
    "s_mov_b64 exec, s[42:43]\n"
    "ds_read_b128 v[128:131], v138 offset:13824\n"
    "s_mov_b64 exec, -1\n"
    "v_exp_f32 v42, v42\n"
    "v_exp_f32 v43, v43\n"
    "v_exp_f32 v44, v44\n"
    "v_exp_f32 v45, v45\n"
    "v_mfma_f32_32x32x16_bf16 v[64:79], v[122:125], v[116:119], v[64:79]\n"
    "ds_read_b128 v[122:125], v137 offset:18432\n"
    "v_exp_f32 v46, v46\n"
    "v_exp_f32 v47, v47\n"
    "v_exp_f32 v48, v48\n"
    "v_exp_f32 v49, v49\n"
    "v_smfmac_f32_32x32x32_bf16 v[96:111], v[132:135], v[40:47], v136\n"
    "s_mov_b64 exec, s[42:43]\n"
    "ds_read_b128 v[132:135], v2 offset:13824\n"
    "s_mov_b64 exec, -1\n"
    "s_setprio 2\n"
    "v_exp_f32 v50, v50\n"
    "v_exp_f32 v51, v51\n"
    "v_exp_f32 v52, v52\n"
    "s_waitcnt lgkmcnt(3)\n"
    "v_mfma_f32_32x32x4_2b_bf16 v[16:47], v[126:127], v[120:121], 0\n"
    "ds_read_b32 v126, v139 offset:25216\n"
    "v_exp_f32 v53, v53\n"
    "v_exp_f32 v54, v54\n"
    "v_exp_f32 v55, v55\n"
    "v_exp_f32 v56, v56\n"
    "v_exp_f32 v57, v57\n"
    "v_exp_f32 v58, v58\n"
    "v_exp_f32 v59, v59\n"
    "v_exp_f32 v60, v60\n"
    "s_waitcnt lgkmcnt(3)\n"
    "v_smfmac_f32_32x32x32_bf16 v[80:95], v[128:131], v[48:55], v136\n"
    "buffer_load_dwordx4 v1, s[20:23], m0 offen sc1 lds\n"
    "s_add_u32 m0, m0, 8192\n"
    "v_exp_f32 v61, v61\n"
    "v_exp_f32 v62, v62\n"
    "v_exp_f32 v63, v63\n"
    "v_exp_f32 v64, v64\n"
    "v_exp_f32 v65, v65\n"
    "s_waitcnt lgkmcnt(1)\n"
    "v_smfmac_f32_32x32x32_bf16 v[80:95], v[132:135], v[56:63], v136\n"
    "buffer_load_dwordx4 v1, s[20:23], m0 offen sc1 lds\n"
    "s_add_u32 m0, m0, 8192\n"
    "v_exp_f32 v66, v66\n"
    "v_exp_f32 v67, v67\n"
    "v_exp_f32 v68, v68\n"
    "v_mfma_f32_32x32x16_bf16 v[16:31], v[122:125], v[112:115], v[16:31]\n"
    "buffer_load_dwordx4 v1, s[20:23], m0 offen sc1 lds\n"
    "s_add_u32 m0, m0, 8192\n"
    "s_mov_b64 exec, 0xffff\n"
    "buffer_store_dwordx4 v[240:243], v1, s[16:19], s99 offen offset:2048 sc1\n"
    "s_mov_b64 exec, -1\n"
    "v_exp_f32 v69, v69\n"
    "v_exp_f32 v70, v70\n"
    "v_exp_f32 v71, v71\n"
    "v_exp_f32 v72, v72\n"
    "v_exp_f32 v73, v73\n"
    "v_smfmac_f32_32x32x32_bf16 v[96:111], v[128:131], v[64:71], v136\n"
    "s_mov_b64 exec, s[42:43]\n"
    "ds_read_b128 v[128:131], v138 offset:18432\n"
    "s_mov_b64 exec, -1\n"
    "v_exp_f32 v74, v74\n"
    "v_exp_f32 v75, v75\n"
    "v_exp_f32 v76, v76\n"
    "v_exp_f32 v77, v77\n"
    "v_mfma_f32_32x32x16_bf16 v[32:47], v[122:125], v[116:119], v[32:47]\n"
    "ds_read_b128 v[122:125], v137 offset:23040\n"
    "v_exp_f32 v78, v78\n"
    "v_exp_f32 v79, v79\n"
    "v_exp_f32 v16, v16\n"
    "v_exp_f32 v17, v17\n"
    "v_smfmac_f32_32x32x32_bf16 v[96:111], v[132:135], v[72:79], v136\n"
    "s_mov_b64 exec, s[42:43]\n"
    "ds_read_b128 v[132:135], v2 offset:18432\n"
    "s_mov_b64 exec, -1\n"
    "s_setprio 3\n"
    "v_exp_f32 v18, v18\n"
    "v_exp_f32 v19, v19\n"
    "v_exp_f32 v20, v20\n"
    "s_waitcnt lgkmcnt(3)\n"
    "v_mfma_f32_32x32x4_2b_bf16 v[48:79], v[126:127], v[120:121], 0\n"
    "ds_read_b32 v126, v139 offset:29824\n"
    "v_exp_f32 v21, v21\n"
    "v_exp_f32 v22, v22\n"
    "v_exp_f32 v23, v23\n"
    "v_exp_f32 v24, v24\n"
    "v_exp_f32 v25, v25\n"
    "v_exp_f32 v26, v26\n"
    "v_exp_f32 v27, v27\n"
    "v_exp_f32 v28, v28\n"
    "s_waitcnt lgkmcnt(3)\n"
    "v_smfmac_f32_32x32x32_bf16 v[80:95], v[128:131], v[16:23], v136\n"
    "buffer_load_dwordx4 v[208:211], v4, s[24:27], 0 offen nt\n"
    "v_exp_f32 v29, v29\n"
    "v_exp_f32 v30, v30\n"
    "v_exp_f32 v31, v31\n"
    "v_exp_f32 v32, v32\n"
    "v_exp_f32 v33, v33\n"
    "s_waitcnt lgkmcnt(1)\n"
    "v_smfmac_f32_32x32x32_bf16 v[80:95], v[132:135], v[24:31], v136\n"
    "v_exp_f32 v34, v34\n"
    "v_exp_f32 v35, v35\n"
    "v_exp_f32 v36, v36\n"
    "v_mfma_f32_32x32x16_bf16 v[48:63], v[122:125], v[112:115], v[48:63]\n"
    "v_exp_f32 v37, v37\n"
    "v_exp_f32 v38, v38\n"
    "v_exp_f32 v39, v39\n"
    "v_exp_f32 v40, v40\n"
    "v_exp_f32 v41, v41\n"
    "v_smfmac_f32_32x32x32_bf16 v[96:111], v[128:131], v[32:39], v136\n"
    "s_mov_b64 exec, s[42:43]\n"
    "ds_read_b128 v[128:131], v138 offset:23040\n"
    "s_mov_b64 exec, -1\n"
    "v_exp_f32 v42, v42\n"
    "v_exp_f32 v43, v43\n"
    "v_exp_f32 v44, v44\n"
    "v_exp_f32 v45, v45\n"
    "v_mfma_f32_32x32x16_bf16 v[64:79], v[122:125], v[116:119], v[64:79]\n"
    "ds_read_b128 v[122:125], v137 offset:27648\n"
    "v_exp_f32 v46, v46\n"
    "v_exp_f32 v47, v47\n"
    "v_exp_f32 v48, v48\n"
    "v_exp_f32 v49, v49\n"
    "v_smfmac_f32_32x32x32_bf16 v[96:111], v[132:135], v[40:47], v136\n"
    "s_mov_b64 exec, s[42:43]\n"
    "ds_read_b128 v[132:135], v2 offset:23040\n"
    "s_mov_b64 exec, -1\n"
    "s_setprio 1\n"
    "v_exp_f32 v50, v50\n"
    "v_exp_f32 v51, v51\n"
    "v_exp_f32 v52, v52\n"
    "s_waitcnt lgkmcnt(3)\n"
    "v_mfma_f32_32x32x4_2b_bf16 v[16:47], v[126:127], v[120:121], 0\n"
    "ds_read_b32 v126, v139 offset:34432\n"
    "v_exp_f32 v53, v53\n"
    "v_exp_f32 v54, v54\n"
    "v_exp_f32 v55, v55\n"
    "v_exp_f32 v56, v56\n"
    "v_exp_f32 v57, v57\n"
    "v_exp_f32 v58, v58\n"
    "v_exp_f32 v59, v59\n"
    "v_exp_f32 v60, v60\n"
    "s_waitcnt lgkmcnt(3)\n"
    "v_smfmac_f32_32x32x32_bf16 v[80:95], v[128:131], v[48:55], v136\n"
    "buffer_load_dwordx4 v[212:215], v4, s[24:27], s84 offen nt\n"
    "v_exp_f32 v61, v61\n"
    "v_exp_f32 v62, v62\n"
    "v_exp_f32 v63, v63\n"
    "v_exp_f32 v64, v64\n"
    "v_exp_f32 v65, v65\n"
    "s_waitcnt lgkmcnt(1)\n"
    "v_smfmac_f32_32x32x32_bf16 v[80:95], v[132:135], v[56:63], v136\n"
    "v_exp_f32 v66, v66\n"
    "v_exp_f32 v67, v67\n"
    "v_exp_f32 v68, v68\n"
    "v_mfma_f32_32x32x16_bf16 v[16:31], v[122:125], v[112:115], v[16:31]\n"
    "v_exp_f32 v69, v69\n"
    "v_exp_f32 v70, v70\n"
    "v_exp_f32 v71, v71\n"
    "v_exp_f32 v72, v72\n"
    "v_exp_f32 v73, v73\n"
    "v_smfmac_f32_32x32x32_bf16 v[96:111], v[128:131], v[64:71], v136\n"
    "s_mov_b64 exec, s[42:43]\n"
    "ds_read_b128 v[128:131], v138 offset:27648\n"
    "s_mov_b64 exec, -1\n"
    "v_exp_f32 v74, v74\n"
    "v_exp_f32 v75, v75\n"
    "v_exp_f32 v76, v76\n"
    "v_exp_f32 v77, v77\n"
    "v_mfma_f32_32x32x16_bf16 v[32:47], v[122:125], v[116:119], v[32:47]\n"
    "ds_read_b128 v[122:125], v137 offset:32256\n"
    "v_exp_f32 v78, v78\n"
    "v_exp_f32 v79, v79\n"
    "v_exp_f32 v16, v16\n"
    "v_exp_f32 v17, v17\n"
    "v_smfmac_f32_32x32x32_bf16 v[96:111], v[132:135], v[72:79], v136\n"
    "s_mov_b64 exec, s[42:43]\n"
    "ds_read_b128 v[132:135], v2 offset:27648\n"
    "s_mov_b64 exec, -1\n"
    "v_exp_f32 v18, v18\n"
    "v_exp_f32 v19, v19\n"
    "v_exp_f32 v20, v20\n"
    "s_waitcnt lgkmcnt(3)\n"
    "v_mfma_f32_32x32x4_2b_bf16 v[48:79], v[126:127], v[120:121], 0\n"
    "ds_read_b32 v126, v139 offset:39040\n"
    "v_exp_f32 v21, v21\n"
    "v_exp_f32 v22, v22\n"
    "v_exp_f32 v23, v23\n"
    "v_exp_f32 v24, v24\n"
    "v_exp_f32 v25, v25\n"
    "v_exp_f32 v26, v26\n"
    "v_exp_f32 v27, v27\n"
    "v_exp_f32 v28, v28\n"
    "s_waitcnt lgkmcnt(3)\n"
    "v_smfmac_f32_32x32x32_bf16 v[80:95], v[128:131], v[16:23], v136\n"
    "buffer_load_dwordx4 v[216:219], v4, s[24:27], s83 offen nt\n"
    "v_exp_f32 v29, v29\n"
    "v_exp_f32 v30, v30\n"
    "v_exp_f32 v31, v31\n"
    "v_exp_f32 v32, v32\n"
    "v_exp_f32 v33, v33\n"
    "s_waitcnt lgkmcnt(1)\n"
    "v_smfmac_f32_32x32x32_bf16 v[80:95], v[132:135], v[24:31], v136\n"
    "v_exp_f32 v34, v34\n"
    "v_exp_f32 v35, v35\n"
    "v_exp_f32 v36, v36\n"
    "v_mfma_f32_32x32x16_bf16 v[48:63], v[122:125], v[112:115], v[48:63]\n"
    "v_exp_f32 v37, v37\n"
    "v_exp_f32 v38, v38\n"
    "v_exp_f32 v39, v39\n"
    "v_exp_f32 v40, v40\n"
    "v_exp_f32 v41, v41\n"
    "v_smfmac_f32_32x32x32_bf16 v[96:111], v[128:131], v[32:39], v136\n"
    "s_mov_b64 exec, s[42:43]\n"
    "ds_read_b128 v[128:131], v138 offset:32256\n"
    "s_mov_b64 exec, -1\n"
    "v_exp_f32 v42, v42\n"
    "v_exp_f32 v43, v43\n"
    "v_exp_f32 v44, v44\n"
    "v_exp_f32 v45, v45\n"
    "v_mfma_f32_32x32x16_bf16 v[64:79], v[122:125], v[116:119], v[64:79]\n"
    "ds_read_b128 v[122:125], v137 offset:36864\n"
    "v_exp_f32 v46, v46\n"
    "v_exp_f32 v47, v47\n"
    "v_exp_f32 v48, v48\n"
    "v_exp_f32 v49, v49\n"
    "v_smfmac_f32_32x32x32_bf16 v[96:111], v[132:135], v[40:47], v136\n"
    "s_mov_b64 exec, s[42:43]\n"
    "ds_read_b128 v[132:135], v2 offset:32256\n"
    "s_mov_b64 exec, -1\n"
    "s_setprio 0\n"
    "v_exp_f32 v50, v50\n"
    "v_exp_f32 v51, v51\n"
    "v_exp_f32 v52, v52\n"
    "s_waitcnt lgkmcnt(3)\n"
    "v_mfma_f32_32x32x4_2b_bf16 v[16:47], v[126:127], v[120:121], 0\n"
    "ds_read_b32 v126, v139 offset:43648\n"
    "v_exp_f32 v53, v53\n"
    "v_exp_f32 v54, v54\n"
    "v_exp_f32 v55, v55\n"
    "v_exp_f32 v56, v56\n"
    "v_exp_f32 v57, v57\n"
    "v_exp_f32 v58, v58\n"
    "v_exp_f32 v59, v59\n"
    "v_exp_f32 v60, v60\n"
    "s_waitcnt lgkmcnt(3)\n"
    "v_smfmac_f32_32x32x32_bf16 v[80:95], v[128:131], v[48:55], v136\n"
    "s_add_u32 s45, s83, s84\n"
    "buffer_load_dwordx4 v[220:223], v4, s[24:27], s45 offen nt\n"
    "v_exp_f32 v61, v61\n"
    "v_exp_f32 v62, v62\n"
    "v_exp_f32 v63, v63\n"
    "v_exp_f32 v64, v64\n"
    "v_exp_f32 v65, v65\n"
    "s_waitcnt lgkmcnt(1)\n"
    "v_smfmac_f32_32x32x32_bf16 v[80:95], v[132:135], v[56:63], v136\n"
    "v_exp_f32 v66, v66\n"
    "v_exp_f32 v67, v67\n"
    "v_exp_f32 v68, v68\n"
    "v_mfma_f32_32x32x16_bf16 v[16:31], v[122:125], v[112:115], v[16:31]\n"
    "v_exp_f32 v69, v69\n"
    "v_exp_f32 v70, v70\n"
    "v_exp_f32 v71, v71\n"
    "v_exp_f32 v72, v72\n"
    "v_exp_f32 v73, v73\n"
    "v_smfmac_f32_32x32x32_bf16 v[96:111], v[128:131], v[64:71], v136\n"
    "s_mov_b64 exec, s[42:43]\n"
    "ds_read_b128 v[128:131], v138 offset:36864\n"
    "s_mov_b64 exec, -1\n"
    "v_exp_f32 v74, v74\n"
    "v_exp_f32 v75, v75\n"
    "v_exp_f32 v76, v76\n"
    "v_exp_f32 v77, v77\n"
    "v_mfma_f32_32x32x16_bf16 v[32:47], v[122:125], v[116:119], v[32:47]\n"
    "ds_read_b128 v[122:125], v137 offset:41472\n"
    "v_exp_f32 v78, v78\n"
    "v_exp_f32 v79, v79\n"
    "s_waitcnt vmcnt(5)\n"
    "s_barrier\n"
    "v_exp_f32 v16, v16\n"
    "v_exp_f32 v17, v17\n"
    "v_smfmac_f32_32x32x32_bf16 v[96:111], v[132:135], v[72:79], v136\n"
    "s_mov_b64 exec, s[42:43]\n"
    "ds_read_b128 v[132:135], v2 offset:36864\n"
    "s_mov_b64 exec, -1\n"
    "s_setprio 3\n"
    "v_exp_f32 v18, v18\n"
    "v_exp_f32 v19, v19\n"
    "v_exp_f32 v20, v20\n"
    "s_waitcnt lgkmcnt(3)\n"
    "v_mfma_f32_32x32x4_2b_bf16 v[48:79], v[126:127], v[120:121], 0\n"
    "ds_read_b32 v126, v139 offset:48256\n"
    "v_exp_f32 v21, v21\n"
    "v_exp_f32 v22, v22\n"
    "v_exp_f32 v23, v23\n"
    "v_exp_f32 v24, v24\n"
    "v_exp_f32 v25, v25\n"
    "v_exp_f32 v26, v26\n"
    "v_exp_f32 v27, v27\n"
    "v_exp_f32 v28, v28\n"
    "s_waitcnt lgkmcnt(3)\n"
    "v_smfmac_f32_32x32x32_bf16 v[80:95], v[128:131], v[16:23], v136\n"
    "buffer_load_dwordx4 v[224:227], v4, s[24:27], s82 offen nt\n"
    "v_exp_f32 v29, v29\n"
    "v_exp_f32 v30, v30\n"
    "v_exp_f32 v31, v31\n"
    "v_exp_f32 v32, v32\n"
    "v_exp_f32 v33, v33\n"
    "s_waitcnt lgkmcnt(1)\n"
    "v_smfmac_f32_32x32x32_bf16 v[80:95], v[132:135], v[24:31], v136\n"
    "v_exp_f32 v34, v34\n"
    "v_exp_f32 v35, v35\n"
    "v_exp_f32 v36, v36\n"
    "v_mfma_f32_32x32x16_bf16 v[48:63], v[122:125], v[112:115], v[48:63]\n"
    "v_exp_f32 v37, v37\n"
    "v_exp_f32 v38, v38\n"
    "v_exp_f32 v39, v39\n"
    "v_exp_f32 v40, v40\n"
    "v_exp_f32 v41, v41\n"
    "v_smfmac_f32_32x32x32_bf16 v[96:111], v[128:131], v[32:39], v136\n"
    "s_mov_b64 exec, s[42:43]\n"
    "ds_read_b128 v[128:131], v138 offset:41472\n"
    "s_mov_b64 exec, -1\n"
    "v_exp_f32 v42, v42\n"
    "v_exp_f32 v43, v43\n"
    "v_exp_f32 v44, v44\n"
    "v_exp_f32 v45, v45\n"
    "v_mfma_f32_32x32x16_bf16 v[64:79], v[122:125], v[116:119], v[64:79]\n"
    "ds_read_b128 v[122:125], v137 offset:46080\n"
    "v_exp_f32 v46, v46\n"
    "v_exp_f32 v47, v47\n"
    "v_exp_f32 v48, v48\n"
    "v_exp_f32 v49, v49\n"
    "v_smfmac_f32_32x32x32_bf16 v[96:111], v[132:135], v[40:47], v136\n"
    "s_mov_b64 exec, s[42:43]\n"
    "ds_read_b128 v[132:135], v2 offset:41472\n"
    "s_mov_b64 exec, -1\n"
    "v_exp_f32 v50, v50\n"
    "v_exp_f32 v51, v51\n"
    "v_exp_f32 v52, v52\n"
    "s_waitcnt lgkmcnt(3)\n"
    "v_mfma_f32_32x32x4_2b_bf16 v[16:47], v[126:127], v[120:121], 0\n"
    "ds_read_b32 v126, v139 offset:52864\n"
    "v_exp_f32 v53, v53\n"
    "v_exp_f32 v54, v54\n"
    "v_exp_f32 v55, v55\n"
    "v_exp_f32 v56, v56\n"
    "v_exp_f32 v57, v57\n"
    "v_exp_f32 v58, v58\n"
    "v_exp_f32 v59, v59\n"
    "v_exp_f32 v60, v60\n"
    "s_waitcnt lgkmcnt(3)\n"
    "v_smfmac_f32_32x32x32_bf16 v[80:95], v[128:131], v[48:55], v136\n"
    "s_waitcnt vmcnt(5)\n"
    "v_mov_b32 v147, s60\n"
    "s_mov_b32 s45, 8192\n"
    "buffer_store_dword v147, v13, s[16:19], s45 offen sc1\n"
    "v_exp_f32 v61, v61\n"
    "v_exp_f32 v62, v62\n"
    "v_exp_f32 v63, v63\n"
    "v_exp_f32 v64, v64\n"
    "v_exp_f32 v65, v65\n"
    "s_waitcnt lgkmcnt(1)\n"
    "v_smfmac_f32_32x32x32_bf16 v[80:95], v[132:135], v[56:63], v136\n"
    "s_add_u32 s45, s82, s84\n"
    "buffer_load_dwordx4 v[228:231], v4, s[24:27], s45 offen nt\n"
    "v_exp_f32 v66, v66\n"
    "v_exp_f32 v67, v67\n"
    "v_exp_f32 v68, v68\n"
    "v_mfma_f32_32x32x16_bf16 v[16:31], v[122:125], v[112:115], v[16:31]\n"
    "v_exp_f32 v69, v69\n"
    "v_exp_f32 v70, v70\n"
    "v_exp_f32 v71, v71\n"
    "v_exp_f32 v72, v72\n"
    "v_exp_f32 v73, v73\n"
    "v_smfmac_f32_32x32x32_bf16 v[96:111], v[128:131], v[64:71], v136\n"
    "s_mov_b64 exec, s[42:43]\n"
    "ds_read_b128 v[128:131], v138 offset:46080\n"
    "s_mov_b64 exec, -1\n"
    "v_exp_f32 v74, v74\n"
    "v_exp_f32 v75, v75\n"
    "v_exp_f32 v76, v76\n"
    "v_exp_f32 v77, v77\n"
    "v_mfma_f32_32x32x16_bf16 v[32:47], v[122:125], v[116:119], v[32:47]\n"
    "ds_read_b128 v[122:125], v137 offset:50688\n"
    "v_exp_f32 v78, v78\n"
    "v_exp_f32 v79, v79\n"
    "v_exp_f32 v16, v16\n"
    "v_exp_f32 v17, v17\n"
    "v_smfmac_f32_32x32x32_bf16 v[96:111], v[132:135], v[72:79], v136\n"
    "s_mov_b64 exec, s[42:43]\n"
    "ds_read_b128 v[132:135], v2 offset:46080\n"
    "s_mov_b64 exec, -1\n"
    "v_exp_f32 v18, v18\n"
    "v_exp_f32 v19, v19\n"
    "v_exp_f32 v20, v20\n"
    "s_waitcnt lgkmcnt(3)\n"
    "v_mfma_f32_32x32x4_2b_bf16 v[48:79], v[126:127], v[120:121], 0\n"
    "ds_read_b32 v126, v139 offset:57472\n"
    "v_exp_f32 v21, v21\n"
    "v_exp_f32 v22, v22\n"
    "v_exp_f32 v23, v23\n"
    "v_exp_f32 v24, v24\n"
    "v_exp_f32 v25, v25\n"
    "v_exp_f32 v26, v26\n"
    "v_exp_f32 v27, v27\n"
    "v_exp_f32 v28, v28\n"
    "s_waitcnt lgkmcnt(3)\n"
    "v_smfmac_f32_32x32x32_bf16 v[80:95], v[128:131], v[16:23], v136\n"
    "s_add_u32 s45, s82, s83\n"
    "buffer_load_dwordx4 v[232:235], v4, s[24:27], s45 offen nt\n"
    "v_exp_f32 v29, v29\n"
    "v_exp_f32 v30, v30\n"
    "v_exp_f32 v31, v31\n"
    "v_exp_f32 v32, v32\n"
    "v_exp_f32 v33, v33\n"
    "s_waitcnt lgkmcnt(1)\n"
    "v_smfmac_f32_32x32x32_bf16 v[80:95], v[132:135], v[24:31], v136\n"
    "v_exp_f32 v34, v34\n"
    "v_exp_f32 v35, v35\n"
    "v_exp_f32 v36, v36\n"
    "v_mfma_f32_32x32x16_bf16 v[48:63], v[122:125], v[112:115], v[48:63]\n"
    "v_exp_f32 v37, v37\n"
    "v_exp_f32 v38, v38\n"
    "v_exp_f32 v39, v39\n"
    "v_exp_f32 v40, v40\n"
    "v_exp_f32 v41, v41\n"
    "v_smfmac_f32_32x32x32_bf16 v[96:111], v[128:131], v[32:39], v136\n"
    "s_mov_b64 exec, s[42:43]\n"
    "ds_read_b128 v[128:131], v138 offset:50688\n"
    "s_mov_b64 exec, -1\n"
    "v_exp_f32 v42, v42\n"
    "v_exp_f32 v43, v43\n"
    "v_exp_f32 v44, v44\n"
    "v_exp_f32 v45, v45\n"
    "v_mfma_f32_32x32x16_bf16 v[64:79], v[122:125], v[116:119], v[64:79]\n"
    "ds_read_b128 v[122:125], v137 offset:55296\n"
    "v_exp_f32 v46, v46\n"
    "v_exp_f32 v47, v47\n"
    "v_exp_f32 v48, v48\n"
    "v_exp_f32 v49, v49\n"
    "v_smfmac_f32_32x32x32_bf16 v[96:111], v[132:135], v[40:47], v136\n"
    "s_mov_b64 exec, s[42:43]\n"
    "ds_read_b128 v[132:135], v2 offset:50688\n"
    "s_mov_b64 exec, -1\n"
    "s_setprio 2\n"
    "v_exp_f32 v50, v50\n"
    "v_exp_f32 v51, v51\n"
    "v_exp_f32 v52, v52\n"
    "s_waitcnt lgkmcnt(3)\n"
    "v_mfma_f32_32x32x4_2b_bf16 v[16:47], v[126:127], v[120:121], 0\n"
    "ds_read_b32 v126, v139 offset:62080\n"
    "v_exp_f32 v53, v53\n"
    "v_exp_f32 v54, v54\n"
    "v_exp_f32 v55, v55\n"
    "v_exp_f32 v56, v56\n"
    "v_exp_f32 v57, v57\n"
    "v_exp_f32 v58, v58\n"
    "v_exp_f32 v59, v59\n"
    "v_exp_f32 v60, v60\n"
    "s_waitcnt lgkmcnt(3)\n"
    "v_smfmac_f32_32x32x32_bf16 v[80:95], v[128:131], v[48:55], v136\n"
    "s_add_u32 s45, s82, s83\n"
    "s_add_u32 s45, s45, s84\n"
    "buffer_load_dwordx4 v[236:239], v4, s[24:27], s45 offen nt\n"
    "v_exp_f32 v61, v61\n"
    "v_exp_f32 v62, v62\n"
    "v_exp_f32 v63, v63\n"
    "v_exp_f32 v64, v64\n"
    "v_exp_f32 v65, v65\n"
    "s_waitcnt lgkmcnt(1)\n"
    "v_smfmac_f32_32x32x32_bf16 v[80:95], v[132:135], v[56:63], v136\n"
    "v_exp_f32 v66, v66\n"
    "v_exp_f32 v67, v67\n"
    "v_exp_f32 v68, v68\n"
    "v_mfma_f32_32x32x16_bf16 v[16:31], v[122:125], v[112:115], v[16:31]\n"
    "v_exp_f32 v69, v69\n"
    "v_exp_f32 v70, v70\n"
    "v_exp_f32 v71, v71\n"
    "v_exp_f32 v72, v72\n"
    "v_exp_f32 v73, v73\n"
    "v_smfmac_f32_32x32x32_bf16 v[96:111], v[128:131], v[64:71], v136\n"
    "s_mov_b64 exec, s[42:43]\n"
    "ds_read_b128 v[128:131], v138 offset:55296\n"
    "s_mov_b64 exec, -1\n"
    "v_exp_f32 v74, v74\n"
    "v_exp_f32 v75, v75\n"
    "v_exp_f32 v76, v76\n"
    "v_exp_f32 v77, v77\n"
    "v_mfma_f32_32x32x16_bf16 v[32:47], v[122:125], v[116:119], v[32:47]\n"
    "ds_read_b128 v[122:125], v137 offset:59904\n"
    "v_exp_f32 v78, v78\n"
    "v_exp_f32 v79, v79\n"
    "v_add_u32 v137, 55296, v137\n"
    "v_add_u32 v138, 55296, v138\n"
    "v_add_u32 v2, 55296, v2\n"
    "v_add_u32 v139, 55296, v139\n"
    "v_exp_f32 v16, v16\n"
    "v_exp_f32 v17, v17\n"
    "v_smfmac_f32_32x32x32_bf16 v[96:111], v[132:135], v[72:79], v136\n"
    "s_mov_b64 exec, s[42:43]\n"
    "ds_read_b128 v[132:135], v2 offset:0\n"
    "s_mov_b64 exec, -1\n"
    "s_setprio 3\n"
    "v_exp_f32 v18, v18\n"
    "v_exp_f32 v19, v19\n"
    "v_exp_f32 v20, v20\n"
    "s_waitcnt lgkmcnt(3)\n"
    "v_mfma_f32_32x32x4_2b_bf16 v[48:79], v[126:127], v[120:121], 0\n"
    "ds_read_b32 v126, v139 offset:11392\n"
    "v_exp_f32 v21, v21\n"
    "v_exp_f32 v22, v22\n"
    "v_exp_f32 v23, v23\n"
    "v_exp_f32 v24, v24\n"
    "v_exp_f32 v25, v25\n"
    "v_exp_f32 v26, v26\n"
    "v_exp_f32 v27, v27\n"
    "v_exp_f32 v28, v28\n"
    "s_waitcnt lgkmcnt(3)\n"
    "v_smfmac_f32_32x32x32_bf16 v[80:95], v[128:131], v[16:23], v136\n"
    "s_mov_b32 s45, 212992\n"
    "buffer_load_dwordx4 v[140:143], v1, s[16:19], s45 offen sc1\n"
    "v_exp_f32 v29, v29\n"
    "v_exp_f32 v30, v30\n"
    "v_exp_f32 v31, v31\n"
    "v_exp_f32 v32, v32\n"
    "v_exp_f32 v33, v33\n"
    "s_waitcnt lgkmcnt(1)\n"
    "v_smfmac_f32_32x32x32_bf16 v[80:95], v[132:135], v[24:31], v136\n"
    "s_waitcnt vmcnt(8)\n"
    "ds_write_b128 v3, v[208:211] offset:0\n"
    "ds_write_b128 v3, v[212:215] offset:1024\n"
    "v_exp_f32 v34, v34\n"
    "v_exp_f32 v35, v35\n"
    "v_exp_f32 v36, v36\n"
    "v_mfma_f32_32x32x16_bf16 v[48:63], v[122:125], v[112:115], v[48:63]\n"
    "s_waitcnt vmcnt(6)\n"
    "ds_write_b128 v3, v[216:219] offset:2048\n"
    "ds_write_b128 v3, v[220:223] offset:3072\n"
    "v_exp_f32 v37, v37\n"
    "v_exp_f32 v38, v38\n"
    "v_exp_f32 v39, v39\n"
    "v_exp_f32 v40, v40\n"
    "v_exp_f32 v41, v41\n"
    "v_smfmac_f32_32x32x32_bf16 v[96:111], v[128:131], v[32:39], v136\n"
    "s_mov_b64 exec, s[42:43]\n"
    "ds_read_b128 v[128:131], v138 offset:4608\n"
    "s_mov_b64 exec, -1\n"
    "v_exp_f32 v42, v42\n"
    "v_exp_f32 v43, v43\n"
    "v_exp_f32 v44, v44\n"
    "v_exp_f32 v45, v45\n"
    "v_mfma_f32_32x32x16_bf16 v[64:79], v[122:125], v[116:119], v[64:79]\n"
    "ds_read_b128 v[122:125], v137 offset:9216\n"
    "v_exp_f32 v46, v46\n"
    "v_exp_f32 v47, v47\n"
    "v_exp_f32 v48, v48\n"
    "v_exp_f32 v49, v49\n"
    "v_smfmac_f32_32x32x32_bf16 v[96:111], v[132:135], v[40:47], v136\n"
    "s_mov_b64 exec, s[42:43]\n"
    "ds_read_b128 v[132:135], v2 offset:4608\n"
    "s_mov_b64 exec, -1\n"
    "v_exp_f32 v50, v50\n"
    "v_exp_f32 v51, v51\n"
    "v_exp_f32 v52, v52\n"
    "s_waitcnt lgkmcnt(7)\n"
    "v_mfma_f32_32x32x4_2b_bf16 v[16:47], v[126:127], v[120:121], 0\n"
    "ds_read_b32 v126, v139 offset:16000\n"
    "v_exp_f32 v53, v53\n"
    "v_exp_f32 v54, v54\n"
    "v_exp_f32 v55, v55\n"
    "v_exp_f32 v56, v56\n"
    "v_exp_f32 v57, v57\n"
    "v_exp_f32 v58, v58\n"
    "v_exp_f32 v59, v59\n"
    "v_exp_f32 v60, v60\n"
    "s_waitcnt lgkmcnt(3)\n"
    "v_smfmac_f32_32x32x32_bf16 v[80:95], v[128:131], v[48:55], v136\n"
    "ds_read_b64_tr_b16 v[240:241], v5 offset:0\n"
    "ds_read_b64_tr_b16 v[242:243], v5 offset:512\n"
    "ds_read_b64_tr_b16 v[244:245], v5 offset:32\n"
    "ds_read_b64_tr_b16 v[246:247], v5 offset:544\n"
    "v_exp_f32 v61, v61\n"
    "v_exp_f32 v62, v62\n"
    "v_exp_f32 v63, v63\n"
    "v_exp_f32 v64, v64\n"
    "v_exp_f32 v65, v65\n"
    "s_waitcnt lgkmcnt(5)\n"
    "v_smfmac_f32_32x32x32_bf16 v[80:95], v[132:135], v[56:63], v136\n"
    "ds_read_b64_tr_b16 v[248:249], v5 offset:64\n"
    "ds_read_b64_tr_b16 v[250:251], v5 offset:576\n"
    "ds_read_b64_tr_b16 v[252:253], v5 offset:96\n"
    "ds_read_b64_tr_b16 v[254:255], v5 offset:608\n"
    "v_exp_f32 v66, v66\n"
    "v_exp_f32 v67, v67\n"
    "v_exp_f32 v68, v68\n"
    "v_mfma_f32_32x32x16_bf16 v[16:31], v[122:125], v[112:115], v[16:31]\n"
    "v_exp_f32 v69, v69\n"
    "v_exp_f32 v70, v70\n"
    "v_exp_f32 v71, v71\n"
    "v_exp_f32 v72, v72\n"
    "v_exp_f32 v73, v73\n"
    "v_smfmac_f32_32x32x32_bf16 v[96:111], v[128:131], v[64:71], v136\n"
    "s_mov_b64 exec, s[42:43]\n"
    "ds_read_b128 v[128:131], v138 offset:9216\n"
    "s_mov_b64 exec, -1\n"
    "v_exp_f32 v74, v74\n"
    "v_exp_f32 v75, v75\n"
    "v_exp_f32 v76, v76\n"
    "v_exp_f32 v77, v77\n"
    "v_mfma_f32_32x32x16_bf16 v[32:47], v[122:125], v[116:119], v[32:47]\n"
    "ds_read_b128 v[122:125], v137 offset:13824\n"
    "v_exp_f32 v78, v78\n"
    "v_exp_f32 v79, v79\n"
    "v_exp_f32 v16, v16\n"
    "v_exp_f32 v17, v17\n"
    "v_smfmac_f32_32x32x32_bf16 v[96:111], v[132:135], v[72:79], v136\n"
    "s_mov_b64 exec, s[42:43]\n"
    "ds_read_b128 v[132:135], v2 offset:9216\n"
    "s_mov_b64 exec, -1\n"
    "v_exp_f32 v18, v18\n"
    "v_exp_f32 v19, v19\n"
    "v_exp_f32 v20, v20\n"
    "s_waitcnt lgkmcnt(11)\n"
    "v_mfma_f32_32x32x4_2b_bf16 v[48:79], v[126:127], v[120:121], 0\n"
    "ds_read_b32 v126, v139 offset:20608\n"
    "v_exp_f32 v21, v21\n"
    "v_exp_f32 v22, v22\n"
    "v_exp_f32 v23, v23\n"
    "v_exp_f32 v24, v24\n"
    "v_exp_f32 v25, v25\n"
    "v_exp_f32 v26, v26\n"
    "v_exp_f32 v27, v27\n"
    "v_exp_f32 v28, v28\n"
    "s_waitcnt lgkmcnt(3)\n"
    "v_smfmac_f32_32x32x32_bf16 v[80:95], v[128:131], v[16:23], v136\n"
    "s_mov_b32 s44, s87\n"
    "buffer_store_dwordx4 v[240:243], v6, s[16:19], s44 offen sc1\n"
    "v_exp_f32 v29, v29\n"
    "v_exp_f32 v30, v30\n"
    "v_exp_f32 v31, v31\n"
    "v_exp_f32 v32, v32\n"
    "v_exp_f32 v33, v33\n"
    "s_waitcnt lgkmcnt(1)\n"
    "v_smfmac_f32_32x32x32_bf16 v[80:95], v[132:135], v[24:31], v136\n"
    "v_exp_f32 v34, v34\n"
    "v_exp_f32 v35, v35\n"
    "v_exp_f32 v36, v36\n"
    "v_mfma_f32_32x32x16_bf16 v[48:63], v[122:125], v[112:115], v[48:63]\n"
    "v_exp_f32 v37, v37\n"
    "v_exp_f32 v38, v38\n"
    "v_exp_f32 v39, v39\n"
    "v_exp_f32 v40, v40\n"
    "v_exp_f32 v41, v41\n"
    "v_smfmac_f32_32x32x32_bf16 v[96:111], v[128:131], v[32:39], v136\n"
    "s_mov_b64 exec, s[42:43]\n"
    "ds_read_b128 v[128:131], v138 offset:13824\n"
    "s_mov_b64 exec, -1\n"
    "v_exp_f32 v42, v42\n"
    "v_exp_f32 v43, v43\n"
    "v_exp_f32 v44, v44\n"
    "v_exp_f32 v45, v45\n"
    "v_mfma_f32_32x32x16_bf16 v[64:79], v[122:125], v[116:119], v[64:79]\n"
    "ds_read_b128 v[122:125], v137 offset:18432\n"
    "v_exp_f32 v46, v46\n"
    "v_exp_f32 v47, v47\n"
    "v_exp_f32 v48, v48\n"
    "v_exp_f32 v49, v49\n"
    "v_smfmac_f32_32x32x32_bf16 v[96:111], v[132:135], v[40:47], v136\n"
    "s_mov_b64 exec, s[42:43]\n"
    "ds_read_b128 v[132:135], v2 offset:13824\n"
    "s_mov_b64 exec, -1\n"
    "s_setprio 2\n"
    "v_exp_f32 v50, v50\n"
    "v_exp_f32 v51, v51\n"
    "v_exp_f32 v52, v52\n"
    "s_waitcnt lgkmcnt(3)\n"
    "v_mfma_f32_32x32x4_2b_bf16 v[16:47], v[126:127], v[120:121], 0\n"
    "ds_read_b32 v126, v139 offset:25216\n"
    "v_exp_f32 v53, v53\n"
    "v_exp_f32 v54, v54\n"
    "v_exp_f32 v55, v55\n"
    "v_exp_f32 v56, v56\n"
    "v_exp_f32 v57, v57\n"
    "v_exp_f32 v58, v58\n"
    "v_exp_f32 v59, v59\n"
    "v_exp_f32 v60, v60\n"
    "s_waitcnt lgkmcnt(3)\n"
    "v_smfmac_f32_32x32x32_bf16 v[80:95], v[128:131], v[48:55], v136\n"
    "s_add_u32 s44, s44, s85\n"
    "buffer_store_dwordx4 v[244:247], v6, s[16:19], s44 offen sc1\n"
    "v_exp_f32 v61, v61\n"
    "v_exp_f32 v62, v62\n"
    "v_exp_f32 v63, v63\n"
    "v_exp_f32 v64, v64\n"
    "v_exp_f32 v65, v65\n"
    "s_waitcnt lgkmcnt(1)\n"
    "v_smfmac_f32_32x32x32_bf16 v[80:95], v[132:135], v[56:63], v136\n"
    "v_exp_f32 v66, v66\n"
    "v_exp_f32 v67, v67\n"
    "v_exp_f32 v68, v68\n"
    "v_mfma_f32_32x32x16_bf16 v[16:31], v[122:125], v[112:115], v[16:31]\n"
    "v_exp_f32 v69, v69\n"
    "v_exp_f32 v70, v70\n"
    "v_exp_f32 v71, v71\n"
    "v_exp_f32 v72, v72\n"
    "v_exp_f32 v73, v73\n"
    "v_smfmac_f32_32x32x32_bf16 v[96:111], v[128:131], v[64:71], v136\n"
    "s_mov_b64 exec, s[42:43]\n"
    "ds_read_b128 v[128:131], v138 offset:18432\n"
    "s_mov_b64 exec, -1\n"
    "v_exp_f32 v74, v74\n"
    "v_exp_f32 v75, v75\n"
    "v_exp_f32 v76, v76\n"
    "v_exp_f32 v77, v77\n"
    "v_mfma_f32_32x32x16_bf16 v[32:47], v[122:125], v[116:119], v[32:47]\n"
    "ds_read_b128 v[122:125], v137 offset:23040\n"
    "v_exp_f32 v78, v78\n"
    "v_exp_f32 v79, v79\n"
    "v_exp_f32 v16, v16\n"
    "v_exp_f32 v17, v17\n"
    "v_smfmac_f32_32x32x32_bf16 v[96:111], v[132:135], v[72:79], v136\n"
    "s_mov_b64 exec, s[42:43]\n"
    "ds_read_b128 v[132:135], v2 offset:18432\n"
    "s_mov_b64 exec, -1\n"
    "s_setprio 3\n"
    "v_exp_f32 v18, v18\n"
    "v_exp_f32 v19, v19\n"
    "v_exp_f32 v20, v20\n"
    "s_waitcnt lgkmcnt(3)\n"
    "v_mfma_f32_32x32x4_2b_bf16 v[48:79], v[126:127], v[120:121], 0\n"
    "ds_read_b32 v126, v139 offset:29824\n"
    "v_exp_f32 v21, v21\n"
    "v_exp_f32 v22, v22\n"
    "v_exp_f32 v23, v23\n"
    "v_exp_f32 v24, v24\n"
    "v_exp_f32 v25, v25\n"
    "v_exp_f32 v26, v26\n"
    "v_exp_f32 v27, v27\n"
    "v_exp_f32 v28, v28\n"
    "s_waitcnt lgkmcnt(3)\n"
    "v_smfmac_f32_32x32x32_bf16 v[80:95], v[128:131], v[16:23], v136\n"
    "s_add_u32 s44, s44, s85\n"
    "buffer_store_dwordx4 v[248:251], v6, s[16:19], s44 offen sc1\n"
    "v_exp_f32 v29, v29\n"
    "v_exp_f32 v30, v30\n"
    "v_exp_f32 v31, v31\n"
    "v_exp_f32 v32, v32\n"
    "v_exp_f32 v33, v33\n"
    "s_waitcnt lgkmcnt(1)\n"
    "v_smfmac_f32_32x32x32_bf16 v[80:95], v[132:135], v[24:31], v136\n"
    "v_exp_f32 v34, v34\n"
    "v_exp_f32 v35, v35\n"
    "v_exp_f32 v36, v36\n"
    "v_mfma_f32_32x32x16_bf16 v[48:63], v[122:125], v[112:115], v[48:63]\n"
    "v_exp_f32 v37, v37\n"
    "v_exp_f32 v38, v38\n"
    "v_exp_f32 v39, v39\n"
    "v_exp_f32 v40, v40\n"
    "v_exp_f32 v41, v41\n"
    "v_smfmac_f32_32x32x32_bf16 v[96:111], v[128:131], v[32:39], v136\n"
    "s_mov_b64 exec, s[42:43]\n"
    "ds_read_b128 v[128:131], v138 offset:23040\n"
    "s_mov_b64 exec, -1\n"
    "v_exp_f32 v42, v42\n"
    "v_exp_f32 v43, v43\n"
    "v_exp_f32 v44, v44\n"
    "v_exp_f32 v45, v45\n"
    "v_mfma_f32_32x32x16_bf16 v[64:79], v[122:125], v[116:119], v[64:79]\n"
    "ds_read_b128 v[122:125], v137 offset:27648\n"
    "v_exp_f32 v46, v46\n"
    "v_exp_f32 v47, v47\n"
    "v_exp_f32 v48, v48\n"
    "v_exp_f32 v49, v49\n"
    "v_smfmac_f32_32x32x32_bf16 v[96:111], v[132:135], v[40:47], v136\n"
    "s_mov_b64 exec, s[42:43]\n"
    "ds_read_b128 v[132:135], v2 offset:23040\n"
    "s_mov_b64 exec, -1\n"
    "v_exp_f32 v50, v50\n"
    "v_exp_f32 v51, v51\n"
    "v_exp_f32 v52, v52\n"
    "s_waitcnt lgkmcnt(3)\n"
    "v_mfma_f32_32x32x4_2b_bf16 v[16:47], v[126:127], v[120:121], 0\n"
    "ds_read_b32 v126, v139 offset:34432\n"
    "v_exp_f32 v53, v53\n"
    "v_exp_f32 v54, v54\n"
    "v_exp_f32 v55, v55\n"
    "v_exp_f32 v56, v56\n"
    "v_exp_f32 v57, v57\n"
    "v_exp_f32 v58, v58\n"
    "v_exp_f32 v59, v59\n"
    "v_exp_f32 v60, v60\n"
    "s_waitcnt lgkmcnt(3)\n"
    "v_smfmac_f32_32x32x32_bf16 v[80:95], v[128:131], v[48:55], v136\n"
    "s_add_u32 s44, s44, s85\n"
    "buffer_store_dwordx4 v[252:255], v6, s[16:19], s44 offen sc1\n"
    "v_exp_f32 v61, v61\n"
    "v_exp_f32 v62, v62\n"
    "v_exp_f32 v63, v63\n"
    "v_exp_f32 v64, v64\n"
    "v_exp_f32 v65, v65\n"
    "s_waitcnt lgkmcnt(1)\n"
    "v_smfmac_f32_32x32x32_bf16 v[80:95], v[132:135], v[56:63], v136\n"
    "s_waitcnt vmcnt(7)\n"
    "ds_write_b128 v3, v[224:227] offset:0\n"
    "ds_write_b128 v3, v[228:231] offset:1024\n"
    "v_exp_f32 v66, v66\n"
    "v_exp_f32 v67, v67\n"
    "v_exp_f32 v68, v68\n"
    "v_mfma_f32_32x32x16_bf16 v[16:31], v[122:125], v[112:115], v[16:31]\n"
    "s_waitcnt vmcnt(5)\n"
    "ds_write_b128 v3, v[232:235] offset:2048\n"
    "ds_write_b128 v3, v[236:239] offset:3072\n"
    "v_exp_f32 v69, v69\n"
    "v_exp_f32 v70, v70\n"
    "v_exp_f32 v71, v71\n"
    "v_exp_f32 v72, v72\n"
    "v_exp_f32 v73, v73\n"
    "v_smfmac_f32_32x32x32_bf16 v[96:111], v[128:131], v[64:71], v136\n"
    "s_mov_b64 exec, s[42:43]\n"
    "ds_read_b128 v[128:131], v138 offset:27648\n"
    "s_mov_b64 exec, -1\n"
    "v_exp_f32 v74, v74\n"
    "v_exp_f32 v75, v75\n"
    "v_exp_f32 v76, v76\n"
    "v_exp_f32 v77, v77\n"
    "v_mfma_f32_32x32x16_bf16 v[32:47], v[122:125], v[116:119], v[32:47]\n"
    "ds_read_b128 v[122:125], v137 offset:32256\n"
    "v_exp_f32 v78, v78\n"
    "v_exp_f32 v79, v79\n"
    "v_exp_f32 v16, v16\n"
    "v_exp_f32 v17, v17\n"
    "v_smfmac_f32_32x32x32_bf16 v[96:111], v[132:135], v[72:79], v136\n"
    "s_mov_b64 exec, s[42:43]\n"
    "ds_read_b128 v[132:135], v2 offset:27648\n"
    "s_mov_b64 exec, -1\n"
    "v_exp_f32 v18, v18\n"
    "v_exp_f32 v19, v19\n"
    "v_exp_f32 v20, v20\n"
    "s_waitcnt lgkmcnt(7)\n"
    "v_mfma_f32_32x32x4_2b_bf16 v[48:79], v[126:127], v[120:121], 0\n"
    "ds_read_b32 v126, v139 offset:39040\n"
    "v_exp_f32 v21, v21\n"
    "v_exp_f32 v22, v22\n"
    "v_exp_f32 v23, v23\n"
    "v_exp_f32 v24, v24\n"
    "v_exp_f32 v25, v25\n"
    "v_exp_f32 v26, v26\n"
    "v_exp_f32 v27, v27\n"
    "v_exp_f32 v28, v28\n"
    "s_waitcnt lgkmcnt(3)\n"
    "v_smfmac_f32_32x32x32_bf16 v[80:95], v[128:131], v[16:23], v136\n"
    "ds_read_b64_tr_b16 v[240:241], v5 offset:0\n"
    "ds_read_b64_tr_b16 v[242:243], v5 offset:512\n"
    "ds_read_b64_tr_b16 v[244:245], v5 offset:32\n"
    "ds_read_b64_tr_b16 v[246:247], v5 offset:544\n"
    "v_exp_f32 v29, v29\n"
    "v_exp_f32 v30, v30\n"
    "v_exp_f32 v31, v31\n"
    "v_exp_f32 v32, v32\n"
    "v_exp_f32 v33, v33\n"
    "s_waitcnt lgkmcnt(5)\n"
    "v_smfmac_f32_32x32x32_bf16 v[80:95], v[132:135], v[24:31], v136\n"
    "ds_read_b64_tr_b16 v[248:249], v5 offset:64\n"
    "ds_read_b64_tr_b16 v[250:251], v5 offset:576\n"
    "ds_read_b64_tr_b16 v[252:253], v5 offset:96\n"
    "ds_read_b64_tr_b16 v[254:255], v5 offset:608\n"
    "v_exp_f32 v34, v34\n"
    "v_exp_f32 v35, v35\n"
    "v_exp_f32 v36, v36\n"
    "v_mfma_f32_32x32x16_bf16 v[48:63], v[122:125], v[112:115], v[48:63]\n"
    "s_waitcnt vmcnt(4)\n"
    "s_mov_b32 s48, 0\n"
    "kb_fa21_%=:\n"
    "v_xor_b32 v140, s60, v140\n"
    "v_xor_b32 v141, s60, v141\n"
    "v_xor_b32 v142, s60, v142\n"
    "v_xor_b32 v143, s60, v143\n"
    "v_or3_b32 v140, v140, v141, v142\n"
    "v_or_b32 v140, v140, v143\n"
    "v_cmp_ne_u32 vcc, 0, v140\n"
    "s_cbranch_vccnz kb_fb22_%=\n"
    "kb_fa21_%=x:\n"
    "v_exp_f32 v37, v37\n"
    "v_exp_f32 v38, v38\n"
    "v_exp_f32 v39, v39\n"
    "v_exp_f32 v40, v40\n"
    "v_exp_f32 v41, v41\n"
    "v_smfmac_f32_32x32x32_bf16 v[96:111], v[128:131], v[32:39], v136\n"
    "s_mov_b64 exec, s[42:43]\n"
    "ds_read_b128 v[128:131], v138 offset:32256\n"
    "s_mov_b64 exec, -1\n"
    "v_exp_f32 v42, v42\n"
    "v_exp_f32 v43, v43\n"
    "v_exp_f32 v44, v44\n"
    "v_exp_f32 v45, v45\n"
    "v_mfma_f32_32x32x16_bf16 v[64:79], v[122:125], v[116:119], v[64:79]\n"
    "ds_read_b128 v[122:125], v137 offset:36864\n"
    "v_exp_f32 v46, v46\n"
    "v_exp_f32 v47, v47\n"
    "v_exp_f32 v48, v48\n"
    "v_exp_f32 v49, v49\n"
    "v_smfmac_f32_32x32x32_bf16 v[96:111], v[132:135], v[40:47], v136\n"
    "s_mov_b64 exec, s[42:43]\n"
    "ds_read_b128 v[132:135], v2 offset:32256\n"
    "s_mov_b64 exec, -1\n"
    "s_setprio 2\n"
    "v_exp_f32 v50, v50\n"
    "v_exp_f32 v51, v51\n"
    "v_exp_f32 v52, v52\n"
    "s_waitcnt lgkmcnt(11)\n"
    "v_mfma_f32_32x32x4_2b_bf16 v[16:47], v[126:127], v[120:121], 0\n"
    "ds_read_b32 v126, v139 offset:43648\n"
    "s_lshl_b32 s39, s89, 5\n"
    "v_exp_f32 v53, v53\n"
    "v_exp_f32 v54, v54\n"
    "v_exp_f32 v55, v55\n"
    "v_exp_f32 v56, v56\n"
    "v_exp_f32 v57, v57\n"
    "v_exp_f32 v58, v58\n"
    "v_exp_f32 v59, v59\n"
    "v_exp_f32 v60, v60\n"
    "s_waitcnt lgkmcnt(3)\n"
    "v_smfmac_f32_32x32x32_bf16 v[80:95], v[128:131], v[48:55], v136\n"
    "s_add_u32 s44, s87, s86\n"
    "buffer_store_dwordx4 v[240:243], v6, s[16:19], s44 offen sc1\n"
    "s_lshl4_add_u32 s39, s90, s39\n"
    "v_exp_f32 v61, v61\n"
    "v_exp_f32 v62, v62\n"
    "v_exp_f32 v63, v63\n"
    "v_exp_f32 v64, v64\n"
    "v_exp_f32 v65, v65\n"
    "s_waitcnt lgkmcnt(1)\n"
    "v_smfmac_f32_32x32x32_bf16 v[80:95], v[132:135], v[56:63], v136\n"
    "buffer_load_dwordx4 v[148:151], v7, s[16:19], s46 offen sc1\n"
    "buffer_load_dwordx4 v[152:155], v8, s[16:19], s47 offen sc1\n"
    "buffer_load_dwordx4 v[156:159], v9, s[16:19], s46 offen offset:2048 sc1\n"
    "s_add_u32 s46, s46, 36864\n"
    "s_add_u32 s47, s47, s93\n"
    "s_mul_i32 s39, s39, 0x480\n"
    "v_exp_f32 v66, v66\n"
    "v_exp_f32 v67, v67\n"
    "v_exp_f32 v68, v68\n"
    "v_mfma_f32_32x32x16_bf16 v[16:31], v[122:125], v[112:115], v[16:31]\n"
    "s_lshl_b32 s51, s92, 5\n"
    "s_add_u32 s39, s39, s51\n"
    "v_and_b32 v4, 15, v0\n"
    "v_exp_f32 v69, v69\n"
    "v_exp_f32 v70, v70\n"
    "v_exp_f32 v71, v71\n"
    "v_exp_f32 v72, v72\n"
    "v_exp_f32 v73, v73\n"
    "v_smfmac_f32_32x32x32_bf16 v[96:111], v[128:131], v[64:71], v136\n"
    "s_mov_b64 exec, s[42:43]\n"
    "ds_read_b128 v[128:131], v138 offset:36864\n"
    "s_mov_b64 exec, -1\n"
    "v_exp_f32 v74, v74\n"
    "v_exp_f32 v75, v75\n"
    "v_exp_f32 v76, v76\n"
    "v_exp_f32 v77, v77\n"
    "v_mfma_f32_32x32x16_bf16 v[32:47], v[122:125], v[116:119], v[32:47]\n"
    "ds_read_b128 v[122:125], v137 offset:41472\n"
    "v_mul_u32_u24 v4, 0x480, v4\n"
    "v_add_u32 v4, s39, v4\n"
    "v_exp_f32 v78, v78\n"
    "v_exp_f32 v79, v79\n"
    "v_exp_f32 v16, v16\n"
    "v_exp_f32 v17, v17\n"
    "v_smfmac_f32_32x32x32_bf16 v[96:111], v[132:135], v[72:79], v136\n"
    "s_mov_b64 exec, s[42:43]\n"
    "ds_read_b128 v[132:135], v2 offset:36864\n"
    "s_mov_b64 exec, -1\n"
    "s_setprio 3\n"
    "v_exp_f32 v18, v18\n"
    "v_exp_f32 v19, v19\n"
    "v_exp_f32 v20, v20\n"
    "s_waitcnt lgkmcnt(3)\n"
    "v_mfma_f32_32x32x4_2b_bf16 v[48:79], v[126:127], v[120:121], 0\n"
    "ds_read_b32 v126, v139 offset:48256\n"
    "v_lshrrev_b32 v11, 4, v0\n"
    "v_exp_f32 v21, v21\n"
    "v_exp_f32 v22, v22\n"
    "v_exp_f32 v23, v23\n"
    "v_exp_f32 v24, v24\n"
    "v_exp_f32 v25, v25\n"
    "v_exp_f32 v26, v26\n"
    "v_exp_f32 v27, v27\n"
    "v_exp_f32 v28, v28\n"
    "s_waitcnt lgkmcnt(3)\n"
    "v_smfmac_f32_32x32x32_bf16 v[80:95], v[128:131], v[16:23], v136\n"
    "s_add_u32 s44, s44, s85\n"
    "buffer_store_dwordx4 v[244:247], v6, s[16:19], s44 offen sc1\n"
    "v_lshl_add_u32 v4, v11, 3, v4\n"
    "v_exp_f32 v29, v29\n"
    "v_exp_f32 v30, v30\n"
    "v_exp_f32 v31, v31\n"
    "v_exp_f32 v32, v32\n"
    "v_exp_f32 v33, v33\n"
    "s_waitcnt lgkmcnt(1)\n"
    "v_smfmac_f32_32x32x32_bf16 v[80:95], v[132:135], v[24:31], v136\n"
    "buffer_load_dwordx4 v[160:163], v7, s[16:19], s46 offen sc1\n"
    "buffer_load_dwordx4 v[164:167], v8, s[16:19], s47 offen sc1\n"
    "buffer_load_dwordx4 v[168:171], v9, s[16:19], s46 offen offset:2048 sc1\n"
    "s_add_u32 s46, s46, 36864\n"
    "s_add_u32 s47, s47, s93\n"
    "s_lshl_b32 s39, s89, 5\n"
    "v_exp_f32 v34, v34\n"
    "v_exp_f32 v35, v35\n"
    "v_exp_f32 v36, v36\n"
    "v_mfma_f32_32x32x16_bf16 v[48:63], v[122:125], v[112:115], v[48:63]\n"
    "s_lshl4_add_u32 s39, s90, s39\n"
    "s_lshl4_add_u32 s39, s39, 0x2015000\n"
    "v_and_b32 v5, 15, v0\n"
    "v_exp_f32 v37, v37\n"
    "v_exp_f32 v38, v38\n"
    "v_exp_f32 v39, v39\n"
    "v_exp_f32 v40, v40\n"
    "v_exp_f32 v41, v41\n"
    "v_smfmac_f32_32x32x32_bf16 v[96:111], v[128:131], v[32:39], v136\n"
    "s_mov_b64 exec, s[42:43]\n"
    "ds_read_b128 v[128:131], v138 offset:41472\n"
    "s_mov_b64 exec, -1\n"
    "v_exp_f32 v42, v42\n"
    "v_exp_f32 v43, v43\n"
    "v_exp_f32 v44, v44\n"
    "v_exp_f32 v45, v45\n"
    "v_mfma_f32_32x32x16_bf16 v[64:79], v[122:125], v[116:119], v[64:79]\n"
    "ds_read_b128 v[122:125], v137 offset:46080\n"
    "v_lshl_add_u32 v5, v5, 4, s39\n"
    "v_exp_f32 v46, v46\n"
    "v_exp_f32 v47, v47\n"
    "v_exp_f32 v48, v48\n"
    "v_exp_f32 v49, v49\n"
    "v_smfmac_f32_32x32x32_bf16 v[96:111], v[132:135], v[40:47], v136\n"
    "s_mov_b64 exec, s[42:43]\n"
    "ds_read_b128 v[132:135], v2 offset:41472\n"
    "s_mov_b64 exec, -1\n"
    "v_exp_f32 v50, v50\n"
    "v_exp_f32 v51, v51\n"
    "v_exp_f32 v52, v52\n"
    "s_waitcnt lgkmcnt(3)\n"
    "v_mfma_f32_32x32x4_2b_bf16 v[16:47], v[126:127], v[120:121], 0\n"
    "ds_read_b32 v126, v139 offset:52864\n"
    "v_exp_f32 v53, v53\n"
    "v_exp_f32 v54, v54\n"
    "v_exp_f32 v55, v55\n"
    "v_exp_f32 v56, v56\n"
    "v_exp_f32 v57, v57\n"
    "v_exp_f32 v58, v58\n"
    "v_exp_f32 v59, v59\n"
    "v_exp_f32 v60, v60\n"
    "s_waitcnt lgkmcnt(3)\n"
    "v_smfmac_f32_32x32x32_bf16 v[80:95], v[128:131], v[48:55], v136\n"
    "s_add_u32 s44, s44, s85\n"
    "buffer_store_dwordx4 v[248:251], v6, s[16:19], s44 offen sc1\n"
    "v_exp_f32 v61, v61\n"
    "v_exp_f32 v62, v62\n"
    "v_exp_f32 v63, v63\n"
    "v_exp_f32 v64, v64\n"
    "v_exp_f32 v65, v65\n"
    "s_waitcnt lgkmcnt(1)\n"
    "v_smfmac_f32_32x32x32_bf16 v[80:95], v[132:135], v[56:63], v136\n"
    "buffer_load_dwordx4 v[172:175], v7, s[16:19], s46 offen sc1\n"
    "buffer_load_dwordx4 v[176:179], v8, s[16:19], s47 offen sc1\n"
    "buffer_load_dwordx4 v[180:183], v9, s[16:19], s46 offen offset:2048 sc1\n"
    "s_add_u32 s46, s46, 36864\n"
    "s_add_u32 s47, s47, s93\n"
    "v_exp_f32 v66, v66\n"
    "v_exp_f32 v67, v67\n"
    "v_exp_f32 v68, v68\n"
    "v_mfma_f32_32x32x16_bf16 v[16:31], v[122:125], v[112:115], v[16:31]\n"
    "v_exp_f32 v69, v69\n"
    "v_exp_f32 v70, v70\n"
    "v_exp_f32 v71, v71\n"
    "v_exp_f32 v72, v72\n"
    "v_exp_f32 v73, v73\n"
    "v_smfmac_f32_32x32x32_bf16 v[96:111], v[128:131], v[64:71], v136\n"
    "s_mov_b64 exec, s[42:43]\n"
    "ds_read_b128 v[128:131], v138 offset:46080\n"
    "s_mov_b64 exec, -1\n"
    "v_exp_f32 v74, v74\n"
    "v_exp_f32 v75, v75\n"
    "v_exp_f32 v76, v76\n"
    "v_exp_f32 v77, v77\n"
    "v_mfma_f32_32x32x16_bf16 v[32:47], v[122:125], v[116:119], v[32:47]\n"
    "ds_read_b128 v[122:125], v137 offset:50688\n"
    "v_exp_f32 v78, v78\n"
    "v_exp_f32 v79, v79\n"
    "v_exp_f32 v16, v16\n"
    "v_exp_f32 v17, v17\n"
    "v_smfmac_f32_32x32x32_bf16 v[96:111], v[132:135], v[72:79], v136\n"
    "s_mov_b64 exec, s[42:43]\n"
    "ds_read_b128 v[132:135], v2 offset:46080\n"
    "s_mov_b64 exec, -1\n"
    "v_exp_f32 v18, v18\n"
    "v_exp_f32 v19, v19\n"
    "v_exp_f32 v20, v20\n"
    "s_waitcnt lgkmcnt(3)\n"
    "v_mfma_f32_32x32x4_2b_bf16 v[48:79], v[126:127], v[120:121], 0\n"
    "ds_read_b32 v126, v139 offset:57472\n"
    "v_exp_f32 v21, v21\n"
    "v_exp_f32 v22, v22\n"
    "v_exp_f32 v23, v23\n"
    "v_exp_f32 v24, v24\n"
    "v_exp_f32 v25, v25\n"
    "v_exp_f32 v26, v26\n"
    "v_exp_f32 v27, v27\n"
    "v_exp_f32 v28, v28\n"
    "s_waitcnt lgkmcnt(3)\n"
    "v_smfmac_f32_32x32x32_bf16 v[80:95], v[128:131], v[16:23], v136\n"
    "s_add_u32 s44, s44, s85\n"
    "buffer_store_dwordx4 v[252:255], v6, s[16:19], s44 offen sc1\n"
    "v_exp_f32 v29, v29\n"
    "v_exp_f32 v30, v30\n"
    "v_exp_f32 v31, v31\n"
    "v_exp_f32 v32, v32\n"
    "v_exp_f32 v33, v33\n"
    "s_waitcnt lgkmcnt(1)\n"
    "v_smfmac_f32_32x32x32_bf16 v[80:95], v[132:135], v[24:31], v136\n"
    "buffer_load_dwordx4 v[184:187], v7, s[16:19], s46 offen sc1\n"
    "buffer_load_dwordx4 v[188:191], v8, s[16:19], s47 offen sc1\n"
    "buffer_load_dwordx4 v[192:195], v9, s[16:19], s46 offen offset:2048 sc1\n"
    "s_add_u32 s46, s46, 36864\n"
    "s_add_u32 s47, s47, s93\n"
    "v_exp_f32 v34, v34\n"
    "v_exp_f32 v35, v35\n"
    "v_exp_f32 v36, v36\n"
    "v_mfma_f32_32x32x16_bf16 v[48:63], v[122:125], v[112:115], v[48:63]\n"
    "v_exp_f32 v37, v37\n"
    "v_exp_f32 v38, v38\n"
    "v_exp_f32 v39, v39\n"
    "v_exp_f32 v40, v40\n"
    "v_exp_f32 v41, v41\n"
    "v_smfmac_f32_32x32x32_bf16 v[96:111], v[128:131], v[32:39], v136\n"
    "s_mov_b64 exec, s[42:43]\n"
    "ds_read_b128 v[128:131], v138 offset:50688\n"
    "s_mov_b64 exec, -1\n"
    "v_exp_f32 v42, v42\n"
    "v_exp_f32 v43, v43\n"
    "v_exp_f32 v44, v44\n"
    "v_exp_f32 v45, v45\n"
    "v_mfma_f32_32x32x16_bf16 v[64:79], v[122:125], v[116:119], v[64:79]\n"
    "ds_read_b128 v[122:125], v137 offset:55296\n"
    "v_exp_f32 v46, v46\n"
    "v_exp_f32 v47, v47\n"
    "v_exp_f32 v48, v48\n"
    "v_exp_f32 v49, v49\n"
    "v_smfmac_f32_32x32x32_bf16 v[96:111], v[132:135], v[40:47], v136\n"
    "s_mov_b64 exec, s[42:43]\n"
    "ds_read_b128 v[132:135], v2 offset:50688\n"
    "s_mov_b64 exec, -1\n"
    "s_setprio 2\n"
    "v_exp_f32 v50, v50\n"
    "v_exp_f32 v51, v51\n"
    "v_exp_f32 v52, v52\n"
    "s_waitcnt lgkmcnt(3)\n"
    "v_mfma_f32_32x32x4_2b_bf16 v[16:47], v[126:127], v[120:121], 0\n"
    "ds_read_b32 v126, v139 offset:62080\n"
    "v_exp_f32 v53, v53\n"
    "v_exp_f32 v54, v54\n"
    "v_exp_f32 v55, v55\n"
    "v_exp_f32 v56, v56\n"
    "v_exp_f32 v57, v57\n"
    "v_exp_f32 v58, v58\n"
    "v_exp_f32 v59, v59\n"
    "v_exp_f32 v60, v60\n"
    "s_waitcnt lgkmcnt(3)\n"
    "v_smfmac_f32_32x32x32_bf16 v[80:95], v[128:131], v[48:55], v136\n"
    "buffer_load_dwordx4 v[196:199], v7, s[16:19], s46 offen sc1\n"
    "buffer_load_dwordx4 v[200:203], v8, s[16:19], s47 offen sc1\n"
    "buffer_load_dwordx4 v[204:207], v9, s[16:19], s46 offen offset:2048 sc1\n"
    "s_add_u32 s46, s46, 36864\n"
    "s_add_u32 s47, s47, s93\n"
    "v_exp_f32 v61, v61\n"
    "v_exp_f32 v62, v62\n"
    "v_exp_f32 v63, v63\n"
    "v_exp_f32 v64, v64\n"
    "v_exp_f32 v65, v65\n"
    "s_waitcnt lgkmcnt(1)\n"
    "v_smfmac_f32_32x32x32_bf16 v[80:95], v[132:135], v[56:63], v136\n"
    "v_exp_f32 v66, v66\n"
    "v_exp_f32 v67, v67\n"
    "v_exp_f32 v68, v68\n"
    "v_mfma_f32_32x32x16_bf16 v[16:31], v[122:125], v[112:115], v[16:31]\n"
    "v_exp_f32 v69, v69\n"
    "v_exp_f32 v70, v70\n"
    "v_exp_f32 v71, v71\n"
    "v_exp_f32 v72, v72\n"
    "v_exp_f32 v73, v73\n"
    "v_smfmac_f32_32x32x32_bf16 v[96:111], v[128:131], v[64:71], v136\n"
    "s_mov_b64 exec, s[42:43]\n"
    "ds_read_b128 v[128:131], v138 offset:55296\n"
    "s_mov_b64 exec, -1\n"
    "v_exp_f32 v74, v74\n"
    "v_exp_f32 v75, v75\n"
    "v_exp_f32 v76, v76\n"
    "v_exp_f32 v77, v77\n"
    "v_mfma_f32_32x32x16_bf16 v[32:47], v[122:125], v[116:119], v[32:47]\n"
    "ds_read_b128 v[122:125], v137 offset:59904\n"
    "v_exp_f32 v78, v78\n"
    "v_exp_f32 v79, v79\n"
    "v_add_u32 v137, 55296, v137\n"
    "v_add_u32 v138, 55296, v138\n"
    "v_add_u32 v2, 55296, v2\n"
    "v_add_u32 v139, 55296, v139\n"
    "v_exp_f32 v16, v16\n"
    "v_exp_f32 v17, v17\n"
    "v_smfmac_f32_32x32x32_bf16 v[96:111], v[132:135], v[72:79], v136\n"
    "s_mov_b64 exec, s[42:43]\n"
    "ds_read_b128 v[132:135], v2 offset:0\n"
    "s_mov_b64 exec, -1\n"
    "s_setprio 3\n"
    "v_exp_f32 v18, v18\n"
    "v_exp_f32 v19, v19\n"
    "v_exp_f32 v20, v20\n"
    "s_waitcnt lgkmcnt(3)\n"
    "v_mfma_f32_32x32x4_2b_bf16 v[48:79], v[126:127], v[120:121], 0\n"
    "ds_read_b32 v126, v139 offset:11392\n"
    "v_exp_f32 v21, v21\n"
    "v_exp_f32 v22, v22\n"
    "v_exp_f32 v23, v23\n"
    "v_exp_f32 v24, v24\n"
    "v_exp_f32 v25, v25\n"
    "v_exp_f32 v26, v26\n"
    "v_exp_f32 v27, v27\n"
    "v_exp_f32 v28, v28\n"
    "s_waitcnt lgkmcnt(3)\n"
    "v_smfmac_f32_32x32x32_bf16 v[80:95], v[128:131], v[16:23], v136\n"
    "buffer_load_dwordx4 v[208:211], v7, s[16:19], s46 offen sc1\n"
    "buffer_load_dwordx4 v[212:215], v8, s[16:19], s47 offen sc1\n"
    "buffer_load_dwordx4 v[216:219], v9, s[16:19], s46 offen offset:2048 sc1\n"
    "s_add_u32 s46, s46, 36864\n"
    "s_add_u32 s47, s47, s93\n"
    "v_exp_f32 v29, v29\n"
    "v_exp_f32 v30, v30\n"
    "v_exp_f32 v31, v31\n"
    "v_exp_f32 v32, v32\n"
    "v_exp_f32 v33, v33\n"
    "s_waitcnt lgkmcnt(1)\n"
    "v_smfmac_f32_32x32x32_bf16 v[80:95], v[132:135], v[24:31], v136\n"
    "v_exp_f32 v34, v34\n"
    "v_exp_f32 v35, v35\n"
    "v_exp_f32 v36, v36\n"
    "v_mfma_f32_32x32x16_bf16 v[48:63], v[122:125], v[112:115], v[48:63]\n"
    "v_exp_f32 v37, v37\n"
    "v_exp_f32 v38, v38\n"
    "v_exp_f32 v39, v39\n"
    "v_exp_f32 v40, v40\n"
    "v_exp_f32 v41, v41\n"
    "v_smfmac_f32_32x32x32_bf16 v[96:111], v[128:131], v[32:39], v136\n"
    "s_mov_b64 exec, s[42:43]\n"
    "ds_read_b128 v[128:131], v138 offset:4608\n"
    "s_mov_b64 exec, -1\n"
    "v_exp_f32 v42, v42\n"
    "v_exp_f32 v43, v43\n"
    "v_exp_f32 v44, v44\n"
    "v_exp_f32 v45, v45\n"
    "v_mfma_f32_32x32x16_bf16 v[64:79], v[122:125], v[116:119], v[64:79]\n"
    "ds_read_b128 v[122:125], v137 offset:9216\n"
    "v_exp_f32 v46, v46\n"
    "v_exp_f32 v47, v47\n"
    "v_exp_f32 v48, v48\n"
    "v_exp_f32 v49, v49\n"
    "v_smfmac_f32_32x32x32_bf16 v[96:111], v[132:135], v[40:47], v136\n"
    "s_mov_b64 exec, s[42:43]\n"
    "ds_read_b128 v[132:135], v2 offset:4608\n"
    "s_mov_b64 exec, -1\n"
    "v_exp_f32 v50, v50\n"
    "v_exp_f32 v51, v51\n"
    "v_exp_f32 v52, v52\n"
    "s_waitcnt lgkmcnt(3)\n"
    "v_mfma_f32_32x32x4_2b_bf16 v[16:47], v[126:127], v[120:121], 0\n"
    "ds_read_b32 v126, v139 offset:16000\n"
    "v_exp_f32 v53, v53\n"
    "v_exp_f32 v54, v54\n"
    "v_exp_f32 v55, v55\n"
    "v_exp_f32 v56, v56\n"
    "v_exp_f32 v57, v57\n"
    "v_exp_f32 v58, v58\n"
    "v_exp_f32 v59, v59\n"
    "v_exp_f32 v60, v60\n"
    "s_waitcnt lgkmcnt(3)\n"
    "v_smfmac_f32_32x32x32_bf16 v[80:95], v[128:131], v[48:55], v136\n"
    "buffer_load_dwordx4 v[220:223], v7, s[16:19], s46 offen sc1\n"
    "buffer_load_dwordx4 v[224:227], v8, s[16:19], s47 offen sc1\n"
    "buffer_load_dwordx4 v[228:231], v9, s[16:19], s46 offen offset:2048 sc1\n"
    "s_add_u32 s46, s46, 36864\n"
    "s_add_u32 s47, s47, s93\n"
    "v_exp_f32 v61, v61\n"
    "v_exp_f32 v62, v62\n"
    "v_exp_f32 v63, v63\n"
    "v_exp_f32 v64, v64\n"
    "v_exp_f32 v65, v65\n"
    "s_waitcnt lgkmcnt(1)\n"
    "v_smfmac_f32_32x32x32_bf16 v[80:95], v[132:135], v[56:63], v136\n"
    "v_exp_f32 v66, v66\n"
    "v_exp_f32 v67, v67\n"
    "v_exp_f32 v68, v68\n"
    "v_mfma_f32_32x32x16_bf16 v[16:31], v[122:125], v[112:115], v[16:31]\n"
    "v_exp_f32 v69, v69\n"
    "v_exp_f32 v70, v70\n"
    "v_exp_f32 v71, v71\n"
    "v_exp_f32 v72, v72\n"
    "v_exp_f32 v73, v73\n"
    "v_smfmac_f32_32x32x32_bf16 v[96:111], v[128:131], v[64:71], v136\n"
    "s_mov_b64 exec, s[42:43]\n"
    "ds_read_b128 v[128:131], v138 offset:9216\n"
    "s_mov_b64 exec, -1\n"
    "v_exp_f32 v74, v74\n"
    "v_exp_f32 v75, v75\n"
    "v_exp_f32 v76, v76\n"
    "v_exp_f32 v77, v77\n"
    "v_mfma_f32_32x32x16_bf16 v[32:47], v[122:125], v[116:119], v[32:47]\n"
    "ds_read_b128 v[122:125], v137 offset:13824\n"
    "v_exp_f32 v78, v78\n"
    "v_exp_f32 v79, v79\n"
    "v_exp_f32 v16, v16\n"
    "v_exp_f32 v17, v17\n"
    "v_smfmac_f32_32x32x32_bf16 v[96:111], v[132:135], v[72:79], v136\n"
    "s_mov_b64 exec, s[42:43]\n"
    "ds_read_b128 v[132:135], v2 offset:9216\n"
    "s_mov_b64 exec, -1\n"
    "s_setprio 1\n"
    "v_exp_f32 v18, v18\n"
    "v_exp_f32 v19, v19\n"
    "v_exp_f32 v20, v20\n"
    "s_waitcnt lgkmcnt(3)\n"
    "v_mfma_f32_32x32x4_2b_bf16 v[48:79], v[126:127], v[120:121], 0\n"
    "ds_read_b32 v126, v139 offset:20608\n"
    "v_exp_f32 v21, v21\n"
    "v_exp_f32 v22, v22\n"
    "v_exp_f32 v23, v23\n"
    "v_exp_f32 v24, v24\n"
    "v_exp_f32 v25, v25\n"
    "v_exp_f32 v26, v26\n"
    "v_exp_f32 v27, v27\n"
    "v_exp_f32 v28, v28\n"
    "s_waitcnt lgkmcnt(3)\n"
    "v_smfmac_f32_32x32x32_bf16 v[80:95], v[128:131], v[16:23], v136\n"
    "buffer_load_dwordx4 v[232:235], v7, s[16:19], s46 offen sc1\n"
    "buffer_load_dwordx4 v[236:239], v8, s[16:19], s47 offen sc1\n"
    "buffer_load_dwordx4 v[240:243], v9, s[16:19], s46 offen offset:2048 sc1\n"
    "s_add_u32 s46, s46, 36864\n"
    "s_add_u32 s47, s47, s93\n"
    "v_exp_f32 v29, v29\n"
    "v_exp_f32 v30, v30\n"
    "v_exp_f32 v31, v31\n"
    "v_exp_f32 v32, v32\n"
    "v_exp_f32 v33, v33\n"
    "s_waitcnt lgkmcnt(1)\n"
    "v_smfmac_f32_32x32x32_bf16 v[80:95], v[132:135], v[24:31], v136\n"
    "s_waitcnt vmcnt(15)\n"
    "buffer_store_dword v147, v13, s[16:19], 0 offen sc1\n"
    "v_exp_f32 v34, v34\n"
    "v_exp_f32 v35, v35\n"
    "v_exp_f32 v36, v36\n"
    "v_mfma_f32_32x32x16_bf16 v[48:63], v[122:125], v[112:115], v[48:63]\n"
    "v_exp_f32 v37, v37\n"
    "v_exp_f32 v38, v38\n"
    "v_exp_f32 v39, v39\n"
    "v_exp_f32 v40, v40\n"
    "v_exp_f32 v41, v41\n"
    "v_smfmac_f32_32x32x32_bf16 v[96:111], v[128:131], v[32:39], v136\n"
    "s_mov_b64 exec, s[42:43]\n"
    "ds_read_b128 v[128:131], v138 offset:13824\n"
    "s_mov_b64 exec, -1\n"
    "v_exp_f32 v42, v42\n"
    "v_exp_f32 v43, v43\n"
    "v_exp_f32 v44, v44\n"
    "v_exp_f32 v45, v45\n"
    "v_mfma_f32_32x32x16_bf16 v[64:79], v[122:125], v[116:119], v[64:79]\n"
    "ds_read_b128 v[122:125], v137 offset:18432\n"
    "v_exp_f32 v46, v46\n"
    "v_exp_f32 v47, v47\n"
    "v_exp_f32 v48, v48\n"
    "v_exp_f32 v49, v49\n"
    "v_smfmac_f32_32x32x32_bf16 v[96:111], v[132:135], v[40:47], v136\n"
    "s_mov_b64 exec, s[42:43]\n"
    "ds_read_b128 v[132:135], v2 offset:13824\n"
    "s_mov_b64 exec, -1\n"
    "v_exp_f32 v50, v50\n"
    "v_exp_f32 v51, v51\n"
    "v_exp_f32 v52, v52\n"
    "s_waitcnt lgkmcnt(3)\n"
    "v_mfma_f32_32x32x4_2b_bf16 v[16:47], v[126:127], v[120:121], 0\n"
    "ds_read_b32 v126, v139 offset:25216\n"
    "s_bfe_u32 s94, s2, 0x20001\n"
    "v_exp_f32 v53, v53\n"
    "v_exp_f32 v54, v54\n"
    "v_exp_f32 v55, v55\n"
    "v_exp_f32 v56, v56\n"
    "v_exp_f32 v57, v57\n"
    "v_exp_f32 v58, v58\n"
    "v_exp_f32 v59, v59\n"
    "v_exp_f32 v60, v60\n"
    "s_waitcnt lgkmcnt(3)\n"
    "v_smfmac_f32_32x32x32_bf16 v[80:95], v[128:131], v[48:55], v136\n"
    "buffer_load_dwordx4 v[244:247], v7, s[16:19], s46 offen sc1\n"
    "buffer_load_dwordx4 v[248:251], v8, s[16:19], s47 offen sc1\n"
    "buffer_load_dwordx4 v[252:255], v9, s[16:19], s46 offen offset:2048 sc1\n"
    "s_and_b32 s95, s2, 1\n"
    "v_exp_f32 v61, v61\n"
    "v_exp_f32 v62, v62\n"
    "v_exp_f32 v63, v63\n"
    "v_exp_f32 v64, v64\n"
    "v_exp_f32 v65, v65\n"
    "s_waitcnt lgkmcnt(1)\n"
    "v_smfmac_f32_32x32x32_bf16 v[80:95], v[132:135], v[56:63], v136\n"
    "s_lshr_b32 s45, s2, 3\n"
    "s_lshl3_add_u32 s45, s45, s3\n"
    "s_lshl_b32 s45, s45, 7\n"
    "s_min_u32 s45, s45, kb_code_end-kb_epi17_%=\n"
    "v_mov_b32 v147, s45\n"
    "s_getpc_b64 s[36:37]\n"
    "kb_pc23_%=:\n"
    "s_add_u32 s36, s36, kb_epi17_%=-kb_pc23_%=\n"
    "s_addc_u32 s37, s37, 0\n"
    "global_load_dword v147, v147, s[36:37]\n"
    "s_lshr_b32 s39, s2, 5\n"
    "v_exp_f32 v66, v66\n"
    "v_exp_f32 v67, v67\n"
    "v_exp_f32 v68, v68\n"
    "v_mfma_f32_32x32x16_bf16 v[16:31], v[122:125], v[112:115], v[16:31]\n"
    "s_lshl3_add_u32 s95, s95, s39\n"
    "s_lshl2_add_u32 s96, s94, s88\n"
    "s_lshl_b32 s97, s89, 5\n"
    "v_exp_f32 v69, v69\n"
    "v_exp_f32 v70, v70\n"
    "v_exp_f32 v71, v71\n"
    "v_exp_f32 v72, v72\n"
    "v_exp_f32 v73, v73\n"
    "v_smfmac_f32_32x32x32_bf16 v[96:111], v[128:131], v[64:71], v136\n"
    "s_mov_b64 exec, s[42:43]\n"
    "ds_read_b128 v[128:131], v138 offset:18432\n"
    "s_mov_b64 exec, -1\n"
    "v_exp_f32 v74, v74\n"
    "v_exp_f32 v75, v75\n"
    "v_exp_f32 v76, v76\n"
    "v_exp_f32 v77, v77\n"
    "v_mfma_f32_32x32x16_bf16 v[32:47], v[122:125], v[116:119], v[32:47]\n"
    "ds_read_b128 v[122:125], v137 offset:23040\n"
    "s_lshl2_add_u32 s97, s3, s97\n"
    "s_lshl_b32 s97, s97, 10\n"
    "v_exp_f32 v78, v78\n"
    "v_exp_f32 v79, v79\n"
    "v_exp_f32 v16, v16\n"
    "v_exp_f32 v17, v17\n"
    "v_smfmac_f32_32x32x32_bf16 v[96:111], v[132:135], v[72:79], v136\n"
    "s_mov_b64 exec, s[42:43]\n"
    "ds_read_b128 v[132:135], v2 offset:18432\n"
    "s_mov_b64 exec, -1\n"
    "v_exp_f32 v18, v18\n"
    "v_exp_f32 v19, v19\n"
    "v_exp_f32 v20, v20\n"
    "s_waitcnt lgkmcnt(3)\n"
    "v_mfma_f32_32x32x4_2b_bf16 v[48:79], v[126:127], v[120:121], 0\n"
    "ds_read_b32 v126, v139 offset:29824\n"
    "s_add_u32 s97, s97, 0xb80000\n"
    "v_exp_f32 v21, v21\n"
    "v_exp_f32 v22, v22\n"
    "v_exp_f32 v23, v23\n"
    "v_exp_f32 v24, v24\n"
    "v_exp_f32 v25, v25\n"
    "v_exp_f32 v26, v26\n"
    "v_exp_f32 v27, v27\n"
    "v_exp_f32 v28, v28\n"
    "s_waitcnt lgkmcnt(3)\n"
    "v_smfmac_f32_32x32x32_bf16 v[80:95], v[128:131], v[16:23], v136\n"
    "buffer_load_dwordx2 v[140:141], v4, s[28:31], 0 offen offset:0\n"
    "buffer_load_dwordx2 v[142:143], v4, s[28:31], 0 offen offset:32\n"
    "buffer_load_dwordx2 v[144:145], v4, s[28:31], 0 offen offset:64\n"
    "buffer_load_dword v146, v5, s[16:19], 0 offen sc1\n"
    "s_mul_i32 s98, s3, 0x2400\n"
    "v_exp_f32 v29, v29\n"
    "v_exp_f32 v30, v30\n"
    "v_exp_f32 v31, v31\n"
    "v_exp_f32 v32, v32\n"
    "v_exp_f32 v33, v33\n"
    "s_waitcnt lgkmcnt(1)\n"
    "v_smfmac_f32_32x32x32_bf16 v[80:95], v[132:135], v[24:31], v136\n"
    "s_lshl_b32 s39, s73, 15\n"
    "s_lshl_b32 s51, s72, 9\n"
    "s_add_u32 s39, s39, s51\n"
    "v_exp_f32 v34, v34\n"
    "v_exp_f32 v35, v35\n"
    "v_exp_f32 v36, v36\n"
    "v_mfma_f32_32x32x16_bf16 v[48:63], v[122:125], v[112:115], v[48:63]\n"
    "s_add_u32 s39, s39, 0xb80000\n"
    "v_bfe_u32 v6, v0, 4, 1\n"
    "v_lshl_add_u32 v6, v6, 14, s39\n"
    "v_exp_f32 v37, v37\n"
    "v_exp_f32 v38, v38\n"
    "v_exp_f32 v39, v39\n"
    "v_exp_f32 v40, v40\n"
    "v_exp_f32 v41, v41\n"
    "v_smfmac_f32_32x32x32_bf16 v[96:111], v[128:131], v[32:39], v136\n"
    "s_mov_b64 exec, s[42:43]\n"
    "ds_read_b128 v[128:131], v138 offset:23040\n"
    "s_mov_b64 exec, -1\n"
    "v_exp_f32 v42, v42\n"
    "v_exp_f32 v43, v43\n"
    "v_exp_f32 v44, v44\n"
    "v_exp_f32 v45, v45\n"
    "v_mfma_f32_32x32x16_bf16 v[64:79], v[122:125], v[116:119], v[64:79]\n"
    "ds_read_b128 v[122:125], v137 offset:27648\n"
    "v_lshrrev_b32 v11, 5, v0\n"
    "v_lshl_add_u32 v6, v11, 8, v6\n"
    "v_exp_f32 v46, v46\n"
    "v_exp_f32 v47, v47\n"
    "v_exp_f32 v48, v48\n"
    "v_exp_f32 v49, v49\n"
    "v_smfmac_f32_32x32x32_bf16 v[96:111], v[132:135], v[40:47], v136\n"
    "s_mov_b64 exec, s[42:43]\n"
    "ds_read_b128 v[132:135], v2 offset:23040\n"
    "s_mov_b64 exec, -1\n"
    "v_exp_f32 v50, v50\n"
    "v_exp_f32 v51, v51\n"
    "v_exp_f32 v52, v52\n"
    "s_waitcnt lgkmcnt(3)\n"
    "v_mfma_f32_32x32x4_2b_bf16 v[16:47], v[126:127], v[120:121], 0\n"
    "ds_read_b32 v126, v139 offset:34432\n"
    "v_and_b32 v11, 15, v0\n"
    "v_exp_f32 v53, v53\n"
    "v_exp_f32 v54, v54\n"
    "v_exp_f32 v55, v55\n"
    "v_exp_f32 v56, v56\n"
    "v_exp_f32 v57, v57\n"
    "v_exp_f32 v58, v58\n"
    "v_exp_f32 v59, v59\n"
    "v_exp_f32 v60, v60\n"
    "s_waitcnt lgkmcnt(3)\n"
    "v_smfmac_f32_32x32x32_bf16 v[80:95], v[128:131], v[48:55], v136\n"
    "s_add_u32 s45, s75, 57344\n"
    "buffer_load_dwordx2 v[4:5], v1, s[16:19], s45 offen sc1\n"
    "buffer_load_dwordx2 v[8:9], v1, s[16:19], s45 offen offset:8 sc1\n"
    "v_lshl_add_u32 v6, v11, 4, v6\n"
    "v_exp_f32 v61, v61\n"
    "v_exp_f32 v62, v62\n"
    "v_exp_f32 v63, v63\n"
    "v_exp_f32 v64, v64\n"
    "v_exp_f32 v65, v65\n"
    "s_waitcnt lgkmcnt(1)\n"
    "v_smfmac_f32_32x32x32_bf16 v[80:95], v[132:135], v[56:63], v136\n"
    "s_lshl_b32 s39, s73, 7\n"
    "s_lshl_b32 s51, s72, 13\n"
    "s_add_u32 s39, s39, s51\n"
    "v_exp_f32 v66, v66\n"
    "v_exp_f32 v67, v67\n"
    "v_exp_f32 v68, v68\n"
    "v_mfma_f32_32x32x16_bf16 v[16:31], v[122:125], v[112:115], v[16:31]\n"
    "s_add_u32 s39, s39, 0xd80000\n"
    "v_lshl_add_u32 v13, v0, 2, s39\n"
    "v_mov_b32 v11, 0x80000000\n"
    "v_exp_f32 v69, v69\n"
    "v_exp_f32 v70, v70\n"
    "v_exp_f32 v71, v71\n"
    "v_exp_f32 v72, v72\n"
    "v_exp_f32 v73, v73\n"
    "v_smfmac_f32_32x32x32_bf16 v[96:111], v[128:131], v[64:71], v136\n"
    "s_mov_b64 exec, s[42:43]\n"
    "ds_read_b128 v[128:131], v138 offset:27648\n"
    "s_mov_b64 exec, -1\n"
    "v_exp_f32 v74, v74\n"
    "v_exp_f32 v75, v75\n"
    "v_exp_f32 v76, v76\n"
    "v_exp_f32 v77, v77\n"
    "v_mfma_f32_32x32x16_bf16 v[32:47], v[122:125], v[116:119], v[32:47]\n"
    "ds_read_b128 v[122:125], v137 offset:32256\n"
    "v_cmp_gt_u32 vcc, 32, v0\n"
    "s_nop 1\n"
    "v_cndmask_b32 v13, v11, v13, vcc\n"
    "v_exp_f32 v78, v78\n"
    "v_exp_f32 v79, v79\n"
    "v_exp_f32 v16, v16\n"
    "v_exp_f32 v17, v17\n"
    "v_smfmac_f32_32x32x32_bf16 v[96:111], v[132:135], v[72:79], v136\n"
    "s_mov_b64 exec, s[42:43]\n"
    "ds_read_b128 v[132:135], v2 offset:27648\n"
    "s_mov_b64 exec, -1\n"
    "v_exp_f32 v18, v18\n"
    "v_exp_f32 v19, v19\n"
    "v_exp_f32 v20, v20\n"
    "s_waitcnt lgkmcnt(3)\n"
    "v_mfma_f32_32x32x4_2b_bf16 v[48:79], v[126:127], v[120:121], 0\n"
    "v_exp_f32 v21, v21\n"
    "v_exp_f32 v22, v22\n"
    "v_exp_f32 v23, v23\n"
    "v_exp_f32 v24, v24\n"
    "v_exp_f32 v25, v25\n"
    "v_exp_f32 v26, v26\n"
    "v_exp_f32 v27, v27\n"
    "v_exp_f32 v28, v28\n"
    "s_waitcnt lgkmcnt(2)\n"
    "v_smfmac_f32_32x32x32_bf16 v[80:95], v[128:131], v[16:23], v136\n"
    "v_exp_f32 v29, v29\n"
    "v_exp_f32 v30, v30\n"
    "v_exp_f32 v31, v31\n"
    "v_exp_f32 v32, v32\n"
    "v_exp_f32 v33, v33\n"
    "s_waitcnt lgkmcnt(0)\n"
    "v_smfmac_f32_32x32x32_bf16 v[80:95], v[132:135], v[24:31], v136\n"
    "v_exp_f32 v34, v34\n"
    "v_exp_f32 v35, v35\n"
    "v_exp_f32 v36, v36\n"
    "v_mfma_f32_32x32x16_bf16 v[48:63], v[122:125], v[112:115], v[48:63]\n"
    "v_exp_f32 v37, v37\n"
    "v_exp_f32 v38, v38\n"
    "v_exp_f32 v39, v39\n"
    "v_exp_f32 v40, v40\n"
    "v_exp_f32 v41, v41\n"
    "v_smfmac_f32_32x32x32_bf16 v[96:111], v[128:131], v[32:39], v136\n"
    "s_mov_b64 exec, s[42:43]\n"
    "ds_read_b128 v[128:131], v138 offset:32256\n"
    "s_mov_b64 exec, -1\n"
    "v_exp_f32 v42, v42\n"
    "v_exp_f32 v43, v43\n"
    "v_exp_f32 v44, v44\n"
    "v_exp_f32 v45, v45\n"
    "v_mfma_f32_32x32x16_bf16 v[64:79], v[122:125], v[116:119], v[64:79]\n"
    "v_exp_f32 v46, v46\n"
    "v_exp_f32 v47, v47\n"
    "v_exp_f32 v48, v48\n"
    "v_exp_f32 v49, v49\n"
    "v_smfmac_f32_32x32x32_bf16 v[96:111], v[132:135], v[40:47], v136\n"
    "s_mov_b64 exec, s[42:43]\n"
    "ds_read_b128 v[132:135], v2 offset:32256\n"
    "s_mov_b64 exec, -1\n"
    "s_setprio 0\n"
    "v_exp_f32 v50, v50\n"
    "v_exp_f32 v51, v51\n"
    "v_exp_f32 v52, v52\n"
    "v_exp_f32 v53, v53\n"
    "v_exp_f32 v54, v54\n"
    "v_exp_f32 v55, v55\n"
    "v_exp_f32 v56, v56\n"
    "v_exp_f32 v57, v57\n"
    "v_exp_f32 v58, v58\n"
    "v_exp_f32 v59, v59\n"
    "v_exp_f32 v60, v60\n"
    "s_waitcnt lgkmcnt(1)\n"
    "v_smfmac_f32_32x32x32_bf16 v[80:95], v[128:131], v[48:55], v136\n"
    "v_exp_f32 v61, v61\n"
    "v_exp_f32 v62, v62\n"
    "v_exp_f32 v63, v63\n"
    "v_exp_f32 v64, v64\n"
    "v_exp_f32 v65, v65\n"
    "s_waitcnt lgkmcnt(0)\n"
    "v_smfmac_f32_32x32x32_bf16 v[80:95], v[132:135], v[56:63], v136\n"
    "v_exp_f32 v66, v66\n"
    "v_exp_f32 v67, v67\n"
    "v_exp_f32 v68, v68\n"
    "v_exp_f32 v69, v69\n"
    "v_exp_f32 v70, v70\n"
    "v_exp_f32 v71, v71\n"
    "v_exp_f32 v72, v72\n"
    "v_exp_f32 v73, v73\n"
    "v_smfmac_f32_32x32x32_bf16 v[96:111], v[128:131], v[64:71], v136\n"
    "v_exp_f32 v74, v74\n"
    "v_exp_f32 v75, v75\n"
    "v_exp_f32 v76, v76\n"
    "v_exp_f32 v77, v77\n"
    "v_exp_f32 v78, v78\n"
    "v_exp_f32 v79, v79\n"
    "s_nop 1\n"
    "v_smfmac_f32_32x32x32_bf16 v[96:111], v[132:135], v[72:79], v136\n"
    "s_waitcnt vmcnt(0)\n"
    "s_mov_b32 s48, 0\n"
    "kb_fa24_%=:\n"
    "v_xor_b32 v8, s60, v8\n"
    "v_xor_b32 v9, s60, v9\n"
    "v_xor_b32 v4, s60, v4\n"
    "v_xor_b32 v5, s60, v5\n"
    "v_or3_b32 v8, v8, v9, v4\n"
    "v_or_b32 v8, v8, v5\n"
    "v_cmp_ne_u32 vcc, 0, v8\n"
    "s_cbranch_vccnz kb_fb25_%=\n"
    "kb_fa24_%=x:\n"
    "kb_epi17_%=:\n"
    "s_nop 4\n"
    "v_maximum3_f32 v16, |v80|, |v81|, |v82|\n"
    "v_maximum3_f32 v16, |v83|, |v84|, v16\n"
    "v_maximum3_f32 v16, |v85|, |v86|, v16\n"
    "v_maximum3_f32 v16, |v87|, |v88|, v16\n"
    "v_maximum3_f32 v16, |v89|, |v90|, v16\n"
    "v_mov_b32 v17, 0x5d800000\n"
    "v_mov_b32 v18, 0x21800000\n"
    "v_xor_b32 v24, 0x24000, v3\n"
    "v_lshrrev_b32 v25, 2, v1\n"
    "v_and_b32 v25, 0x1c, v25\n"
    "s_lshr_b32 s45, s75, 8\n"
    "v_mov_b32 v26, s45\n"
    "ds_write_b128 v3, v[96:99] offset:0\n"
    "ds_write_b128 v3, v[100:103] offset:1024\n"
    "ds_write_b128 v3, v[104:107] offset:2048\n"
    "v_maximum3_f32 v16, |v96|, |v97|, v16\n"
    "v_maximum3_f32 v16, |v98|, |v99|, v16\n"
    "v_maximum3_f32 v16, |v100|, |v101|, v16\n"
    "v_maximum3_f32 v16, |v102|, |v103|, v16\n"
    "v_maximum3_f32 v16, |v104|, |v105|, v16\n"
    "v_maximum3_f32 v19, v90, v106, v106\n"
    "v_min_f32 v20, v90, v106\n"
    "v_cmp_ngt_f32 vcc, 0x7f800000, v16\n"
    "v_cmp_nle_f32 s[46:47], v19, v17\n"
    "v_cmp_nge_f32 s[44:45], v20, v18\n"
    "s_or_b64 vcc, vcc, s[46:47]\n"
    "s_cmp_eq_u32 s74, 0\n"
    "s_cselect_b32 s44, s44, 0\n"
    "s_or_b32 s46, vcc_lo, vcc_hi\n"
    "s_or_b32 s46, s46, s44\n"
    "s_cmp_eq_u32 s41, 0\n"
    "s_cselect_b32 s46, s46, 0\n"
    "v_mov_b32 v16, s46\n"
    "ds_write_b32 v26, v16 offset:16384\n"
    "s_waitcnt lgkmcnt(0)\n"
    "s_barrier\n"
    "ds_read_b32 v16, v25 offset:16384\n"
    "ds_read_b128 v[104:107], v24 offset:2048\n"
    "ds_read_b128 v[96:99], v24\n"
    "ds_read_b128 v[100:103], v24 offset:1024\n"
    "s_waitcnt lgkmcnt(2)\n"
    "v_cmp_ne_u32 vcc, 0, v16\n"
    "s_cbranch_vccnz kb_redo26_%=\n"
    "v_add_f32 v16, v90, v106\n"
    "v_add_f32 v88, v88, v104\n"
    "v_mov_b32 v17, v16\n"
    "v_add_f32 v89, v89, v105\n"
    "s_nop 0\n"
    "v_permlane32_swap_b32 v16, v17\n"
    "v_rcp_f32 v16, v16\n"
    "s_waitcnt lgkmcnt(0)\n"
    "v_pk_add_f32 v[80:81], v[80:81], v[96:97]\n"
    "v_pk_add_f32 v[82:83], v[82:83], v[98:99]\n"
    "v_pk_add_f32 v[84:85], v[84:85], v[100:101]\n"
    "v_pk_add_f32 v[86:87], v[86:87], v[102:103]\n"
    "v_pk_mul_f32 v[80:81], v[80:81], v[16:17] op_sel_hi:[1,0]\n"
    "v_pk_mul_f32 v[82:83], v[82:83], v[16:17] op_sel_hi:[1,0]\n"
    "v_pk_mul_f32 v[84:85], v[84:85], v[16:17] op_sel_hi:[1,0]\n"
    "v_pk_mul_f32 v[86:87], v[86:87], v[16:17] op_sel_hi:[1,0]\n"
    "v_pk_mul_f32 v[88:89], v[88:89], v[16:17] op_sel_hi:[1,0]\n"
    "v_cvt_pk_bf16_f32 v96, v80, v81\n"
    "v_cvt_pk_bf16_f32 v97, v82, v83\n"
    "v_cvt_pk_bf16_f32 v98, v84, v85\n"
    "v_cvt_pk_bf16_f32 v99, v86, v87\n"
    "v_cvt_pk_bf16_f32 v100, v88, v89\n"
    "buffer_store_dwordx4 v[96:99], v6, s[16:19], 0 offen sc1\n"
    "buffer_store_dword v100, v13, s[16:19], 0 offen sc1\n"
    "s_lshl_b32 s39, s3, 15\n"
    "s_lshl_b32 s51, s89, 7\n"
    "s_add_u32 s39, s39, s51\n"
    "s_add_u32 s39, s39, 0xd80000\n"
    "v_and_b32 v4, 60, v0\n"
    "v_add_u32 v4, s39, v4\n"
    "v_and_b32 v11, 3, v0\n"
    "v_lshl_add_u32 v4, v11, 13, v4\n"
    "s_add_u32 s43, s75, 0xc000\n"
    "s_and_b32 s39, s3, 3\n"
    "s_lshr_b32 s51, s3, 2\n"
    "s_mul_i32 s51, s51, 18\n"
    "s_lshl2_add_u32 s39, s39, s51\n"
    "s_lshl_b32 s39, s39, 10\n"
    "s_add_u32 s39, s39, 0x1b000\n"
    "v_add_u32 v7, s39, v1\n"
    "s_lshl_b32 s39, s3, 8\n"
    "s_add_u32 s39, s39, 0x1f000\n"
    "v_lshl_add_u32 v8, v0, 2, s39\n"
    "s_mul_i32 s39, s90, 0x6c00\n"
    "s_add_u32 s39, s39, 0x1b000\n"
    "v_add_u32 v9, s39, v1\n"
    "s_mul_i32 s39, s90, 0x2400\n"
    "s_sub_u32 s39, 0x1f800, s39\n"
    "v_add_u32 v2, s39, v1\n"
    "s_mov_b32 s44, 0x8400\n"
    "s_cmp_eq_u32 s91, 3\n"
    "s_cselect_b32 s44, 0x8000, s44\n"
    "s_lshl_b32 s39, s90, 8\n"
    "s_add_u32 s44, s44, s39\n"
    "s_lshl_b32 s39, s90, 9\n"
    "s_add_u32 s39, s39, s44\n"
    "v_lshl_add_u32 v16, v0, 2, s39\n"
    "v_xor_b32 v17, 0x200, v16\n"
    "s_lshl_b32 s39, s90, 9\n"
    "s_add_u32 s39, s39, 0x8000\n"
    "v_lshl_add_u32 v18, v0, 2, s39\n"
    "s_mul_i32 s39, s89, 0x9000\n"
    "s_mul_i32 s51, s90, 0x4800\n"
    "s_add_u32 s39, s39, s51\n"
    "s_lshl_b32 s51, s92, 9\n"
    "s_add_u32 s39, s39, s51\n"
    "s_add_u32 s39, s39, 0xdc0000\n"
    "v_lshrrev_b32 v19, 5, v0\n"
    "v_lshl_add_u32 v19, v19, 8, s39\n"
    "v_and_b32 v11, 15, v0\n"
    "v_lshl_add_u32 v19, v11, 4, v19\n"
    "v_bfe_u32 v11, v0, 4, 1\n"
    "v_lshl_add_u32 v19, v11, 3, v19\n"
    "s_cmp_eq_u32 s91, 3\n"
    "s_cselect_b32 s39, 0x80000000, 0\n"
    "v_add_u32 v20, s39, v19\n"
    "s_lshl_b32 s39, s89, 12\n"
    "s_lshl_b32 s51, s90, 11\n"
    "s_add_u32 s39, s39, s51\n"
    "s_lshl2_add_u32 s51, s88, s91\n"
    "s_lshl_b32 s51, s51, 7\n"
    "s_add_u32 s39, s39, s51\n"
    "s_add_u32 s39, s39, 0x1000000\n"
    "v_lshl_add_u32 v21, v0, 3, s39\n"
    "v_mov_b32 v11, 0x80000000\n"
    "v_cmp_gt_u32 vcc, 16, v0\n"
    "s_nop 1\n"
    "v_cndmask_b32 v21, v11, v21, vcc\n"
    "s_cmp_eq_u32 s91, 3\n"
    "s_cselect_b32 s45, 0, 0x3f800000\n"
    "s_mul_i32 s38, s96, 0xa2000\n"
    "s_add_u32 s38, s38, 0x1040000\n"
    "s_add_u32 s20, s4, s38\n"
    "s_addc_u32 s21, s5, 0\n"
    "s_and_b32 s21, s21, 0xffff\n"
    "s_mov_b32 s22, 663552\n"
    "s_mov_b32 s23, 0x20000\n"
    "s_mov_b32 s71, 19255296\n"
    "s_mov_b32 s48, 0\n"
    "s_waitcnt vmcnt(0)\n"
    "buffer_load_dwordx4 v[24:27], v1, s[16:19], s97 offen offset:0 sc1\n"
    "buffer_load_dwordx4 v[28:31], v1, s[16:19], s97 offen offset:1024 sc1\n"
    "buffer_load_dwordx4 v[32:35], v1, s[16:19], s97 offen offset:2048 sc1\n"
    "buffer_load_dwordx4 v[36:39], v1, s[16:19], s97 offen offset:3072 sc1\n"
    "buffer_load_dword v40, v4, s[16:19], 0 offen sc1\n"
    "buffer_load_dword v41, v4, s[16:19], 0 offen offset:64 sc1\n"
    "buffer_load_dwordx4 v[44:47], v1, s[16:19], s43 offen sc1\n"
    "s_lshl2_add_u32 s39, s89, s88\n"
    "s_lshl_b32 s39, s39, 7\n"
    "s_add_u32 s39, s39, 0x10000\n"
    "v_mov_b32 v13, s39\n"
    "v_mov_b32 v11, 0x80000000\n"
    "s_xor_b32 s39, s3, 6\n"
    "v_add_u32 v12, s39, v0\n"
    "v_cmp_eq_u32 vcc, 0, v12\n"
    "s_nop 1\n"
    "v_cndmask_b32 v13, v11, v13, vcc\n"
    "s_waitcnt vmcnt(0)\n"
    "kb_bk30_%=:\n"
    "v_max3_u32 v128, v24, v25, v26\n"
    "v_max3_u32 v128, v27, v28, v128\n"
    "v_max3_u32 v128, v29, v30, v128\n"
    "v_max3_u32 v128, v31, v32, v128\n"
    "v_max3_u32 v128, v33, v34, v128\n"
    "v_max3_u32 v128, v35, v36, v128\n"
    "v_max3_u32 v128, v37, v38, v128\n"
    "v_max3_u32 v128, v39, v40, v128\n"
    "v_max_u32 v128, v41, v128\n"
    "v_cmp_eq_u32 vcc, -1, v128\n"
    "v_xor_b32 v44, s60, v44\n"
    "v_xor_b32 v45, s60, v45\n"
    "v_xor_b32 v46, s60, v46\n"
    "v_xor_b32 v47, s60, v47\n"
    "v_or3_b32 v44, v44, v45, v46\n"
    "v_or_b32 v44, v44, v47\n"
    "v_cmp_ne_u32 s[46:47], 0, v44\n"
    "s_or_b64 vcc, vcc, s[46:47]\n"
    "s_cbranch_vccnz kb_rt31_%=\n"
    "kb_bk30_%=x:\n"
    "ds_write_b128 v7, v[24:27] offset:0\n"
    "ds_write_b128 v7, v[28:31] offset:1024\n"
    "ds_write_b128 v7, v[32:35] offset:2048\n"
    "ds_write_b128 v7, v[36:39] offset:3072\n"
    "ds_write_b32 v8, v40\n"
    "ds_write_b32 v8, v41 offset:18432\n"
    "s_waitcnt lgkmcnt(0)\n"
    "s_barrier\n"
    "ds_read_b128 v[24:27], v9 offset:0\n"
    "ds_read_b128 v[28:31], v2 offset:0\n"
    "ds_read_b128 v[32:35], v9 offset:1024\n"
    "ds_read_b128 v[36:39], v2 offset:1024\n"
    "ds_read_b128 v[40:43], v9 offset:2048\n"
    "ds_read_b128 v[44:47], v2 offset:2048\n"
    "ds_read_b128 v[48:51], v9 offset:3072\n"
    "ds_read_b128 v[52:55], v2 offset:3072\n"
    "ds_read_b128 v[56:59], v9 offset:4096\n"
    "ds_read_b128 v[60:63], v2 offset:4096\n"
    "ds_read_b128 v[64:67], v9 offset:5120\n"
    "ds_read_b128 v[68:71], v2 offset:5120\n"
    "ds_read_b128 v[72:75], v9 offset:6144\n"
    "ds_read_b128 v[76:79], v2 offset:6144\n"
    "ds_read_b128 v[80:83], v9 offset:7168\n"
    "ds_read_b128 v[84:87], v2 offset:7168\n"
    "ds_read_b128 v[88:91], v9 offset:8192\n"
    "ds_read_b128 v[92:95], v2 offset:8192\n"
    "s_cmp_ge_u32 s91, 2\n"
    "s_cbranch_scc1 kb_two32_%=\n"
    "s_waitcnt lgkmcnt(15)\n"
    "v_mfma_f32_16x16x32_bf16 v[108:111], v[148:151], v[28:31], 0\n"
    "s_add_u32 m0, s98, 36864\n"
    "s_add_u32 s40, s98, 0\n"
    "buffer_load_dwordx4 v1, s[20:23], s40 offen offset:0 sc1 lds\n"
    "v_mfma_f32_16x16x32_bf16 v[104:107], v[148:151], v[24:27], 0\n"
    "v_mfma_f32_16x16x32_bf16 v[116:119], v[152:155], v[28:31], 0\n"
    "v_mfma_f32_16x16x32_bf16 v[112:115], v[152:155], v[24:27], 0\n"
    "buffer_load_dwordx4 v1, s[20:23], s40 offen offset:1024 sc1 lds\n"
    "v_mfma_f32_16x16x32_bf16 v[124:127], v[156:159], v[28:31], 0\n"
    "v_mfma_f32_16x16x32_bf16 v[120:123], v[156:159], v[24:27], 0\n"
    "s_waitcnt lgkmcnt(14)\n"
    "v_mfma_f32_16x16x32_bf16 v[108:111], v[160:163], v[36:39], v[108:111]\n"
    "buffer_load_dwordx4 v1, s[20:23], s40 offen offset:2048 sc1 lds\n"
    "v_mfma_f32_16x16x32_bf16 v[104:107], v[160:163], v[32:35], v[104:107]\n"
    "v_mfma_f32_16x16x32_bf16 v[116:119], v[164:167], v[36:39], v[116:119]\n"
    "v_mfma_f32_16x16x32_bf16 v[112:115], v[164:167], v[32:35], v[112:115]\n"
    "buffer_load_dwordx4 v1, s[20:23], s40 offen offset:3072 sc1 lds\n"
    "v_mfma_f32_16x16x32_bf16 v[124:127], v[168:171], v[36:39], v[124:127]\n"
    "v_mfma_f32_16x16x32_bf16 v[120:123], v[168:171], v[32:35], v[120:123]\n"
    "s_waitcnt lgkmcnt(12)\n"
    "v_mfma_f32_16x16x32_bf16 v[108:111], v[172:175], v[44:47], v[108:111]\n"
    "s_add_u32 m0, s98, 40960\n"
    "s_add_u32 s40, s98, 4096\n"
    "buffer_load_dwordx4 v1, s[20:23], s40 offen offset:0 sc1 lds\n"
    "v_mfma_f32_16x16x32_bf16 v[104:107], v[172:175], v[40:43], v[104:107]\n"
    "v_mfma_f32_16x16x32_bf16 v[116:119], v[176:179], v[44:47], v[116:119]\n"
    "v_mfma_f32_16x16x32_bf16 v[112:115], v[176:179], v[40:43], v[112:115]\n"
    "buffer_load_dwordx4 v1, s[20:23], s40 offen offset:1024 sc1 lds\n"
    "v_mfma_f32_16x16x32_bf16 v[124:127], v[180:183], v[44:47], v[124:127]\n"
    "v_mfma_f32_16x16x32_bf16 v[120:123], v[180:183], v[40:43], v[120:123]\n"
    "s_waitcnt lgkmcnt(10)\n"
    "v_mfma_f32_16x16x32_bf16 v[108:111], v[184:187], v[52:55], v[108:111]\n"
    "buffer_load_dwordx4 v1, s[20:23], s40 offen offset:2048 sc1 lds\n"
    "v_mfma_f32_16x16x32_bf16 v[104:107], v[184:187], v[48:51], v[104:107]\n"
    "v_mfma_f32_16x16x32_bf16 v[116:119], v[188:191], v[52:55], v[116:119]\n"
    "v_mfma_f32_16x16x32_bf16 v[112:115], v[188:191], v[48:51], v[112:115]\n"
    "buffer_load_dwordx4 v1, s[20:23], s40 offen offset:3072 sc1 lds\n"
    "v_mfma_f32_16x16x32_bf16 v[124:127], v[192:195], v[52:55], v[124:127]\n"
    "v_mfma_f32_16x16x32_bf16 v[120:123], v[192:195], v[48:51], v[120:123]\n"
    "s_waitcnt lgkmcnt(8)\n"
    "v_mfma_f32_16x16x32_bf16 v[108:111], v[196:199], v[60:63], v[108:111]\n"
    "s_add_u32 m0, s98, 45056\n"
    "s_add_u32 s40, s98, 8192\n"
    "buffer_load_dwordx4 v1, s[20:23], s40 offen offset:0 sc1 lds\n"
    "v_mfma_f32_16x16x32_bf16 v[104:107], v[196:199], v[56:59], v[104:107]\n"
    "v_mfma_f32_16x16x32_bf16 v[116:119], v[200:203], v[60:63], v[116:119]\n"
    "v_mfma_f32_16x16x32_bf16 v[112:115], v[200:203], v[56:59], v[112:115]\n"
    "v_mfma_f32_16x16x32_bf16 v[124:127], v[204:207], v[60:63], v[124:127]\n"
    "v_mfma_f32_16x16x32_bf16 v[120:123], v[204:207], v[56:59], v[120:123]\n"
    "s_waitcnt lgkmcnt(6)\n"
    "v_mfma_f32_16x16x32_bf16 v[108:111], v[208:211], v[68:71], v[108:111]\n"
    "v_mfma_f32_16x16x32_bf16 v[104:107], v[208:211], v[64:67], v[104:107]\n"
    "v_mfma_f32_16x16x32_bf16 v[116:119], v[212:215], v[68:71], v[116:119]\n"
    "v_mfma_f32_16x16x32_bf16 v[112:115], v[212:215], v[64:67], v[112:115]\n"
    "v_mfma_f32_16x16x32_bf16 v[124:127], v[216:219], v[68:71], v[124:127]\n"
    "v_mfma_f32_16x16x32_bf16 v[120:123], v[216:219], v[64:67], v[120:123]\n"
    "s_waitcnt lgkmcnt(4)\n"
    "v_mfma_f32_16x16x32_bf16 v[108:111], v[220:223], v[76:79], v[108:111]\n"
    "v_mfma_f32_16x16x32_bf16 v[104:107], v[220:223], v[72:75], v[104:107]\n"
    "v_mfma_f32_16x16x32_bf16 v[116:119], v[224:227], v[76:79], v[116:119]\n"
    "v_mfma_f32_16x16x32_bf16 v[112:115], v[224:227], v[72:75], v[112:115]\n"
    "v_mfma_f32_16x16x32_bf16 v[124:127], v[228:231], v[76:79], v[124:127]\n"
    "v_mfma_f32_16x16x32_bf16 v[120:123], v[228:231], v[72:75], v[120:123]\n"
    "s_waitcnt lgkmcnt(2)\n"
    "v_mfma_f32_16x16x32_bf16 v[108:111], v[232:235], v[84:87], v[108:111]\n"
    "v_mfma_f32_16x16x32_bf16 v[104:107], v[232:235], v[80:83], v[104:107]\n"
    "v_mfma_f32_16x16x32_bf16 v[116:119], v[236:239], v[84:87], v[116:119]\n"
    "v_mfma_f32_16x16x32_bf16 v[112:115], v[236:239], v[80:83], v[112:115]\n"
    "v_mfma_f32_16x16x32_bf16 v[124:127], v[240:243], v[84:87], v[124:127]\n"
    "v_mfma_f32_16x16x32_bf16 v[120:123], v[240:243], v[80:83], v[120:123]\n"
    "s_waitcnt lgkmcnt(0)\n"
    "v_mfma_f32_16x16x32_bf16 v[108:111], v[244:247], v[92:95], v[108:111]\n"
    "v_mfma_f32_16x16x32_bf16 v[104:107], v[244:247], v[88:91], v[104:107]\n"
    "v_mfma_f32_16x16x32_bf16 v[116:119], v[248:251], v[92:95], v[116:119]\n"
    "v_mfma_f32_16x16x32_bf16 v[112:115], v[248:251], v[88:91], v[112:115]\n"
    "v_mfma_f32_16x16x32_bf16 v[124:127], v[252:255], v[92:95], v[124:127]\n"
    "v_mfma_f32_16x16x32_bf16 v[120:123], v[252:255], v[88:91], v[120:123]\n"
    "v_lshlrev_b32 v24, 16, v140\n"
    "v_and_b32 v25, 0xffff0000, v140\n"
    "v_lshlrev_b32 v26, 16, v141\n"
    "v_and_b32 v27, 0xffff0000, v141\n"
    "v_lshlrev_b32 v28, 16, v142\n"
    "v_and_b32 v29, 0xffff0000, v142\n"
    "v_lshlrev_b32 v30, 16, v143\n"
    "v_and_b32 v31, 0xffff0000, v143\n"
    "v_lshlrev_b32 v32, 16, v144\n"
    "v_and_b32 v33, 0xffff0000, v144\n"
    "v_lshlrev_b32 v34, 16, v145\n"
    "v_and_b32 v35, 0xffff0000, v145\n"
    "v_xor_b32 v133, 0x1000, v3\n"
    "ds_write_b128 v3, v[108:111] offset:0\n"
    "ds_write_b128 v3, v[116:119] offset:1024\n"
    "ds_write_b128 v3, v[124:127] offset:2048\n"
    "s_waitcnt lgkmcnt(0)\n"
    "s_barrier\n"
    "s_setprio 1\n"
    "ds_read2st64_b32 v[128:129], v18 offset0:0 offset1:1\n"
    "ds_read_b128 v[108:111], v133 offset:0\n"
    "ds_read_b128 v[116:119], v133 offset:1024\n"
    "ds_read_b128 v[124:127], v133 offset:2048\n"
    "s_waitcnt lgkmcnt(3)\n"
    "v_add_f32 v128, v128, v129\n"
    "v_add_f32 v128, v128, v146\n"
    "v_cvt_pk_bf16_f32 v132, 0, v128\n"
    "s_waitcnt lgkmcnt(2)\n"
    "v_pk_add_f32 v[104:105], v[104:105], v[108:109]\n"
    "v_pk_add_f32 v[106:107], v[106:107], v[110:111]\n"
    "v_cvt_pk_bf16_f32 v104, 0, v104\n"
    "v_cvt_pk_bf16_f32 v105, 0, v105\n"
    "v_cvt_pk_bf16_f32 v106, 0, v106\n"
    "v_cvt_pk_bf16_f32 v107, 0, v107\n"
    "v_pk_add_f32 v[104:105], v[104:105], v[24:25]\n"
    "v_pk_add_f32 v[106:107], v[106:107], v[26:27]\n"
    "v_cvt_pk_bf16_f32 v242, v104, v105\n"
    "v_cvt_pk_bf16_f32 v243, v106, v107\n"
    "v_lshlrev_b32 v104, 16, v242\n"
    "v_and_b32 v105, 0xffff0000, v242\n"
    "v_lshlrev_b32 v106, 16, v243\n"
    "v_and_b32 v107, 0xffff0000, v243\n"
    "v_pk_add_f32 v[104:105], v[104:105], v[132:133] op_sel_hi:[1,0] neg_lo:[0,1] neg_hi:[0,1]\n"
    "v_pk_add_f32 v[106:107], v[106:107], v[132:133] op_sel_hi:[1,0] neg_lo:[0,1] neg_hi:[0,1]\n"
    "v_cvt_pk_bf16_f32 v108, v104, v105\n"
    "v_cvt_pk_bf16_f32 v109, v106, v107\n"
    "buffer_store_dwordx2 v[108:109], v19, s[16:19], 0 offen offset:0 sc1\n"
    "s_waitcnt lgkmcnt(1)\n"
    "v_pk_add_f32 v[112:113], v[112:113], v[116:117]\n"
    "v_pk_add_f32 v[114:115], v[114:115], v[118:119]\n"
    "v_cvt_pk_bf16_f32 v112, 0, v112\n"
    "v_cvt_pk_bf16_f32 v113, 0, v113\n"
    "v_cvt_pk_bf16_f32 v114, 0, v114\n"
    "v_cvt_pk_bf16_f32 v115, 0, v115\n"
    "v_pk_add_f32 v[112:113], v[112:113], v[28:29]\n"
    "v_pk_add_f32 v[114:115], v[114:115], v[30:31]\n"
    "v_cvt_pk_bf16_f32 v244, v112, v113\n"
    "v_cvt_pk_bf16_f32 v245, v114, v115\n"
    "v_lshlrev_b32 v112, 16, v244\n"
    "v_and_b32 v113, 0xffff0000, v244\n"
    "v_lshlrev_b32 v114, 16, v245\n"
    "v_and_b32 v115, 0xffff0000, v245\n"
    "v_pk_add_f32 v[112:113], v[112:113], v[132:133] op_sel_hi:[1,0] neg_lo:[0,1] neg_hi:[0,1]\n"
    "v_pk_add_f32 v[114:115], v[114:115], v[132:133] op_sel_hi:[1,0] neg_lo:[0,1] neg_hi:[0,1]\n"
    "v_cvt_pk_bf16_f32 v116, v112, v113\n"
    "v_cvt_pk_bf16_f32 v117, v114, v115\n"
    "buffer_store_dwordx2 v[116:117], v19, s[16:19], 0 offen offset:512 sc1\n"
    "s_waitcnt lgkmcnt(0)\n"
    "v_pk_add_f32 v[120:121], v[120:121], v[124:125]\n"
    "v_pk_add_f32 v[122:123], v[122:123], v[126:127]\n"
    "v_cvt_pk_bf16_f32 v120, 0, v120\n"
    "v_cvt_pk_bf16_f32 v121, 0, v121\n"
    "v_cvt_pk_bf16_f32 v122, 0, v122\n"
    "v_cvt_pk_bf16_f32 v123, 0, v123\n"
    "v_pk_add_f32 v[120:121], v[120:121], v[32:33]\n"
    "v_pk_add_f32 v[122:123], v[122:123], v[34:35]\n"
    "v_cvt_pk_bf16_f32 v246, v120, v121\n"
    "v_cvt_pk_bf16_f32 v247, v122, v123\n"
    "v_lshlrev_b32 v120, 16, v246\n"
    "v_and_b32 v121, 0xffff0000, v246\n"
    "v_lshlrev_b32 v122, 16, v247\n"
    "v_and_b32 v123, 0xffff0000, v247\n"
    "v_pk_add_f32 v[120:121], v[120:121], v[132:133] op_sel_hi:[1,0] neg_lo:[0,1] neg_hi:[0,1]\n"
    "v_pk_add_f32 v[122:123], v[122:123], v[132:133] op_sel_hi:[1,0] neg_lo:[0,1] neg_hi:[0,1]\n"
    "v_cvt_pk_bf16_f32 v124, v120, v121\n"
    "v_cvt_pk_bf16_f32 v125, v122, v123\n"
    "buffer_store_dwordx2 v[124:125], v19, s[16:19], 0 offen offset:1024 sc1\n"
    "s_setprio 0\n"
    "v_mov_b32 v248, v104\n"
    "v_mul_f32 v249, v104, v104\n"
    "v_add_f32 v248, v248, v105\n"
    "v_fmac_f32 v249, v105, v105\n"
    "v_add_f32 v248, v248, v106\n"
    "v_fmac_f32 v249, v106, v106\n"
    "v_add_f32 v248, v248, v107\n"
    "v_fmac_f32 v249, v107, v107\n"
    "v_add_f32 v248, v248, v112\n"
    "v_fmac_f32 v249, v112, v112\n"
    "v_add_f32 v248, v248, v113\n"
    "v_fmac_f32 v249, v113, v113\n"
    "v_add_f32 v248, v248, v114\n"
    "v_fmac_f32 v249, v114, v114\n"
    "v_add_f32 v248, v248, v115\n"
    "v_fmac_f32 v249, v115, v115\n"
    "v_add_f32 v248, v248, v120\n"
    "v_fmac_f32 v249, v120, v120\n"
    "v_add_f32 v248, v248, v121\n"
    "v_fmac_f32 v249, v121, v121\n"
    "v_add_f32 v248, v248, v122\n"
    "v_fmac_f32 v249, v122, v122\n"
    "v_add_f32 v248, v248, v123\n"
    "v_fmac_f32 v249, v123, v123\n"
    "v_mov_b32 v128, v248\n"
    "v_mov_b32 v129, v249\n"
    "s_nop 0\n"
    "v_permlane16_swap_b32 v248, v128\n"
    "v_permlane16_swap_b32 v249, v129\n"
    "v_add_f32 v248, v248, v128\n"
    "v_add_f32 v249, v249, v129\n"
    "v_mov_b32 v128, v248\n"
    "v_mov_b32 v129, v249\n"
    "s_nop 0\n"
    "v_permlane32_swap_b32 v248, v128\n"
    "v_permlane32_swap_b32 v249, v129\n"
    "v_add_f32 v248, v248, v128\n"
    "v_add_f32 v249, v249, v129\n"
    "s_waitcnt vmcnt(3)\n"
    "s_barrier\n"
    "s_lshl2_add_u32 s76, s95, s3\n"
    "s_mul_i32 s76, s76, 0x9000\n"
    "s_add_u32 s76, s76, 0xdc0000\n"
    "s_lshl_b32 s77, s3, 11\n"
    "s_mul_i32 s41, s95, 0x90\n"
    "s_mul_i32 s39, s96, 9\n"
    "s_add_u32 s41, s41, s39\n"
    "s_lshl_b32 s41, s41, 13\n"
    "s_add_u32 s41, s41, s77\n"
    "s_add_u32 s41, s41, 0x225d000\n"
    "v_add_u32 v250, 36864, v1\n"
    "v_add_u32 v252, 73728, v1\n"
    "v_add_u32 v251, 110592, v1\n"
    "s_mov_b32 s40, 1\n"
    "s_mov_b32 s44, 110592\n"
    "v_mov_b32 v237, s60\n"
    "s_mov_b32 s48, 0\n"
    "s_waitcnt vmcnt(0)\n"
    "s_add_u32 s42, s76, 0\n"
    "s_add_u32 s43, s76, 18432\n"
    "buffer_load_dwordx4 v[24:27], v1, s[16:19], s42 offen offset:0 sc1\n"
    "buffer_load_dwordx4 v[28:31], v1, s[16:19], s43 offen offset:0 sc1\n"
    "buffer_load_dwordx4 v[32:35], v1, s[16:19], s42 offen offset:1024 sc1\n"
    "buffer_load_dwordx4 v[36:39], v1, s[16:19], s43 offen offset:1024 sc1\n"
    "buffer_load_dwordx4 v[40:43], v1, s[16:19], s42 offen offset:2048 sc1\n"
    "buffer_load_dwordx4 v[44:47], v1, s[16:19], s43 offen offset:2048 sc1\n"
    "ds_read_b128 v[168:171], v250 offset:0\n"
    "ds_read_b128 v[172:175], v250 offset:1024\n"
    "ds_read_b128 v[176:179], v250 offset:2048\n"
    "ds_read_b128 v[180:183], v250 offset:18432\n"
    "ds_read_b128 v[184:187], v250 offset:19456\n"
    "ds_read_b128 v[188:191], v250 offset:20480\n"
    "s_add_u32 s42, s76, 3072\n"
    "s_add_u32 s43, s76, 21504\n"
    "buffer_load_dwordx4 v[48:51], v1, s[16:19], s42 offen offset:0 sc1\n"
    "buffer_load_dwordx4 v[52:55], v1, s[16:19], s43 offen offset:0 sc1\n"
    "buffer_load_dwordx4 v[56:59], v1, s[16:19], s42 offen offset:1024 sc1\n"
    "buffer_load_dwordx4 v[60:63], v1, s[16:19], s43 offen offset:1024 sc1\n"
    "buffer_load_dwordx4 v[64:67], v1, s[16:19], s42 offen offset:2048 sc1\n"
    "buffer_load_dwordx4 v[68:71], v1, s[16:19], s43 offen offset:2048 sc1\n"
    "v_lshrrev_b32 v2, 4, v0\n"
    "v_mul_u32_u24 v2, 20, v2\n"
    "v_sub_u32 v2, v0, v2\n"
    "v_mov_b32 v4, 0x3f80\n"
    "v_lshlrev_b32 v12, 4, v2\n"
    "v_lshlrev_b32 v4, v12, v4\n"
    "v_cmp_gt_u32 vcc, 2, v2\n"
    "s_nop 1\n"
    "v_cndmask_b32 v4, 0, v4, vcc\n"
    "v_subrev_u32 v2, 2, v2\n"
    "v_mov_b32 v5, 0x3f80\n"
    "v_lshlrev_b32 v12, 4, v2\n"
    "v_lshlrev_b32 v5, v12, v5\n"
    "v_cmp_gt_u32 vcc, 2, v2\n"
    "s_nop 1\n"
    "v_cndmask_b32 v5, 0, v5, vcc\n"
    "v_mov_b64 v[2:3], 0\n"
    "v_mov_b64 v[6:7], 0\n"
    "s_mov_b32 s39, 0x6000\n"
    "v_lshrrev_b32 v240, 5, v0\n"
    "v_lshl_add_u32 v240, v240, 8, s39\n"
    "v_and_b32 v12, 15, v0\n"
    "v_lshl_add_u32 v240, v12, 4, v240\n"
    "v_bfe_u32 v12, v0, 4, 1\n"
    "v_lshl_add_u32 v240, v12, 3, v240\n"
    "s_lshl_b32 s39, s3, 7\n"
    "s_add_u32 s39, s39, 0x8800\n"
    "v_and_b32 v241, 15, v0\n"
    "v_lshl_add_u32 v241, v241, 2, s39\n"
    "s_lshl2_add_u32 s79, s95, s88\n"
    "s_lshl2_add_u32 s79, s79, s3\n"
    "s_mul_i32 s78, s79, 0x6000\n"
    "s_add_u32 s78, s78, 0x40000\n"
    "s_mul_i32 s39, s79, 0xc00\n"
    "s_add_u32 s79, s39, 0x640000\n"
    "s_sub_u32 s39, s3, s94\n"
    "s_and_b32 s39, s39, 3\n"
    "s_lshl_b32 s39, s39, 10\n"
    "s_add_u32 s39, s79, s39\n"
    "s_sub_u32 s39, s39, 0x400\n"
    "s_cmp_eq_u32 s3, s94\n"
    "s_cselect_b32 s39, 0x80000000, s39\n"
    "v_add_u32 v254, s39, v1\n"
    "s_cmp_eq_u32 s3, s94\n"
    "s_cselect_b32 s39, s79, 0x80000000\n"
    "v_add_u32 v255, s39, v1\n"
    "s_lshl2_add_u32 s39, s95, s3\n"
    "s_mul_i32 s39, s39, 0x9000\n"
    "s_mul_i32 s51, s88, 0x120\n"
    "s_add_u32 s39, s39, s51\n"
    "s_lshl_b32 s51, s94, 6\n"
    "s_add_u32 s39, s39, s51\n"
    "v_and_b32 v8, 31, v0\n"
    "v_mul_u32_u24 v8, 0x480, v8\n"
    "v_add_u32 v8, s39, v8\n"
    "v_lshrrev_b32 v12, 5, v0\n"
    "v_lshl_add_u32 v8, v12, 4, v8\n"
    "s_lshl_b32 s39, s94, 6\n"
    "s_sub_u32 s39, 0x100, s39\n"
    "s_cmp_eq_u32 s3, s94\n"
    "s_cselect_b32 s39, s39, 0x80000000\n"
    "v_add_u32 v9, s39, v8\n"
    "s_mul_i32 s79, s94, 0x1800\n"
    "s_add_u32 s79, s78, s79\n"
    "s_mul_i32 s80, s94, 0x1800\n"
    "s_add_u32 s80, s78, s80\n"
    "s_add_u32 s80, s80, 0x800\n"
    "s_mul_i32 s81, s94, 0x1800\n"
    "s_add_u32 s81, s78, s81\n"
    "s_add_u32 s81, s81, 0x1000\n"
    "s_sub_u32 s82, s94, 1\n"
    "s_and_b32 s82, s82, 3\n"
    "s_mul_i32 s82, s82, 0x1800\n"
    "s_add_u32 s82, s78, s82\n"
    "s_sub_u32 s83, s94, 2\n"
    "s_and_b32 s83, s83, 3\n"
    "s_mul_i32 s83, s83, 0x1800\n"
    "s_add_u32 s83, s78, s83\n"
    "s_add_u32 s83, s83, 0x800\n"
    "s_sub_u32 s84, s94, 3\n"
    "s_and_b32 s84, s84, 3\n"
    "s_mul_i32 s84, s84, 0x1800\n"
    "s_add_u32 s84, s78, s84\n"
    "s_add_u32 s84, s84, 0x1000\n"
    "s_mov_b32 s24, s14\n"
    "s_and_b32 s25, s15, 0xffff\n"
    "s_mov_b32 s26, 2359296\n"
    "s_mov_b32 s27, 0x20000\n"
    "s_waitcnt vmcnt(6) lgkmcnt(3)\n"
    "kb_bk34_%=:\n"
    "v_max3_u32 v236, v24, v25, v26\n"
    "v_max3_u32 v236, v27, v28, v236\n"
    "v_max3_u32 v236, v29, v30, v236\n"
    "v_max3_u32 v236, v31, v32, v236\n"
    "v_max3_u32 v236, v33, v34, v236\n"
    "v_max3_u32 v236, v35, v36, v236\n"
    "v_max3_u32 v236, v37, v38, v236\n"
    "v_max3_u32 v236, v39, v40, v236\n"
    "v_max3_u32 v236, v41, v42, v236\n"
    "v_max3_u32 v236, v43, v44, v236\n"
    "v_max3_u32 v236, v45, v46, v236\n"
    "v_max_u32 v236, v47, v236\n"
    "v_cmp_eq_u32 vcc, -1, v236\n"
    "s_cbranch_vccnz kb_rt35_%=\n"
    "kb_bk34_%=x:\n"
    "v_mfma_f32_16x16x32_bf16 v[204:207], v[168:171], v[24:27], 0\n"
    "ds_read_b128 v[192:195], v252 offset:0\n"
    "v_mfma_f32_16x16x32_bf16 v[204:207], v[172:175], v[32:35], v[204:207]\n"
    "ds_read_b128 v[196:199], v252 offset:1024\n"
    "v_mfma_f32_16x16x32_bf16 v[204:207], v[176:179], v[40:43], v[204:207]\n"
    "ds_read_b128 v[200:203], v252 offset:2048\n"
    "v_mfma_f32_16x16x32_bf16 v[208:211], v[168:171], v[28:31], 0\n"
    "v_mfma_f32_16x16x32_bf16 v[208:211], v[172:175], v[36:39], v[208:211]\n"
    "s_add_u32 s42, s76, 6144\n"
    "s_add_u32 s43, s76, 24576\n"
    "buffer_load_dwordx4 v[72:75], v1, s[16:19], s42 offen offset:0 sc1\n"
    "v_mfma_f32_16x16x32_bf16 v[208:211], v[176:179], v[44:47], v[208:211]\n"
    "buffer_load_dwordx4 v[76:79], v1, s[16:19], s43 offen offset:0 sc1\n"
    "s_waitcnt lgkmcnt(3)\n"
    "v_mfma_f32_16x16x32_bf16 v[212:215], v[180:183], v[24:27], 0\n"
    "ds_read_b128 v[168:171], v252 offset:18432\n"
    "v_mfma_f32_16x16x32_bf16 v[212:215], v[184:187], v[32:35], v[212:215]\n"
    "ds_read_b128 v[172:175], v252 offset:19456\n"
    "v_mfma_f32_16x16x32_bf16 v[212:215], v[188:191], v[40:43], v[212:215]\n"
    "ds_read_b128 v[176:179], v252 offset:20480\n"
    "v_mfma_f32_16x16x32_bf16 v[216:219], v[180:183], v[28:31], 0\n"
    "v_mfma_f32_16x16x32_bf16 v[216:219], v[184:187], v[36:39], v[216:219]\n"
    "buffer_load_dwordx4 v[80:83], v1, s[16:19], s42 offen offset:1024 sc1\n"
    "v_mfma_f32_16x16x32_bf16 v[216:219], v[188:191], v[44:47], v[216:219]\n"
    "buffer_load_dwordx4 v[84:87], v1, s[16:19], s43 offen offset:1024 sc1\n"
    "s_waitcnt lgkmcnt(3)\n"
    "v_mfma_f32_16x16x32_bf16 v[220:223], v[192:195], v[24:27], 0\n"
    "ds_read_b128 v[180:183], v250 offset:3072\n"
    "v_mfma_f32_16x16x32_bf16 v[220:223], v[196:199], v[32:35], v[220:223]\n"
    "ds_read_b128 v[184:187], v250 offset:4096\n"
    "v_mfma_f32_16x16x32_bf16 v[220:223], v[200:203], v[40:43], v[220:223]\n"
    "ds_read_b128 v[188:191], v250 offset:5120\n"
    "v_mfma_f32_16x16x32_bf16 v[224:227], v[192:195], v[28:31], 0\n"
    "v_mfma_f32_16x16x32_bf16 v[224:227], v[196:199], v[36:39], v[224:227]\n"
    "buffer_load_dwordx4 v[88:91], v1, s[16:19], s42 offen offset:2048 sc1\n"
    "v_mfma_f32_16x16x32_bf16 v[224:227], v[200:203], v[44:47], v[224:227]\n"
    "buffer_load_dwordx4 v[92:95], v1, s[16:19], s43 offen offset:2048 sc1\n"
    "s_waitcnt lgkmcnt(3)\n"
    "v_mfma_f32_16x16x32_bf16 v[228:231], v[168:171], v[24:27], 0\n"
    "ds_read_b128 v[192:195], v250 offset:21504\n"
    "v_mfma_f32_16x16x32_bf16 v[228:231], v[172:175], v[32:35], v[228:231]\n"
    "ds_read_b128 v[196:199], v250 offset:22528\n"
    "v_mfma_f32_16x16x32_bf16 v[228:231], v[176:179], v[40:43], v[228:231]\n"
    "ds_read_b128 v[200:203], v250 offset:23552\n"
    "v_mfma_f32_16x16x32_bf16 v[232:235], v[168:171], v[28:31], 0\n"
    "v_mfma_f32_16x16x32_bf16 v[232:235], v[172:175], v[36:39], v[232:235]\n"
    "s_add_u32 s42, s76, 9216\n"
    "s_add_u32 s43, s76, 27648\n"
    "buffer_load_dwordx4 v[96:99], v1, s[16:19], s42 offen offset:0 sc1\n"
    "v_mfma_f32_16x16x32_bf16 v[232:235], v[176:179], v[44:47], v[232:235]\n"
    "buffer_load_dwordx4 v[100:103], v1, s[16:19], s43 offen offset:0 sc1\n"
    "s_waitcnt vmcnt(8) lgkmcnt(3)\n"
    "kb_bk36_%=:\n"
    "v_max3_u32 v236, v48, v49, v50\n"
    "v_max3_u32 v236, v51, v52, v236\n"
    "v_max3_u32 v236, v53, v54, v236\n"
    "v_max3_u32 v236, v55, v56, v236\n"
    "v_max3_u32 v236, v57, v58, v236\n"
    "v_max3_u32 v236, v59, v60, v236\n"
    "v_max3_u32 v236, v61, v62, v236\n"
    "v_max3_u32 v236, v63, v64, v236\n"
    "v_max3_u32 v236, v65, v66, v236\n"
    "v_max3_u32 v236, v67, v68, v236\n"
    "v_max3_u32 v236, v69, v70, v236\n"
    "v_max_u32 v236, v71, v236\n"
    "v_cmp_eq_u32 vcc, -1, v236\n"
    "s_cbranch_vccnz kb_rt37_%=\n"
    "kb_bk36_%=x:\n"
    "v_mfma_f32_16x16x32_bf16 v[204:207], v[180:183], v[48:51], v[204:207]\n"
    "ds_read_b128 v[168:171], v252 offset:3072\n"
    "v_mfma_f32_16x16x32_bf16 v[204:207], v[184:187], v[56:59], v[204:207]\n"
    "ds_read_b128 v[172:175], v252 offset:4096\n"
    "v_mfma_f32_16x16x32_bf16 v[204:207], v[188:191], v[64:67], v[204:207]\n"
    "ds_read_b128 v[176:179], v252 offset:5120\n"
    "v_mfma_f32_16x16x32_bf16 v[208:211], v[180:183], v[52:55], v[208:211]\n"
    "v_mfma_f32_16x16x32_bf16 v[208:211], v[184:187], v[60:63], v[208:211]\n"
    "buffer_load_dwordx4 v[104:107], v1, s[16:19], s42 offen offset:1024 sc1\n"
    "v_mfma_f32_16x16x32_bf16 v[208:211], v[188:191], v[68:71], v[208:211]\n"
    "buffer_load_dwordx4 v[108:111], v1, s[16:19], s43 offen offset:1024 sc1\n"
    "s_waitcnt lgkmcnt(3)\n"
    "v_mfma_f32_16x16x32_bf16 v[212:215], v[192:195], v[48:51], v[212:215]\n"
    "ds_read_b128 v[180:183], v252 offset:21504\n"
    "v_mfma_f32_16x16x32_bf16 v[212:215], v[196:199], v[56:59], v[212:215]\n"
    "ds_read_b128 v[184:187], v252 offset:22528\n"
    "v_mfma_f32_16x16x32_bf16 v[212:215], v[200:203], v[64:67], v[212:215]\n"
    "ds_read_b128 v[188:191], v252 offset:23552\n"
    "v_mfma_f32_16x16x32_bf16 v[216:219], v[192:195], v[52:55], v[216:219]\n"
    "v_mfma_f32_16x16x32_bf16 v[216:219], v[196:199], v[60:63], v[216:219]\n"
    "buffer_load_dwordx4 v[112:115], v1, s[16:19], s42 offen offset:2048 sc1\n"
    "v_mfma_f32_16x16x32_bf16 v[216:219], v[200:203], v[68:71], v[216:219]\n"
    "buffer_load_dwordx4 v[116:119], v1, s[16:19], s43 offen offset:2048 sc1\n"
    "s_waitcnt lgkmcnt(3)\n"
    "v_mfma_f32_16x16x32_bf16 v[220:223], v[168:171], v[48:51], v[220:223]\n"
    "ds_read_b128 v[192:195], v250 offset:6144\n"
    "v_mfma_f32_16x16x32_bf16 v[220:223], v[172:175], v[56:59], v[220:223]\n"
    "ds_read_b128 v[196:199], v250 offset:7168\n"
    "v_mfma_f32_16x16x32_bf16 v[220:223], v[176:179], v[64:67], v[220:223]\n"
    "ds_read_b128 v[200:203], v250 offset:8192\n"
    "v_mfma_f32_16x16x32_bf16 v[224:227], v[168:171], v[52:55], v[224:227]\n"
    "v_mfma_f32_16x16x32_bf16 v[224:227], v[172:175], v[60:63], v[224:227]\n"
    "s_add_u32 s42, s76, 12288\n"
    "s_add_u32 s43, s76, 30720\n"
    "buffer_load_dwordx4 v[120:123], v1, s[16:19], s42 offen offset:0 sc1\n"
    "v_mfma_f32_16x16x32_bf16 v[224:227], v[176:179], v[68:71], v[224:227]\n"
    "buffer_load_dwordx4 v[124:127], v1, s[16:19], s43 offen offset:0 sc1\n"
    "s_waitcnt lgkmcnt(3)\n"
    "v_mfma_f32_16x16x32_bf16 v[228:231], v[180:183], v[48:51], v[228:231]\n"
    "ds_read_b128 v[168:171], v250 offset:24576\n"
    "v_mfma_f32_16x16x32_bf16 v[228:231], v[184:187], v[56:59], v[228:231]\n"
    "ds_read_b128 v[172:175], v250 offset:25600\n"
    "v_mfma_f32_16x16x32_bf16 v[228:231], v[188:191], v[64:67], v[228:231]\n"
    "ds_read_b128 v[176:179], v250 offset:26624\n"
    "v_mfma_f32_16x16x32_bf16 v[232:235], v[180:183], v[52:55], v[232:235]\n"
    "v_mfma_f32_16x16x32_bf16 v[232:235], v[184:187], v[60:63], v[232:235]\n"
    "buffer_load_dwordx4 v[128:131], v1, s[16:19], s42 offen offset:1024 sc1\n"
    "v_mfma_f32_16x16x32_bf16 v[232:235], v[188:191], v[68:71], v[232:235]\n"
    "buffer_load_dwordx4 v[132:135], v1, s[16:19], s43 offen offset:1024 sc1\n"
    "s_waitcnt vmcnt(10) lgkmcnt(3)\n"
    "kb_bk38_%=:\n"
    "v_max3_u32 v236, v72, v73, v74\n"
    "v_max3_u32 v236, v75, v76, v236\n"
    "v_max3_u32 v236, v77, v78, v236\n"
    "v_max3_u32 v236, v79, v80, v236\n"
    "v_max3_u32 v236, v81, v82, v236\n"
    "v_max3_u32 v236, v83, v84, v236\n"
    "v_max3_u32 v236, v85, v86, v236\n"
    "v_max3_u32 v236, v87, v88, v236\n"
    "v_max3_u32 v236, v89, v90, v236\n"
    "v_max3_u32 v236, v91, v92, v236\n"
    "v_max3_u32 v236, v93, v94, v236\n"
    "v_max_u32 v236, v95, v236\n"
    "v_cmp_eq_u32 vcc, -1, v236\n"
    "s_cbranch_vccnz kb_rt39_%=\n"
    "kb_bk38_%=x:\n"
    "v_mfma_f32_16x16x32_bf16 v[204:207], v[192:195], v[72:75], v[204:207]\n"
    "ds_read_b128 v[180:183], v252 offset:6144\n"
    "v_mfma_f32_16x16x32_bf16 v[204:207], v[196:199], v[80:83], v[204:207]\n"
    "ds_read_b128 v[184:187], v252 offset:7168\n"
    "v_mfma_f32_16x16x32_bf16 v[204:207], v[200:203], v[88:91], v[204:207]\n"
    "ds_read_b128 v[188:191], v252 offset:8192\n"
    "v_mfma_f32_16x16x32_bf16 v[208:211], v[192:195], v[76:79], v[208:211]\n"
    "v_mfma_f32_16x16x32_bf16 v[208:211], v[196:199], v[84:87], v[208:211]\n"
    "buffer_load_dwordx4 v[136:139], v1, s[16:19], s42 offen offset:2048 sc1\n"
    "v_mfma_f32_16x16x32_bf16 v[208:211], v[200:203], v[92:95], v[208:211]\n"
    "buffer_load_dwordx4 v[140:143], v1, s[16:19], s43 offen offset:2048 sc1\n"
    "s_waitcnt lgkmcnt(3)\n"
    "v_mfma_f32_16x16x32_bf16 v[212:215], v[168:171], v[72:75], v[212:215]\n"
    "ds_read_b128 v[192:195], v252 offset:24576\n"
    "v_mfma_f32_16x16x32_bf16 v[212:215], v[172:175], v[80:83], v[212:215]\n"
    "ds_read_b128 v[196:199], v252 offset:25600\n"
    "v_mfma_f32_16x16x32_bf16 v[212:215], v[176:179], v[88:91], v[212:215]\n"
    "ds_read_b128 v[200:203], v252 offset:26624\n"
    "v_mfma_f32_16x16x32_bf16 v[216:219], v[168:171], v[76:79], v[216:219]\n"
    "v_mfma_f32_16x16x32_bf16 v[216:219], v[172:175], v[84:87], v[216:219]\n"
    "s_add_u32 s42, s76, 15360\n"
    "s_add_u32 s43, s76, 33792\n"
    "buffer_load_dwordx4 v[144:147], v1, s[16:19], s42 offen offset:0 sc1\n"
    "v_mfma_f32_16x16x32_bf16 v[216:219], v[176:179], v[92:95], v[216:219]\n"
    "buffer_load_dwordx4 v[148:151], v1, s[16:19], s43 offen offset:0 sc1\n"
    "s_waitcnt lgkmcnt(3)\n"
    "v_mfma_f32_16x16x32_bf16 v[220:223], v[180:183], v[72:75], v[220:223]\n"
    "ds_read_b128 v[168:171], v250 offset:9216\n"
    "v_mfma_f32_16x16x32_bf16 v[220:223], v[184:187], v[80:83], v[220:223]\n"
    "ds_read_b128 v[172:175], v250 offset:10240\n"
    "v_mfma_f32_16x16x32_bf16 v[220:223], v[188:191], v[88:91], v[220:223]\n"
    "ds_read_b128 v[176:179], v250 offset:11264\n"
    "v_mfma_f32_16x16x32_bf16 v[224:227], v[180:183], v[76:79], v[224:227]\n"
    "v_mfma_f32_16x16x32_bf16 v[224:227], v[184:187], v[84:87], v[224:227]\n"
    "buffer_load_dwordx4 v[152:155], v1, s[16:19], s42 offen offset:1024 sc1\n"
    "v_mfma_f32_16x16x32_bf16 v[224:227], v[188:191], v[92:95], v[224:227]\n"
    "buffer_load_dwordx4 v[156:159], v1, s[16:19], s43 offen offset:1024 sc1\n"
    "s_waitcnt lgkmcnt(3)\n"
    "v_mfma_f32_16x16x32_bf16 v[228:231], v[192:195], v[72:75], v[228:231]\n"
    "ds_read_b128 v[180:183], v250 offset:27648\n"
    "v_mfma_f32_16x16x32_bf16 v[228:231], v[196:199], v[80:83], v[228:231]\n"
    "ds_read_b128 v[184:187], v250 offset:28672\n"
    "v_mfma_f32_16x16x32_bf16 v[228:231], v[200:203], v[88:91], v[228:231]\n"
    "ds_read_b128 v[188:191], v250 offset:29696\n"
    "v_mfma_f32_16x16x32_bf16 v[232:235], v[192:195], v[76:79], v[232:235]\n"
    "v_mfma_f32_16x16x32_bf16 v[232:235], v[196:199], v[84:87], v[232:235]\n"
    "buffer_load_dwordx4 v[160:163], v1, s[16:19], s42 offen offset:2048 sc1\n"
    "v_mfma_f32_16x16x32_bf16 v[232:235], v[200:203], v[92:95], v[232:235]\n"
    "buffer_load_dwordx4 v[164:167], v1, s[16:19], s43 offen offset:2048 sc1\n"
    "s_waitcnt vmcnt(12) lgkmcnt(3)\n"
    "kb_bk40_%=:\n"
    "v_max3_u32 v236, v96, v97, v98\n"
    "v_max3_u32 v236, v99, v100, v236\n"
    "v_max3_u32 v236, v101, v102, v236\n"
    "v_max3_u32 v236, v103, v104, v236\n"
    "v_max3_u32 v236, v105, v106, v236\n"
    "v_max3_u32 v236, v107, v108, v236\n"
    "v_max3_u32 v236, v109, v110, v236\n"
    "v_max3_u32 v236, v111, v112, v236\n"
    "v_max3_u32 v236, v113, v114, v236\n"
    "v_max3_u32 v236, v115, v116, v236\n"
    "v_max3_u32 v236, v117, v118, v236\n"
    "v_max_u32 v236, v119, v236\n"
    "v_cmp_eq_u32 vcc, -1, v236\n"
    "s_cbranch_vccnz kb_rt41_%=\n"
    "kb_bk40_%=x:\n"
    "v_mfma_f32_16x16x32_bf16 v[204:207], v[168:171], v[96:99], v[204:207]\n"
    "ds_read_b128 v[192:195], v252 offset:9216\n"
    "v_mfma_f32_16x16x32_bf16 v[204:207], v[172:175], v[104:107], v[204:207]\n"
    "ds_read_b128 v[196:199], v252 offset:10240\n"
    "v_mfma_f32_16x16x32_bf16 v[204:207], v[176:179], v[112:115], v[204:207]\n"
    "ds_read_b128 v[200:203], v252 offset:11264\n"
    "v_mfma_f32_16x16x32_bf16 v[208:211], v[168:171], v[100:103], v[208:211]\n"
    "v_mfma_f32_16x16x32_bf16 v[208:211], v[172:175], v[108:111], v[208:211]\n"
    "v_mfma_f32_16x16x32_bf16 v[208:211], v[176:179], v[116:119], v[208:211]\n"
    "s_waitcnt lgkmcnt(3)\n"
    "s_barrier\n"
    "v_mfma_f32_16x16x32_bf16 v[212:215], v[180:183], v[96:99], v[212:215]\n"
    "ds_read_b128 v[168:171], v252 offset:27648\n"
    "v_mfma_f32_16x16x32_bf16 v[212:215], v[184:187], v[104:107], v[212:215]\n"
    "ds_read_b128 v[172:175], v252 offset:28672\n"
    "v_mfma_f32_16x16x32_bf16 v[212:215], v[188:191], v[112:115], v[212:215]\n"
    "ds_read_b128 v[176:179], v252 offset:29696\n"
    "v_mfma_f32_16x16x32_bf16 v[216:219], v[180:183], v[100:103], v[216:219]\n"
    "v_mfma_f32_16x16x32_bf16 v[216:219], v[184:187], v[108:111], v[216:219]\n"
    "v_mfma_f32_16x16x32_bf16 v[216:219], v[188:191], v[116:119], v[216:219]\n"
    "s_waitcnt lgkmcnt(3)\n"
    "v_mfma_f32_16x16x32_bf16 v[220:223], v[192:195], v[96:99], v[220:223]\n"
    "ds_read_b128 v[180:183], v250 offset:12288\n"
    "v_mfma_f32_16x16x32_bf16 v[220:223], v[196:199], v[104:107], v[220:223]\n"
    "ds_read_b128 v[184:187], v250 offset:13312\n"
    "v_mfma_f32_16x16x32_bf16 v[220:223], v[200:203], v[112:115], v[220:223]\n"
    "ds_read_b128 v[188:191], v250 offset:14336\n"
    "v_mfma_f32_16x16x32_bf16 v[224:227], v[192:195], v[100:103], v[224:227]\n"
    "v_mfma_f32_16x16x32_bf16 v[224:227], v[196:199], v[108:111], v[224:227]\n"
    "v_mfma_f32_16x16x32_bf16 v[224:227], v[200:203], v[116:119], v[224:227]\n"
    "s_waitcnt lgkmcnt(3)\n"
    "v_mfma_f32_16x16x32_bf16 v[228:231], v[168:171], v[96:99], v[228:231]\n"
    "ds_read_b128 v[192:195], v250 offset:30720\n"
    "v_mfma_f32_16x16x32_bf16 v[228:231], v[172:175], v[104:107], v[228:231]\n"
    "ds_read_b128 v[196:199], v250 offset:31744\n"
    "v_mfma_f32_16x16x32_bf16 v[228:231], v[176:179], v[112:115], v[228:231]\n"
    "ds_read_b128 v[200:203], v250 offset:32768\n"
    "v_mfma_f32_16x16x32_bf16 v[232:235], v[168:171], v[100:103], v[232:235]\n"
    "v_mfma_f32_16x16x32_bf16 v[232:235], v[172:175], v[108:111], v[232:235]\n"
    "v_mfma_f32_16x16x32_bf16 v[232:235], v[176:179], v[116:119], v[232:235]\n"
    "s_waitcnt vmcnt(6) lgkmcnt(3)\n"
    "kb_bk42_%=:\n"
    "v_max3_u32 v236, v120, v121, v122\n"
    "v_max3_u32 v236, v123, v124, v236\n"
    "v_max3_u32 v236, v125, v126, v236\n"
    "v_max3_u32 v236, v127, v128, v236\n"
    "v_max3_u32 v236, v129, v130, v236\n"
    "v_max3_u32 v236, v131, v132, v236\n"
    "v_max3_u32 v236, v133, v134, v236\n"
    "v_max3_u32 v236, v135, v136, v236\n"
    "v_max3_u32 v236, v137, v138, v236\n"
    "v_max3_u32 v236, v139, v140, v236\n"
    "v_max3_u32 v236, v141, v142, v236\n"
    "v_max_u32 v236, v143, v236\n"
    "v_cmp_eq_u32 vcc, -1, v236\n"
    "s_cbranch_vccnz kb_rt43_%=\n"
    "kb_bk42_%=x:\n"
    "v_mfma_f32_16x16x32_bf16 v[204:207], v[180:183], v[120:123], v[204:207]\n"
    "ds_read_b128 v[168:171], v252 offset:12288\n"
    "v_mfma_f32_16x16x32_bf16 v[204:207], v[184:187], v[128:131], v[204:207]\n"
    "ds_read_b128 v[172:175], v252 offset:13312\n"
    "v_mfma_f32_16x16x32_bf16 v[204:207], v[188:191], v[136:139], v[204:207]\n"
    "ds_read_b128 v[176:179], v252 offset:14336\n"
    "v_mfma_f32_16x16x32_bf16 v[208:211], v[180:183], v[124:127], v[208:211]\n"
    "v_mfma_f32_16x16x32_bf16 v[208:211], v[184:187], v[132:135], v[208:211]\n"
    "v_mfma_f32_16x16x32_bf16 v[208:211], v[188:191], v[140:143], v[208:211]\n"
    "s_waitcnt lgkmcnt(3)\n"
    "v_mfma_f32_16x16x32_bf16 v[212:215], v[192:195], v[120:123], v[212:215]\n"
    "ds_read_b128 v[180:183], v252 offset:30720\n"
    "v_mfma_f32_16x16x32_bf16 v[212:215], v[196:199], v[128:131], v[212:215]\n"
    "ds_read_b128 v[184:187], v252 offset:31744\n"
    "v_mfma_f32_16x16x32_bf16 v[212:215], v[200:203], v[136:139], v[212:215]\n"
    "ds_read_b128 v[188:191], v252 offset:32768\n"
    "v_mfma_f32_16x16x32_bf16 v[216:219], v[192:195], v[124:127], v[216:219]\n"
    "v_mfma_f32_16x16x32_bf16 v[216:219], v[196:199], v[132:135], v[216:219]\n"
    "buffer_store_dwordx2 v[242:243], v19, s[16:19], s71 offen offset:0 sc1\n"
    "v_mfma_f32_16x16x32_bf16 v[216:219], v[200:203], v[140:143], v[216:219]\n"
    "s_waitcnt lgkmcnt(3)\n"
    "v_mfma_f32_16x16x32_bf16 v[220:223], v[168:171], v[120:123], v[220:223]\n"
    "ds_read_b128 v[192:195], v250 offset:15360\n"
    "v_mfma_f32_16x16x32_bf16 v[220:223], v[172:175], v[128:131], v[220:223]\n"
    "ds_read_b128 v[196:199], v250 offset:16384\n"
    "v_mfma_f32_16x16x32_bf16 v[220:223], v[176:179], v[136:139], v[220:223]\n"
    "ds_read_b128 v[200:203], v250 offset:17408\n"
    "v_mfma_f32_16x16x32_bf16 v[224:227], v[168:171], v[124:127], v[224:227]\n"
    "v_mfma_f32_16x16x32_bf16 v[224:227], v[172:175], v[132:135], v[224:227]\n"
    "buffer_store_dwordx2 v[244:245], v19, s[16:19], s71 offen offset:512 sc1\n"
    "v_mfma_f32_16x16x32_bf16 v[224:227], v[176:179], v[140:143], v[224:227]\n"
    "s_waitcnt lgkmcnt(3)\n"
    "v_mfma_f32_16x16x32_bf16 v[228:231], v[180:183], v[120:123], v[228:231]\n"
    "ds_read_b128 v[168:171], v250 offset:33792\n"
    "v_mfma_f32_16x16x32_bf16 v[228:231], v[184:187], v[128:131], v[228:231]\n"
    "ds_read_b128 v[172:175], v250 offset:34816\n"
    "v_mfma_f32_16x16x32_bf16 v[228:231], v[188:191], v[136:139], v[228:231]\n"
    "ds_read_b128 v[176:179], v250 offset:35840\n"
    "v_mfma_f32_16x16x32_bf16 v[232:235], v[180:183], v[124:127], v[232:235]\n"
    "v_mfma_f32_16x16x32_bf16 v[232:235], v[184:187], v[132:135], v[232:235]\n"
    "buffer_store_dwordx2 v[246:247], v19, s[16:19], s71 offen offset:1024 sc1\n"
    "v_mfma_f32_16x16x32_bf16 v[232:235], v[188:191], v[140:143], v[232:235]\n"
    "s_waitcnt vmcnt(3) lgkmcnt(3)\n"
    "kb_bk44_%=:\n"
    "v_max3_u32 v236, v144, v145, v146\n"
    "v_max3_u32 v236, v147, v148, v236\n"
    "v_max3_u32 v236, v149, v150, v236\n"
    "v_max3_u32 v236, v151, v152, v236\n"
    "v_max3_u32 v236, v153, v154, v236\n"
    "v_max3_u32 v236, v155, v156, v236\n"
    "v_max3_u32 v236, v157, v158, v236\n"
    "v_max3_u32 v236, v159, v160, v236\n"
    "v_max3_u32 v236, v161, v162, v236\n"
    "v_max3_u32 v236, v163, v164, v236\n"
    "v_max3_u32 v236, v165, v166, v236\n"
    "v_max_u32 v236, v167, v236\n"
    "v_cmp_eq_u32 vcc, -1, v236\n"
    "s_cbranch_vccnz kb_rt45_%=\n"
    "kb_bk44_%=x:\n"
    "v_mfma_f32_16x16x32_bf16 v[204:207], v[192:195], v[144:147], v[204:207]\n"
    "ds_read_b128 v[180:183], v252 offset:15360\n"
    "v_mfma_f32_16x16x32_bf16 v[204:207], v[196:199], v[152:155], v[204:207]\n"
    "ds_read_b128 v[184:187], v252 offset:16384\n"
    "v_mfma_f32_16x16x32_bf16 v[204:207], v[200:203], v[160:163], v[204:207]\n"
    "ds_read_b128 v[188:191], v252 offset:17408\n"
    "v_mfma_f32_16x16x32_bf16 v[208:211], v[192:195], v[148:151], v[208:211]\n"
    "v_mfma_f32_16x16x32_bf16 v[208:211], v[196:199], v[156:159], v[208:211]\n"
    "buffer_store_dwordx2 v[248:249], v21, s[16:19], 0 offen sc1\n"
    "v_mfma_f32_16x16x32_bf16 v[208:211], v[200:203], v[164:167], v[208:211]\n"
    "s_waitcnt lgkmcnt(3)\n"
    "v_mfma_f32_16x16x32_bf16 v[212:215], v[168:171], v[144:147], v[212:215]\n"
    "ds_read_b128 v[192:195], v252 offset:33792\n"
    "v_mfma_f32_16x16x32_bf16 v[212:215], v[172:175], v[152:155], v[212:215]\n"
    "ds_read_b128 v[196:199], v252 offset:34816\n"
    "v_mfma_f32_16x16x32_bf16 v[212:215], v[176:179], v[160:163], v[212:215]\n"
    "ds_read_b128 v[200:203], v252 offset:35840\n"
    "v_mfma_f32_16x16x32_bf16 v[216:219], v[168:171], v[148:151], v[216:219]\n"
    "v_mfma_f32_16x16x32_bf16 v[216:219], v[172:175], v[156:159], v[216:219]\n"
    "v_mfma_f32_16x16x32_bf16 v[216:219], v[176:179], v[164:167], v[216:219]\n"
    "s_waitcnt lgkmcnt(3)\n"
    "v_mfma_f32_16x16x32_bf16 v[220:223], v[180:183], v[144:147], v[220:223]\n"
    "v_mfma_f32_16x16x32_bf16 v[220:223], v[184:187], v[152:155], v[220:223]\n"
    "v_mfma_f32_16x16x32_bf16 v[220:223], v[188:191], v[160:163], v[220:223]\n"
    "s_waitcnt lgkmcnt(0)\n"
    "s_barrier\n"
    "ds_read_b128 v[168:171], v251 offset:0\n"
    "v_mfma_f32_16x16x32_bf16 v[224:227], v[180:183], v[148:151], v[224:227]\n"
    "ds_read_b128 v[172:175], v251 offset:1024\n"
    "v_mfma_f32_16x16x32_bf16 v[224:227], v[184:187], v[156:159], v[224:227]\n"
    "ds_read_b128 v[176:179], v251 offset:2048\n"
    "v_mfma_f32_16x16x32_bf16 v[224:227], v[188:191], v[164:167], v[224:227]\n"
    "v_mul_f32 v242, v204, v212\n"
    "v_mul_f32 v243, v205, v213\n"
    "v_mfma_f32_16x16x32_bf16 v[228:231], v[192:195], v[144:147], v[228:231]\n"
    "ds_read_b128 v[180:183], v251 offset:3072\n"
    "v_mul_f32 v244, v206, v214\n"
    "v_mfma_f32_16x16x32_bf16 v[228:231], v[196:199], v[152:155], v[228:231]\n"
    "ds_read_b128 v[184:187], v251 offset:4096\n"
    "v_mul_f32 v245, v207, v215\n"
    "v_mfma_f32_16x16x32_bf16 v[228:231], v[200:203], v[160:163], v[228:231]\n"
    "ds_read_b128 v[188:191], v251 offset:5120\n"
    "v_mul_f32 v246, v208, v216\n"
    "v_mfma_f32_16x16x32_bf16 v[232:235], v[192:195], v[148:151], v[232:235]\n"
    "v_mul_f32 v247, v209, v217\n"
    "v_mul_f32 v248, v210, v218\n"
    "v_mfma_f32_16x16x32_bf16 v[232:235], v[196:199], v[156:159], v[232:235]\n"
    "v_mul_f32 v249, v211, v219\n"
    "v_cvt_pk_bf16_f32 v16, v242, v243\n"
    "v_mfma_f32_16x16x32_bf16 v[232:235], v[200:203], v[164:167], v[232:235]\n"
    "v_cvt_pk_bf16_f32 v17, v244, v245\n"
    "v_cvt_pk_bf16_f32 v20, v246, v247\n"
    "v_cvt_pk_bf16_f32 v21, v248, v249\n"
    "v_mov_b32 v250, v251\n"
    ".p2align 6\n"
    "kb_la46_%=:\n"
    "s_waitcnt lgkmcnt(3)\n"
    "v_mfma_f32_16x16x32_bf16 v[204:207], v[168:171], v[24:27], 0\n"
    "ds_read_b128 v[192:195], v250 offset:6144\n"
    "v_mfma_f32_16x16x32_bf16 v[204:207], v[172:175], v[32:35], v[204:207]\n"
    "ds_read_b128 v[196:199], v250 offset:7168\n"
    "v_mfma_f32_16x16x32_bf16 v[204:207], v[176:179], v[40:43], v[204:207]\n"
    "ds_read_b128 v[200:203], v250 offset:8192\n"
    "s_waitcnt lgkmcnt(3)\n"
    "v_mfma_f32_16x16x32_bf16 v[204:207], v[180:183], v[48:51], v[204:207]\n"
    "v_mfma_f32_16x16x32_bf16 v[204:207], v[184:187], v[56:59], v[204:207]\n"
    "v_mfma_f32_16x16x32_bf16 v[204:207], v[188:191], v[64:67], v[204:207]\n"
    "s_waitcnt lgkmcnt(0)\n"
    "v_mfma_f32_16x16x32_bf16 v[204:207], v[192:195], v[72:75], v[204:207]\n"
    "v_mfma_f32_16x16x32_bf16 v[204:207], v[196:199], v[80:83], v[204:207]\n"
    "v_mfma_f32_16x16x32_bf16 v[204:207], v[200:203], v[88:91], v[204:207]\n"
    "v_mfma_f32_16x16x32_bf16 v[208:211], v[168:171], v[28:31], 0\n"
    "ds_read_b128 v[168:171], v250 offset:9216\n"
    "v_mfma_f32_16x16x32_bf16 v[208:211], v[172:175], v[36:39], v[208:211]\n"
    "ds_read_b128 v[172:175], v250 offset:10240\n"
    "v_mfma_f32_16x16x32_bf16 v[208:211], v[176:179], v[44:47], v[208:211]\n"
    "ds_read_b128 v[176:179], v250 offset:11264\n"
    "v_mfma_f32_16x16x32_bf16 v[208:211], v[180:183], v[52:55], v[208:211]\n"
    "ds_read_b128 v[180:183], v250 offset:12288\n"
    "v_mfma_f32_16x16x32_bf16 v[208:211], v[184:187], v[60:63], v[208:211]\n"
    "ds_read_b128 v[184:187], v250 offset:13312\n"
    "v_mfma_f32_16x16x32_bf16 v[208:211], v[188:191], v[68:71], v[208:211]\n"
    "ds_read_b128 v[188:191], v250 offset:14336\n"
    "v_mfma_f32_16x16x32_bf16 v[208:211], v[192:195], v[76:79], v[208:211]\n"
    "ds_read_b128 v[192:195], v250 offset:15360\n"
    "v_mfma_f32_16x16x32_bf16 v[208:211], v[196:199], v[84:87], v[208:211]\n"
    "ds_read_b128 v[196:199], v250 offset:16384\n"
    "v_mfma_f32_16x16x32_bf16 v[208:211], v[200:203], v[92:95], v[208:211]\n"
    "ds_read_b128 v[200:203], v250 offset:17408\n"
    "s_waitcnt lgkmcnt(6)\n"
    "v_mfma_f32_16x16x32_bf16 v[204:207], v[168:171], v[96:99], v[204:207]\n"
    "v_mul_f32 v242, v220, v228\n"
    "v_mfma_f32_16x16x32_bf16 v[204:207], v[172:175], v[104:107], v[204:207]\n"
    "v_mul_f32 v243, v221, v229\n"
    "v_mfma_f32_16x16x32_bf16 v[204:207], v[176:179], v[112:115], v[204:207]\n"
    "v_mul_f32 v244, v222, v230\n"
    "s_waitcnt lgkmcnt(3)\n"
    "v_mfma_f32_16x16x32_bf16 v[204:207], v[180:183], v[120:123], v[204:207]\n"
    "v_mul_f32 v245, v223, v231\n"
    "v_mfma_f32_16x16x32_bf16 v[204:207], v[184:187], v[128:131], v[204:207]\n"
    "v_mul_f32 v246, v224, v232\n"
    "v_mfma_f32_16x16x32_bf16 v[204:207], v[188:191], v[136:139], v[204:207]\n"
    "v_mul_f32 v247, v225, v233\n"
    "s_waitcnt lgkmcnt(0)\n"
    "v_mfma_f32_16x16x32_bf16 v[204:207], v[192:195], v[144:147], v[204:207]\n"
    "v_mul_f32 v248, v226, v234\n"
    "v_mfma_f32_16x16x32_bf16 v[204:207], v[196:199], v[152:155], v[204:207]\n"
    "v_mul_f32 v249, v227, v235\n"
    "v_mfma_f32_16x16x32_bf16 v[204:207], v[200:203], v[160:163], v[204:207]\n"
    "v_cvt_pk_bf16_f32 v18, v242, v243\n"
    "v_mfma_f32_16x16x32_bf16 v[208:211], v[168:171], v[100:103], v[208:211]\n"
    "ds_read_b128 v[168:171], v250 offset:18432\n"
    "v_mfma_f32_16x16x32_bf16 v[208:211], v[172:175], v[108:111], v[208:211]\n"
    "ds_read_b128 v[172:175], v250 offset:19456\n"
    "v_mfma_f32_16x16x32_bf16 v[208:211], v[176:179], v[116:119], v[208:211]\n"
    "ds_read_b128 v[176:179], v250 offset:20480\n"
    "v_mfma_f32_16x16x32_bf16 v[208:211], v[180:183], v[124:127], v[208:211]\n"
    "ds_read_b128 v[180:183], v250 offset:21504\n"
    "v_mfma_f32_16x16x32_bf16 v[208:211], v[184:187], v[132:135], v[208:211]\n"
    "ds_read_b128 v[184:187], v250 offset:22528\n"
    "v_mfma_f32_16x16x32_bf16 v[208:211], v[188:191], v[140:143], v[208:211]\n"
    "ds_read_b128 v[188:191], v250 offset:23552\n"
    "v_mfma_f32_16x16x32_bf16 v[208:211], v[192:195], v[148:151], v[208:211]\n"
    "ds_read_b128 v[192:195], v250 offset:24576\n"
    "v_mfma_f32_16x16x32_bf16 v[208:211], v[196:199], v[156:159], v[208:211]\n"
    "ds_read_b128 v[196:199], v250 offset:25600\n"
    "v_mfma_f32_16x16x32_bf16 v[208:211], v[200:203], v[164:167], v[208:211]\n"
    "ds_read_b128 v[200:203], v250 offset:26624\n"
    "s_waitcnt lgkmcnt(6)\n"
    "v_mfma_f32_16x16x32_bf16 v[212:215], v[168:171], v[24:27], 0\n"
    "v_cvt_pk_bf16_f32 v19, v244, v245\n"
    "v_mfma_f32_16x16x32_bf16 v[212:215], v[172:175], v[32:35], v[212:215]\n"
    "v_cvt_pk_bf16_f32 v22, v246, v247\n"
    "v_mfma_f32_16x16x32_bf16 v[212:215], v[176:179], v[40:43], v[212:215]\n"
    "v_cvt_pk_bf16_f32 v23, v248, v249\n"
    "s_waitcnt lgkmcnt(3)\n"
    "v_mfma_f32_16x16x32_bf16 v[212:215], v[180:183], v[48:51], v[212:215]\n"
    "s_add_u32 s44, s44, 36864\n"
    "v_mfma_f32_16x16x32_bf16 v[212:215], v[184:187], v[56:59], v[212:215]\n"
    "s_cmp_eq_u32 s44, 147456\n"
    "v_mfma_f32_16x16x32_bf16 v[212:215], v[188:191], v[64:67], v[212:215]\n"
    "s_cselect_b32 s44, 0, s44\n"
    "s_waitcnt lgkmcnt(0)\n"
    "v_mfma_f32_16x16x32_bf16 v[212:215], v[192:195], v[72:75], v[212:215]\n"
    "v_add_u32 v251, s44, v1\n"
    "v_mfma_f32_16x16x32_bf16 v[212:215], v[196:199], v[80:83], v[212:215]\n"
    "v_mfma_f32_16x16x32_bf16 v[212:215], v[200:203], v[88:91], v[212:215]\n"
    "v_mfma_f32_16x16x32_bf16 v[216:219], v[168:171], v[28:31], 0\n"
    "ds_read_b128 v[168:171], v250 offset:27648\n"
    "v_mfma_f32_16x16x32_bf16 v[216:219], v[172:175], v[36:39], v[216:219]\n"
    "ds_read_b128 v[172:175], v250 offset:28672\n"
    "v_mfma_f32_16x16x32_bf16 v[216:219], v[176:179], v[44:47], v[216:219]\n"
    "ds_read_b128 v[176:179], v250 offset:29696\n"
    "v_mfma_f32_16x16x32_bf16 v[216:219], v[180:183], v[52:55], v[216:219]\n"
    "ds_read_b128 v[180:183], v250 offset:30720\n"
    "v_mfma_f32_16x16x32_bf16 v[216:219], v[184:187], v[60:63], v[216:219]\n"
    "ds_read_b128 v[184:187], v250 offset:31744\n"
    "v_mfma_f32_16x16x32_bf16 v[216:219], v[188:191], v[68:71], v[216:219]\n"
    "ds_read_b128 v[188:191], v250 offset:32768\n"
    "v_mfma_f32_16x16x32_bf16 v[216:219], v[192:195], v[76:79], v[216:219]\n"
    "ds_read_b128 v[192:195], v250 offset:33792\n"
    "v_mfma_f32_16x16x32_bf16 v[216:219], v[196:199], v[84:87], v[216:219]\n"
    "ds_read_b128 v[196:199], v250 offset:34816\n"
    "v_mfma_f32_16x16x32_bf16 v[216:219], v[200:203], v[92:95], v[216:219]\n"
    "ds_read_b128 v[200:203], v250 offset:35840\n"
    "s_waitcnt lgkmcnt(6)\n"
    "v_mfma_f32_16x16x32_bf16 v[212:215], v[168:171], v[96:99], v[212:215]\n"
    "v_mfma_f32_16x16x32_bf16 v[212:215], v[172:175], v[104:107], v[212:215]\n"
    "v_mfma_f32_16x16x32_bf16 v[212:215], v[176:179], v[112:115], v[212:215]\n"
    "s_waitcnt lgkmcnt(3)\n"
    "v_mfma_f32_16x16x32_bf16 v[212:215], v[180:183], v[120:123], v[212:215]\n"
    "buffer_store_dwordx4 v[16:19], v1, s[16:19], s41 offen offset:0 sc1\n"
    "v_mfma_f32_16x16x32_bf16 v[212:215], v[184:187], v[128:131], v[212:215]\n"
    "v_mfma_f32_16x16x32_bf16 v[212:215], v[188:191], v[136:139], v[212:215]\n"
    "s_waitcnt lgkmcnt(0)\n"
    "v_mfma_f32_16x16x32_bf16 v[212:215], v[192:195], v[144:147], v[212:215]\n"
    "buffer_store_dwordx4 v[20:23], v1, s[16:19], s41 offen offset:1024 sc1\n"
    "v_mfma_f32_16x16x32_bf16 v[212:215], v[196:199], v[152:155], v[212:215]\n"
    "s_add_u32 s41, s41, 8192\n"
    "v_mfma_f32_16x16x32_bf16 v[212:215], v[200:203], v[160:163], v[212:215]\n"
    "v_mfma_f32_16x16x32_bf16 v[216:219], v[168:171], v[100:103], v[216:219]\n"
    "s_waitcnt vmcnt(2) lgkmcnt(0)\n"
    "s_barrier\n"
    "ds_read_b128 v[168:171], v251 offset:0\n"
    "v_mfma_f32_16x16x32_bf16 v[216:219], v[172:175], v[108:111], v[216:219]\n"
    "ds_read_b128 v[172:175], v251 offset:1024\n"
    "v_mfma_f32_16x16x32_bf16 v[216:219], v[176:179], v[116:119], v[216:219]\n"
    "ds_read_b128 v[176:179], v251 offset:2048\n"
    "v_mfma_f32_16x16x32_bf16 v[216:219], v[180:183], v[124:127], v[216:219]\n"
    "ds_read_b128 v[180:183], v251 offset:3072\n"
    "v_mov_b32 v250, v251\n"
    "v_mfma_f32_16x16x32_bf16 v[216:219], v[184:187], v[132:135], v[216:219]\n"
    "ds_read_b128 v[184:187], v251 offset:4096\n"
    "v_mfma_f32_16x16x32_bf16 v[216:219], v[188:191], v[140:143], v[216:219]\n"
    "ds_read_b128 v[188:191], v251 offset:5120\n"
    "v_mfma_f32_16x16x32_bf16 v[216:219], v[192:195], v[148:151], v[216:219]\n"
    "v_mfma_f32_16x16x32_bf16 v[216:219], v[196:199], v[156:159], v[216:219]\n"
    "v_mfma_f32_16x16x32_bf16 v[216:219], v[200:203], v[164:167], v[216:219]\n"
    "s_waitcnt lgkmcnt(3)\n"
    "v_mfma_f32_16x16x32_bf16 v[220:223], v[168:171], v[24:27], 0\n"
    "ds_read_b128 v[192:195], v250 offset:6144\n"
    "v_mfma_f32_16x16x32_bf16 v[220:223], v[172:175], v[32:35], v[220:223]\n"
    "ds_read_b128 v[196:199], v250 offset:7168\n"
    "v_mfma_f32_16x16x32_bf16 v[220:223], v[176:179], v[40:43], v[220:223]\n"
    "ds_read_b128 v[200:203], v250 offset:8192\n"
    "s_waitcnt lgkmcnt(3)\n"
    "v_mfma_f32_16x16x32_bf16 v[220:223], v[180:183], v[48:51], v[220:223]\n"
    "v_mfma_f32_16x16x32_bf16 v[220:223], v[184:187], v[56:59], v[220:223]\n"
    "v_mfma_f32_16x16x32_bf16 v[220:223], v[188:191], v[64:67], v[220:223]\n"
    "s_waitcnt lgkmcnt(0)\n"
    "v_mfma_f32_16x16x32_bf16 v[220:223], v[192:195], v[72:75], v[220:223]\n"
    "v_mfma_f32_16x16x32_bf16 v[220:223], v[196:199], v[80:83], v[220:223]\n"
    "v_mfma_f32_16x16x32_bf16 v[220:223], v[200:203], v[88:91], v[220:223]\n"
    "v_mfma_f32_16x16x32_bf16 v[224:227], v[168:171], v[28:31], 0\n"
    "ds_read_b128 v[168:171], v250 offset:9216\n"
    "v_mfma_f32_16x16x32_bf16 v[224:227], v[172:175], v[36:39], v[224:227]\n"
    "ds_read_b128 v[172:175], v250 offset:10240\n"
    "v_mfma_f32_16x16x32_bf16 v[224:227], v[176:179], v[44:47], v[224:227]\n"
    "ds_read_b128 v[176:179], v250 offset:11264\n"
    "v_mfma_f32_16x16x32_bf16 v[224:227], v[180:183], v[52:55], v[224:227]\n"
    "ds_read_b128 v[180:183], v250 offset:12288\n"
    "v_mfma_f32_16x16x32_bf16 v[224:227], v[184:187], v[60:63], v[224:227]\n"
    "ds_read_b128 v[184:187], v250 offset:13312\n"
    "v_mfma_f32_16x16x32_bf16 v[224:227], v[188:191], v[68:71], v[224:227]\n"
    "ds_read_b128 v[188:191], v250 offset:14336\n"
    "v_mfma_f32_16x16x32_bf16 v[224:227], v[192:195], v[76:79], v[224:227]\n"
    "ds_read_b128 v[192:195], v250 offset:15360\n"
    "v_mfma_f32_16x16x32_bf16 v[224:227], v[196:199], v[84:87], v[224:227]\n"
    "ds_read_b128 v[196:199], v250 offset:16384\n"
    "v_mfma_f32_16x16x32_bf16 v[224:227], v[200:203], v[92:95], v[224:227]\n"
    "ds_read_b128 v[200:203], v250 offset:17408\n"
    "s_waitcnt lgkmcnt(6)\n"
    "v_mfma_f32_16x16x32_bf16 v[220:223], v[168:171], v[96:99], v[220:223]\n"
    "v_mul_f32 v242, v204, v212\n"
    "v_mfma_f32_16x16x32_bf16 v[220:223], v[172:175], v[104:107], v[220:223]\n"
    "v_mul_f32 v243, v205, v213\n"
    "v_mfma_f32_16x16x32_bf16 v[220:223], v[176:179], v[112:115], v[220:223]\n"
    "v_mul_f32 v244, v206, v214\n"
    "s_waitcnt lgkmcnt(3)\n"
    "v_mfma_f32_16x16x32_bf16 v[220:223], v[180:183], v[120:123], v[220:223]\n"
    "v_mul_f32 v245, v207, v215\n"
    "v_mfma_f32_16x16x32_bf16 v[220:223], v[184:187], v[128:131], v[220:223]\n"
    "v_mul_f32 v246, v208, v216\n"
    "v_mfma_f32_16x16x32_bf16 v[220:223], v[188:191], v[136:139], v[220:223]\n"
    "v_mul_f32 v247, v209, v217\n"
    "s_waitcnt lgkmcnt(0)\n"
    "v_mfma_f32_16x16x32_bf16 v[220:223], v[192:195], v[144:147], v[220:223]\n"
    "v_mul_f32 v248, v210, v218\n"
    "v_mfma_f32_16x16x32_bf16 v[220:223], v[196:199], v[152:155], v[220:223]\n"
    "v_mul_f32 v249, v211, v219\n"
    "v_mfma_f32_16x16x32_bf16 v[220:223], v[200:203], v[160:163], v[220:223]\n"
    "v_cvt_pk_bf16_f32 v16, v242, v243\n"
    "v_mfma_f32_16x16x32_bf16 v[224:227], v[168:171], v[100:103], v[224:227]\n"
    "ds_read_b128 v[168:171], v250 offset:18432\n"
    "v_mfma_f32_16x16x32_bf16 v[224:227], v[172:175], v[108:111], v[224:227]\n"
    "ds_read_b128 v[172:175], v250 offset:19456\n"
    "v_mfma_f32_16x16x32_bf16 v[224:227], v[176:179], v[116:119], v[224:227]\n"
    "ds_read_b128 v[176:179], v250 offset:20480\n"
    "v_mfma_f32_16x16x32_bf16 v[224:227], v[180:183], v[124:127], v[224:227]\n"
    "ds_read_b128 v[180:183], v250 offset:21504\n"
    "v_mfma_f32_16x16x32_bf16 v[224:227], v[184:187], v[132:135], v[224:227]\n"
    "ds_read_b128 v[184:187], v250 offset:22528\n"
    "v_mfma_f32_16x16x32_bf16 v[224:227], v[188:191], v[140:143], v[224:227]\n"
    "ds_read_b128 v[188:191], v250 offset:23552\n"
    "v_mfma_f32_16x16x32_bf16 v[224:227], v[192:195], v[148:151], v[224:227]\n"
    "ds_read_b128 v[192:195], v250 offset:24576\n"
    "v_mfma_f32_16x16x32_bf16 v[224:227], v[196:199], v[156:159], v[224:227]\n"
    "ds_read_b128 v[196:199], v250 offset:25600\n"
    "v_mfma_f32_16x16x32_bf16 v[224:227], v[200:203], v[164:167], v[224:227]\n"
    "ds_read_b128 v[200:203], v250 offset:26624\n"
    "s_waitcnt lgkmcnt(6)\n"
    "v_mfma_f32_16x16x32_bf16 v[228:231], v[168:171], v[24:27], 0\n"
    "v_cvt_pk_bf16_f32 v17, v244, v245\n"
    "v_mfma_f32_16x16x32_bf16 v[228:231], v[172:175], v[32:35], v[228:231]\n"
    "v_cvt_pk_bf16_f32 v20, v246, v247\n"
    "v_mfma_f32_16x16x32_bf16 v[228:231], v[176:179], v[40:43], v[228:231]\n"
    "v_cvt_pk_bf16_f32 v21, v248, v249\n"
    "s_waitcnt lgkmcnt(3)\n"
    "v_mfma_f32_16x16x32_bf16 v[228:231], v[180:183], v[48:51], v[228:231]\n"
    "s_add_u32 s44, s44, 36864\n"
    "v_mfma_f32_16x16x32_bf16 v[228:231], v[184:187], v[56:59], v[228:231]\n"
    "s_cmp_eq_u32 s44, 147456\n"
    "v_mfma_f32_16x16x32_bf16 v[228:231], v[188:191], v[64:67], v[228:231]\n"
    "s_cselect_b32 s44, 0, s44\n"
    "s_waitcnt lgkmcnt(0)\n"
    "v_mfma_f32_16x16x32_bf16 v[228:231], v[192:195], v[72:75], v[228:231]\n"
    "v_add_u32 v251, s44, v1\n"
    "v_mfma_f32_16x16x32_bf16 v[228:231], v[196:199], v[80:83], v[228:231]\n"
    "v_mfma_f32_16x16x32_bf16 v[228:231], v[200:203], v[88:91], v[228:231]\n"
    "v_mfma_f32_16x16x32_bf16 v[232:235], v[168:171], v[28:31], 0\n"
    "ds_read_b128 v[168:171], v250 offset:27648\n"
    "v_mfma_f32_16x16x32_bf16 v[232:235], v[172:175], v[36:39], v[232:235]\n"
    "ds_read_b128 v[172:175], v250 offset:28672\n"
    "v_mfma_f32_16x16x32_bf16 v[232:235], v[176:179], v[44:47], v[232:235]\n"
    "ds_read_b128 v[176:179], v250 offset:29696\n"
    "v_mfma_f32_16x16x32_bf16 v[232:235], v[180:183], v[52:55], v[232:235]\n"
    "ds_read_b128 v[180:183], v250 offset:30720\n"
    "v_mfma_f32_16x16x32_bf16 v[232:235], v[184:187], v[60:63], v[232:235]\n"
    "ds_read_b128 v[184:187], v250 offset:31744\n"
    "v_mfma_f32_16x16x32_bf16 v[232:235], v[188:191], v[68:71], v[232:235]\n"
    "ds_read_b128 v[188:191], v250 offset:32768\n"
    "v_mfma_f32_16x16x32_bf16 v[232:235], v[192:195], v[76:79], v[232:235]\n"
    "ds_read_b128 v[192:195], v250 offset:33792\n"
    "v_mfma_f32_16x16x32_bf16 v[232:235], v[196:199], v[84:87], v[232:235]\n"
    "ds_read_b128 v[196:199], v250 offset:34816\n"
    "v_mfma_f32_16x16x32_bf16 v[232:235], v[200:203], v[92:95], v[232:235]\n"
    "ds_read_b128 v[200:203], v250 offset:35840\n"
    "s_waitcnt lgkmcnt(6)\n"
    "v_mfma_f32_16x16x32_bf16 v[228:231], v[168:171], v[96:99], v[228:231]\n"
    "v_mfma_f32_16x16x32_bf16 v[228:231], v[172:175], v[104:107], v[228:231]\n"
    "v_mfma_f32_16x16x32_bf16 v[228:231], v[176:179], v[112:115], v[228:231]\n"
    "s_waitcnt lgkmcnt(3)\n"
    "v_mfma_f32_16x16x32_bf16 v[228:231], v[180:183], v[120:123], v[228:231]\n"
    "v_mfma_f32_16x16x32_bf16 v[228:231], v[184:187], v[128:131], v[228:231]\n"
    "v_mfma_f32_16x16x32_bf16 v[228:231], v[188:191], v[136:139], v[228:231]\n"
    "s_waitcnt lgkmcnt(0)\n"
    "v_mfma_f32_16x16x32_bf16 v[228:231], v[192:195], v[144:147], v[228:231]\n"
    "v_mfma_f32_16x16x32_bf16 v[228:231], v[196:199], v[152:155], v[228:231]\n"
    "s_add_u32 s40, s40, 1\n"
    "s_cmp_lt_u32 s40, 9\n"
    "v_mfma_f32_16x16x32_bf16 v[228:231], v[200:203], v[160:163], v[228:231]\n"
    "v_mfma_f32_16x16x32_bf16 v[232:235], v[168:171], v[100:103], v[232:235]\n"
    "s_waitcnt lgkmcnt(0)\n"
    "s_barrier\n"
    "ds_read_b128 v[168:171], v251 offset:0\n"
    "v_mfma_f32_16x16x32_bf16 v[232:235], v[172:175], v[108:111], v[232:235]\n"
    "ds_read_b128 v[172:175], v251 offset:1024\n"
    "v_mfma_f32_16x16x32_bf16 v[232:235], v[176:179], v[116:119], v[232:235]\n"
    "ds_read_b128 v[176:179], v251 offset:2048\n"
    "v_mfma_f32_16x16x32_bf16 v[232:235], v[180:183], v[124:127], v[232:235]\n"
    "ds_read_b128 v[180:183], v251 offset:3072\n"
    "v_mov_b32 v250, v251\n"
    "v_mfma_f32_16x16x32_bf16 v[232:235], v[184:187], v[132:135], v[232:235]\n"
    "ds_read_b128 v[184:187], v251 offset:4096\n"
    "v_mfma_f32_16x16x32_bf16 v[232:235], v[188:191], v[140:143], v[232:235]\n"
    "ds_read_b128 v[188:191], v251 offset:5120\n"
    "v_mfma_f32_16x16x32_bf16 v[232:235], v[192:195], v[148:151], v[232:235]\n"
    "v_mfma_f32_16x16x32_bf16 v[232:235], v[196:199], v[156:159], v[232:235]\n"
    "v_mfma_f32_16x16x32_bf16 v[232:235], v[200:203], v[164:167], v[232:235]\n"
    "s_cbranch_scc1 kb_la46_%=\n"
    "v_add_u32 v252, s77, v250\n"
    "ds_read_b128 v[96:99], v252 offset:18432\n"
    "ds_read_b128 v[100:103], v252 offset:19456\n"
    "ds_read_b128 v[104:107], v252 offset:26624\n"
    "ds_read_b128 v[108:111], v252 offset:27648\n"
    "v_mul_f32 v242, v220, v228\n"
    "v_mul_f32 v243, v221, v229\n"
    "v_mul_f32 v244, v222, v230\n"
    "v_mul_f32 v245, v223, v231\n"
    "v_mul_f32 v246, v224, v232\n"
    "v_mul_f32 v247, v225, v233\n"
    "v_mul_f32 v248, v226, v234\n"
    "v_mul_f32 v249, v227, v235\n"
    "s_waitcnt lgkmcnt(0)\n"
    "v_mfma_f32_16x16x32_bf16 v[24:27], v[168:171], v[96:99], 0\n"
    "ds_read_b128 v[192:195], v250 offset:6144\n"
    "s_add_u32 s44, s44, 36864\n"
    "v_mfma_f32_16x16x32_bf16 v[24:27], v[172:175], v[104:107], v[24:27]\n"
    "ds_read_b128 v[196:199], v250 offset:7168\n"
    "s_cmp_eq_u32 s44, 147456\n"
    "v_mfma_f32_16x16x32_bf16 v[28:31], v[168:171], v[100:103], 0\n"
    "s_cselect_b32 s44, 0, s44\n"
    "v_mfma_f32_16x16x32_bf16 v[28:31], v[172:175], v[108:111], v[28:31]\n"
    "s_waitcnt lgkmcnt(4)\n"
    "v_mfma_f32_16x16x32_bf16 v[32:35], v[176:179], v[96:99], 0\n"
    "ds_read_b128 v[200:203], v250 offset:8192\n"
    "v_add_u32 v251, s44, v1\n"
    "v_mfma_f32_16x16x32_bf16 v[32:35], v[180:183], v[104:107], v[32:35]\n"
    "ds_read_b128 v[168:171], v250 offset:9216\n"
    "v_add_u32 v252, s77, v251\n"
    "v_mfma_f32_16x16x32_bf16 v[36:39], v[176:179], v[100:103], 0\n"
    "v_cvt_pk_bf16_f32 v18, v242, v243\n"
    "v_mfma_f32_16x16x32_bf16 v[36:39], v[180:183], v[108:111], v[36:39]\n"
    "s_waitcnt lgkmcnt(4)\n"
    "v_mfma_f32_16x16x32_bf16 v[40:43], v[184:187], v[96:99], 0\n"
    "ds_read_b128 v[172:175], v250 offset:10240\n"
    "v_cvt_pk_bf16_f32 v19, v244, v245\n"
    "v_mfma_f32_16x16x32_bf16 v[40:43], v[188:191], v[104:107], v[40:43]\n"
    "ds_read_b128 v[176:179], v250 offset:11264\n"
    "v_cvt_pk_bf16_f32 v22, v246, v247\n"
    "v_mfma_f32_16x16x32_bf16 v[44:47], v[184:187], v[100:103], 0\n"
    "v_cvt_pk_bf16_f32 v23, v248, v249\n"
    "v_mfma_f32_16x16x32_bf16 v[44:47], v[188:191], v[108:111], v[44:47]\n"
    "s_waitcnt lgkmcnt(4)\n"
    "v_mfma_f32_16x16x32_bf16 v[48:51], v[192:195], v[96:99], 0\n"
    "ds_read_b128 v[180:183], v250 offset:12288\n"
    "buffer_store_dwordx4 v[16:19], v1, s[16:19], s41 offen offset:0 sc1\n"
    "v_mfma_f32_16x16x32_bf16 v[48:51], v[196:199], v[104:107], v[48:51]\n"
    "ds_read_b128 v[184:187], v250 offset:13312\n"
    "buffer_store_dwordx4 v[20:23], v1, s[16:19], s41 offen offset:1024 sc1\n"
    "v_mfma_f32_16x16x32_bf16 v[52:55], v[192:195], v[100:103], 0\n"
    "s_mov_b32 s40, 1\n"
    "v_mfma_f32_16x16x32_bf16 v[52:55], v[196:199], v[108:111], v[52:55]\n"
    "s_waitcnt lgkmcnt(4)\n"
    "v_mfma_f32_16x16x32_bf16 v[56:59], v[200:203], v[96:99], 0\n"
    "ds_read_b128 v[188:191], v250 offset:14336\n"
    "v_mfma_f32_16x16x32_bf16 v[56:59], v[168:171], v[104:107], v[56:59]\n"
    "ds_read_b128 v[192:195], v250 offset:15360\n"
    "v_mfma_f32_16x16x32_bf16 v[60:63], v[200:203], v[100:103], 0\n"
    "v_mfma_f32_16x16x32_bf16 v[60:63], v[168:171], v[108:111], v[60:63]\n"
    "s_waitcnt lgkmcnt(4)\n"
    "v_mfma_f32_16x16x32_bf16 v[64:67], v[172:175], v[96:99], 0\n"
    "ds_read_b128 v[196:199], v250 offset:16384\n"
    "v_mfma_f32_16x16x32_bf16 v[64:67], v[176:179], v[104:107], v[64:67]\n"
    "ds_read_b128 v[200:203], v250 offset:17408\n"
    "v_mfma_f32_16x16x32_bf16 v[68:71], v[172:175], v[100:103], 0\n"
    "v_mfma_f32_16x16x32_bf16 v[68:71], v[176:179], v[108:111], v[68:71]\n"
    "s_waitcnt lgkmcnt(4)\n"
    "v_mfma_f32_16x16x32_bf16 v[72:75], v[180:183], v[96:99], 0\n"
    "v_mfma_f32_16x16x32_bf16 v[72:75], v[184:187], v[104:107], v[72:75]\n"
    "s_waitcnt vmcnt(2) lgkmcnt(0)\n"
    "s_barrier\n"
    "ds_read_b128 v[168:171], v251 offset:0\n"
    "v_mfma_f32_16x16x32_bf16 v[76:79], v[180:183], v[100:103], 0\n"
    "ds_read_b128 v[172:175], v251 offset:1024\n"
    "ds_read_b128 v[112:115], v252 offset:18432\n"
    "v_mfma_f32_16x16x32_bf16 v[76:79], v[184:187], v[108:111], v[76:79]\n"
    "ds_read_b128 v[116:119], v252 offset:19456\n"
    "ds_read_b128 v[120:123], v252 offset:26624\n"
    "v_mfma_f32_16x16x32_bf16 v[80:83], v[188:191], v[96:99], 0\n"
    "ds_read_b128 v[124:127], v252 offset:27648\n"
    "ds_read_b128 v[176:179], v251 offset:2048\n"
    "v_mfma_f32_16x16x32_bf16 v[80:83], v[192:195], v[104:107], v[80:83]\n"
    "ds_read_b128 v[180:183], v251 offset:3072\n"
    "v_mfma_f32_16x16x32_bf16 v[84:87], v[188:191], v[100:103], 0\n"
    "v_mfma_f32_16x16x32_bf16 v[84:87], v[192:195], v[108:111], v[84:87]\n"
    "v_mfma_f32_16x16x32_bf16 v[88:91], v[196:199], v[96:99], 0\n"
    "ds_read_b128 v[184:187], v251 offset:4096\n"
    "v_mfma_f32_16x16x32_bf16 v[88:91], v[200:203], v[104:107], v[88:91]\n"
    "ds_read_b128 v[188:191], v251 offset:5120\n"
    "v_mfma_f32_16x16x32_bf16 v[92:95], v[196:199], v[100:103], 0\n"
    "v_mov_b32 v250, v251\n"
    "v_mfma_f32_16x16x32_bf16 v[92:95], v[200:203], v[108:111], v[92:95]\n"
    "s_waitcnt lgkmcnt(4)\n"
    "v_mfma_f32_16x16x32_bf16 v[24:27], v[168:171], v[112:115], v[24:27]\n"
    "ds_read_b128 v[192:195], v250 offset:6144\n"
    "v_mfma_f32_16x16x32_bf16 v[24:27], v[172:175], v[120:123], v[24:27]\n"
    "ds_read_b128 v[196:199], v250 offset:7168\n"
    "v_mfma_f32_16x16x32_bf16 v[28:31], v[168:171], v[116:119], v[28:31]\n"
    "s_add_u32 s44, s44, 36864\n"
    "v_mfma_f32_16x16x32_bf16 v[28:31], v[172:175], v[124:127], v[28:31]\n"
    "s_waitcnt lgkmcnt(4)\n"
    "v_mfma_f32_16x16x32_bf16 v[32:35], v[176:179], v[112:115], v[32:35]\n"
    "ds_read_b128 v[200:203], v250 offset:8192\n"
    "v_mfma_f32_16x16x32_bf16 v[32:35], v[180:183], v[120:123], v[32:35]\n"
    "ds_read_b128 v[168:171], v250 offset:9216\n"
    "v_mfma_f32_16x16x32_bf16 v[36:39], v[176:179], v[116:119], v[36:39]\n"
    "s_cmp_eq_u32 s44, 147456\n"
    "v_mfma_f32_16x16x32_bf16 v[36:39], v[180:183], v[124:127], v[36:39]\n"
    "s_waitcnt lgkmcnt(4)\n"
    "v_mfma_f32_16x16x32_bf16 v[40:43], v[184:187], v[112:115], v[40:43]\n"
    "ds_read_b128 v[172:175], v250 offset:10240\n"
    "v_mfma_f32_16x16x32_bf16 v[40:43], v[188:191], v[120:123], v[40:43]\n"
    "ds_read_b128 v[176:179], v250 offset:11264\n"
    "v_mfma_f32_16x16x32_bf16 v[44:47], v[184:187], v[116:119], v[44:47]\n"
    "s_cselect_b32 s44, 0, s44\n"
    "v_mfma_f32_16x16x32_bf16 v[44:47], v[188:191], v[124:127], v[44:47]\n"
    "s_waitcnt lgkmcnt(4)\n"
    "v_mfma_f32_16x16x32_bf16 v[48:51], v[192:195], v[112:115], v[48:51]\n"
    "ds_read_b128 v[180:183], v250 offset:12288\n"
    "v_mfma_f32_16x16x32_bf16 v[48:51], v[196:199], v[120:123], v[48:51]\n"
    "ds_read_b128 v[184:187], v250 offset:13312\n"
    "v_mfma_f32_16x16x32_bf16 v[52:55], v[192:195], v[116:119], v[52:55]\n"
    "v_add_u32 v251, s44, v1\n"
    "v_mfma_f32_16x16x32_bf16 v[52:55], v[196:199], v[124:127], v[52:55]\n"
    "s_waitcnt lgkmcnt(4)\n"
    "v_mfma_f32_16x16x32_bf16 v[56:59], v[200:203], v[112:115], v[56:59]\n"
    "ds_read_b128 v[188:191], v250 offset:14336\n"
    "v_mfma_f32_16x16x32_bf16 v[56:59], v[168:171], v[120:123], v[56:59]\n"
    "ds_read_b128 v[192:195], v250 offset:15360\n"
    "v_mfma_f32_16x16x32_bf16 v[60:63], v[200:203], v[116:119], v[60:63]\n"
    "v_add_u32 v252, s77, v251\n"
    "v_mfma_f32_16x16x32_bf16 v[60:63], v[168:171], v[124:127], v[60:63]\n"
    "s_waitcnt lgkmcnt(4)\n"
    "v_mfma_f32_16x16x32_bf16 v[64:67], v[172:175], v[112:115], v[64:67]\n"
    "ds_read_b128 v[196:199], v250 offset:16384\n"
    "v_mfma_f32_16x16x32_bf16 v[64:67], v[176:179], v[120:123], v[64:67]\n"
    "ds_read_b128 v[200:203], v250 offset:17408\n"
    "v_mfma_f32_16x16x32_bf16 v[68:71], v[172:175], v[116:119], v[68:71]\n"
    "v_mfma_f32_16x16x32_bf16 v[68:71], v[176:179], v[124:127], v[68:71]\n"
    "s_waitcnt lgkmcnt(4)\n"
    "v_mfma_f32_16x16x32_bf16 v[72:75], v[180:183], v[112:115], v[72:75]\n"
    "v_mfma_f32_16x16x32_bf16 v[72:75], v[184:187], v[120:123], v[72:75]\n"
    "s_waitcnt lgkmcnt(0)\n"
    "s_barrier\n"
    "ds_read_b128 v[168:171], v251 offset:0\n"
    "v_mfma_f32_16x16x32_bf16 v[76:79], v[180:183], v[116:119], v[76:79]\n"
    "ds_read_b128 v[172:175], v251 offset:1024\n"
    "ds_read_b128 v[96:99], v252 offset:18432\n"
    "v_mfma_f32_16x16x32_bf16 v[76:79], v[184:187], v[124:127], v[76:79]\n"
    "ds_read_b128 v[100:103], v252 offset:19456\n"
    "ds_read_b128 v[104:107], v252 offset:26624\n"
    "v_mfma_f32_16x16x32_bf16 v[80:83], v[188:191], v[112:115], v[80:83]\n"
    "ds_read_b128 v[108:111], v252 offset:27648\n"
    "ds_read_b128 v[176:179], v251 offset:2048\n"
    "v_mfma_f32_16x16x32_bf16 v[80:83], v[192:195], v[120:123], v[80:83]\n"
    "ds_read_b128 v[180:183], v251 offset:3072\n"
    "v_mfma_f32_16x16x32_bf16 v[84:87], v[188:191], v[116:119], v[84:87]\n"
    "v_mfma_f32_16x16x32_bf16 v[84:87], v[192:195], v[124:127], v[84:87]\n"
    "v_mfma_f32_16x16x32_bf16 v[88:91], v[196:199], v[112:115], v[88:91]\n"
    "ds_read_b128 v[184:187], v251 offset:4096\n"
    "v_mfma_f32_16x16x32_bf16 v[88:91], v[200:203], v[120:123], v[88:91]\n"
    "ds_read_b128 v[188:191], v251 offset:5120\n"
    "v_mfma_f32_16x16x32_bf16 v[92:95], v[196:199], v[116:119], v[92:95]\n"
    "v_mov_b32 v250, v251\n"
    "v_mfma_f32_16x16x32_bf16 v[92:95], v[200:203], v[124:127], v[92:95]\n"
    ".p2align 6\n"
    "kb_lb47_%=:\n"
    "s_waitcnt lgkmcnt(4)\n"
    "v_mfma_f32_16x16x32_bf16 v[24:27], v[168:171], v[96:99], v[24:27]\n"
    "ds_read_b128 v[192:195], v250 offset:6144\n"
    "v_mfma_f32_16x16x32_bf16 v[24:27], v[172:175], v[104:107], v[24:27]\n"
    "ds_read_b128 v[196:199], v250 offset:7168\n"
    "v_mfma_f32_16x16x32_bf16 v[28:31], v[168:171], v[100:103], v[28:31]\n"
    "s_add_u32 s44, s44, 36864\n"
    "v_mfma_f32_16x16x32_bf16 v[28:31], v[172:175], v[108:111], v[28:31]\n"
    "s_waitcnt lgkmcnt(4)\n"
    "v_mfma_f32_16x16x32_bf16 v[32:35], v[176:179], v[96:99], v[32:35]\n"
    "ds_read_b128 v[200:203], v250 offset:8192\n"
    "v_mfma_f32_16x16x32_bf16 v[32:35], v[180:183], v[104:107], v[32:35]\n"
    "ds_read_b128 v[168:171], v250 offset:9216\n"
    "v_mfma_f32_16x16x32_bf16 v[36:39], v[176:179], v[100:103], v[36:39]\n"
    "s_cmp_eq_u32 s44, 147456\n"
    "v_mfma_f32_16x16x32_bf16 v[36:39], v[180:183], v[108:111], v[36:39]\n"
    "s_waitcnt lgkmcnt(4)\n"
    "v_mfma_f32_16x16x32_bf16 v[40:43], v[184:187], v[96:99], v[40:43]\n"
    "ds_read_b128 v[172:175], v250 offset:10240\n"
    "v_mfma_f32_16x16x32_bf16 v[40:43], v[188:191], v[104:107], v[40:43]\n"
    "ds_read_b128 v[176:179], v250 offset:11264\n"
    "v_mfma_f32_16x16x32_bf16 v[44:47], v[184:187], v[100:103], v[44:47]\n"
    "s_cselect_b32 s44, 0, s44\n"
    "v_mfma_f32_16x16x32_bf16 v[44:47], v[188:191], v[108:111], v[44:47]\n"
    "s_waitcnt lgkmcnt(4)\n"
    "v_mfma_f32_16x16x32_bf16 v[48:51], v[192:195], v[96:99], v[48:51]\n"
    "ds_read_b128 v[180:183], v250 offset:12288\n"
    "v_mfma_f32_16x16x32_bf16 v[48:51], v[196:199], v[104:107], v[48:51]\n"
    "ds_read_b128 v[184:187], v250 offset:13312\n"
    "v_mfma_f32_16x16x32_bf16 v[52:55], v[192:195], v[100:103], v[52:55]\n"
    "v_add_u32 v251, s44, v1\n"
    "v_mfma_f32_16x16x32_bf16 v[52:55], v[196:199], v[108:111], v[52:55]\n"
    "s_waitcnt lgkmcnt(4)\n"
    "v_mfma_f32_16x16x32_bf16 v[56:59], v[200:203], v[96:99], v[56:59]\n"
    "ds_read_b128 v[188:191], v250 offset:14336\n"
    "v_mfma_f32_16x16x32_bf16 v[56:59], v[168:171], v[104:107], v[56:59]\n"
    "ds_read_b128 v[192:195], v250 offset:15360\n"
    "v_mfma_f32_16x16x32_bf16 v[60:63], v[200:203], v[100:103], v[60:63]\n"
    "v_add_u32 v252, s77, v251\n"
    "v_mfma_f32_16x16x32_bf16 v[60:63], v[168:171], v[108:111], v[60:63]\n"
    "s_waitcnt lgkmcnt(4)\n"
    "v_mfma_f32_16x16x32_bf16 v[64:67], v[172:175], v[96:99], v[64:67]\n"
    "ds_read_b128 v[196:199], v250 offset:16384\n"
    "v_mfma_f32_16x16x32_bf16 v[64:67], v[176:179], v[104:107], v[64:67]\n"
    "ds_read_b128 v[200:203], v250 offset:17408\n"
    "v_mfma_f32_16x16x32_bf16 v[68:71], v[172:175], v[100:103], v[68:71]\n"
    "v_mfma_f32_16x16x32_bf16 v[68:71], v[176:179], v[108:111], v[68:71]\n"
    "s_waitcnt lgkmcnt(4)\n"
    "v_mfma_f32_16x16x32_bf16 v[72:75], v[180:183], v[96:99], v[72:75]\n"
    "v_mfma_f32_16x16x32_bf16 v[72:75], v[184:187], v[104:107], v[72:75]\n"
    "s_waitcnt lgkmcnt(0)\n"
    "s_barrier\n"
    "ds_read_b128 v[168:171], v251 offset:0\n"
    "v_mfma_f32_16x16x32_bf16 v[76:79], v[180:183], v[100:103], v[76:79]\n"
    "ds_read_b128 v[172:175], v251 offset:1024\n"
    "ds_read_b128 v[112:115], v252 offset:18432\n"
    "v_mfma_f32_16x16x32_bf16 v[76:79], v[184:187], v[108:111], v[76:79]\n"
    "ds_read_b128 v[116:119], v252 offset:19456\n"
    "ds_read_b128 v[120:123], v252 offset:26624\n"
    "v_mfma_f32_16x16x32_bf16 v[80:83], v[188:191], v[96:99], v[80:83]\n"
    "ds_read_b128 v[124:127], v252 offset:27648\n"
    "ds_read_b128 v[176:179], v251 offset:2048\n"
    "v_mfma_f32_16x16x32_bf16 v[80:83], v[192:195], v[104:107], v[80:83]\n"
    "ds_read_b128 v[180:183], v251 offset:3072\n"
    "v_mfma_f32_16x16x32_bf16 v[84:87], v[188:191], v[100:103], v[84:87]\n"
    "v_mfma_f32_16x16x32_bf16 v[84:87], v[192:195], v[108:111], v[84:87]\n"
    "v_mfma_f32_16x16x32_bf16 v[88:91], v[196:199], v[96:99], v[88:91]\n"
    "ds_read_b128 v[184:187], v251 offset:4096\n"
    "v_mfma_f32_16x16x32_bf16 v[88:91], v[200:203], v[104:107], v[88:91]\n"
    "ds_read_b128 v[188:191], v251 offset:5120\n"
    "v_mfma_f32_16x16x32_bf16 v[92:95], v[196:199], v[100:103], v[92:95]\n"
    "v_mov_b32 v250, v251\n"
    "v_mfma_f32_16x16x32_bf16 v[92:95], v[200:203], v[108:111], v[92:95]\n"
    "s_add_u32 s40, s40, 1\n"
    "s_cmp_eq_u32 s40, 7\n"
    "s_cbranch_scc1 kb_mid48_%=\n"
    "s_waitcnt lgkmcnt(4)\n"
    "v_mfma_f32_16x16x32_bf16 v[24:27], v[168:171], v[112:115], v[24:27]\n"
    "ds_read_b128 v[192:195], v250 offset:6144\n"
    "v_mfma_f32_16x16x32_bf16 v[24:27], v[172:175], v[120:123], v[24:27]\n"
    "ds_read_b128 v[196:199], v250 offset:7168\n"
    "v_mfma_f32_16x16x32_bf16 v[28:31], v[168:171], v[116:119], v[28:31]\n"
    "s_add_u32 s44, s44, 36864\n"
    "v_mfma_f32_16x16x32_bf16 v[28:31], v[172:175], v[124:127], v[28:31]\n"
    "s_waitcnt lgkmcnt(4)\n"
    "v_mfma_f32_16x16x32_bf16 v[32:35], v[176:179], v[112:115], v[32:35]\n"
    "ds_read_b128 v[200:203], v250 offset:8192\n"
    "v_mfma_f32_16x16x32_bf16 v[32:35], v[180:183], v[120:123], v[32:35]\n"
    "ds_read_b128 v[168:171], v250 offset:9216\n"
    "v_mfma_f32_16x16x32_bf16 v[36:39], v[176:179], v[116:119], v[36:39]\n"
    "s_cmp_eq_u32 s44, 147456\n"
    "v_mfma_f32_16x16x32_bf16 v[36:39], v[180:183], v[124:127], v[36:39]\n"
    "s_waitcnt lgkmcnt(4)\n"
    "v_mfma_f32_16x16x32_bf16 v[40:43], v[184:187], v[112:115], v[40:43]\n"
    "ds_read_b128 v[172:175], v250 offset:10240\n"
    "v_mfma_f32_16x16x32_bf16 v[40:43], v[188:191], v[120:123], v[40:43]\n"
    "ds_read_b128 v[176:179], v250 offset:11264\n"
    "v_mfma_f32_16x16x32_bf16 v[44:47], v[184:187], v[116:119], v[44:47]\n"
    "s_cselect_b32 s44, 0, s44\n"
    "v_mfma_f32_16x16x32_bf16 v[44:47], v[188:191], v[124:127], v[44:47]\n"
    "s_waitcnt lgkmcnt(4)\n"
    "v_mfma_f32_16x16x32_bf16 v[48:51], v[192:195], v[112:115], v[48:51]\n"
    "ds_read_b128 v[180:183], v250 offset:12288\n"
    "v_mfma_f32_16x16x32_bf16 v[48:51], v[196:199], v[120:123], v[48:51]\n"
    "ds_read_b128 v[184:187], v250 offset:13312\n"
    "v_mfma_f32_16x16x32_bf16 v[52:55], v[192:195], v[116:119], v[52:55]\n"
    "v_add_u32 v251, s44, v1\n"
    "v_mfma_f32_16x16x32_bf16 v[52:55], v[196:199], v[124:127], v[52:55]\n"
    "s_waitcnt lgkmcnt(4)\n"
    "v_mfma_f32_16x16x32_bf16 v[56:59], v[200:203], v[112:115], v[56:59]\n"
    "ds_read_b128 v[188:191], v250 offset:14336\n"
    "v_mfma_f32_16x16x32_bf16 v[56:59], v[168:171], v[120:123], v[56:59]\n"
    "ds_read_b128 v[192:195], v250 offset:15360\n"
    "v_mfma_f32_16x16x32_bf16 v[60:63], v[200:203], v[116:119], v[60:63]\n"
    "v_add_u32 v252, s77, v251\n"
    "v_mfma_f32_16x16x32_bf16 v[60:63], v[168:171], v[124:127], v[60:63]\n"
    "s_waitcnt lgkmcnt(4)\n"
    "v_mfma_f32_16x16x32_bf16 v[64:67], v[172:175], v[112:115], v[64:67]\n"
    "ds_read_b128 v[196:199], v250 offset:16384\n"
    "v_mfma_f32_16x16x32_bf16 v[64:67], v[176:179], v[120:123], v[64:67]\n"
    "ds_read_b128 v[200:203], v250 offset:17408\n"
    "v_mfma_f32_16x16x32_bf16 v[68:71], v[172:175], v[116:119], v[68:71]\n"
    "v_mfma_f32_16x16x32_bf16 v[68:71], v[176:179], v[124:127], v[68:71]\n"
    "s_waitcnt lgkmcnt(4)\n"
    "v_mfma_f32_16x16x32_bf16 v[72:75], v[180:183], v[112:115], v[72:75]\n"
    "v_mfma_f32_16x16x32_bf16 v[72:75], v[184:187], v[120:123], v[72:75]\n"
    "s_waitcnt vmcnt(0) lgkmcnt(0)\n"
    "s_barrier\n"
    "ds_read_b128 v[168:171], v251 offset:0\n"
    "v_mfma_f32_16x16x32_bf16 v[76:79], v[180:183], v[116:119], v[76:79]\n"
    "ds_read_b128 v[172:175], v251 offset:1024\n"
    "ds_read_b128 v[96:99], v252 offset:18432\n"
    "v_mfma_f32_16x16x32_bf16 v[76:79], v[184:187], v[124:127], v[76:79]\n"
    "ds_read_b128 v[100:103], v252 offset:19456\n"
    "ds_read_b128 v[104:107], v252 offset:26624\n"
    "v_mfma_f32_16x16x32_bf16 v[80:83], v[188:191], v[112:115], v[80:83]\n"
    "ds_read_b128 v[108:111], v252 offset:27648\n"
    "ds_read_b128 v[176:179], v251 offset:2048\n"
    "v_mfma_f32_16x16x32_bf16 v[80:83], v[192:195], v[120:123], v[80:83]\n"
    "ds_read_b128 v[180:183], v251 offset:3072\n"
    "v_mfma_f32_16x16x32_bf16 v[84:87], v[188:191], v[116:119], v[84:87]\n"
    "v_mfma_f32_16x16x32_bf16 v[84:87], v[192:195], v[124:127], v[84:87]\n"
    "v_mfma_f32_16x16x32_bf16 v[88:91], v[196:199], v[112:115], v[88:91]\n"
    "ds_read_b128 v[184:187], v251 offset:4096\n"
    "v_mfma_f32_16x16x32_bf16 v[88:91], v[200:203], v[120:123], v[88:91]\n"
    "ds_read_b128 v[188:191], v251 offset:5120\n"
    "v_mfma_f32_16x16x32_bf16 v[92:95], v[196:199], v[116:119], v[92:95]\n"
    "v_mov_b32 v250, v251\n"
    "v_mfma_f32_16x16x32_bf16 v[92:95], v[200:203], v[124:127], v[92:95]\n"
    "s_branch kb_lb47_%=\n"
    "kb_mid48_%=:\n"
    "s_waitcnt lgkmcnt(4)\n"
    "v_mfma_f32_16x16x32_bf16 v[24:27], v[168:171], v[112:115], v[24:27]\n"
    "ds_read_b128 v[192:195], v250 offset:6144\n"
    "v_mfma_f32_16x16x32_bf16 v[24:27], v[172:175], v[120:123], v[24:27]\n"
    "ds_read_b128 v[196:199], v250 offset:7168\n"
    "v_mfma_f32_16x16x32_bf16 v[28:31], v[168:171], v[116:119], v[28:31]\n"
    "s_add_u32 s45, s77, 36864\n"
    "v_mfma_f32_16x16x32_bf16 v[28:31], v[172:175], v[124:127], v[28:31]\n"
    "v_add_u32 v252, s45, v1\n"
    "s_waitcnt lgkmcnt(4)\n"
    "v_mfma_f32_16x16x32_bf16 v[32:35], v[176:179], v[112:115], v[32:35]\n"
    "ds_read_b128 v[200:203], v250 offset:8192\n"
    "v_mfma_f32_16x16x32_bf16 v[32:35], v[180:183], v[120:123], v[32:35]\n"
    "ds_read_b128 v[168:171], v250 offset:9216\n"
    "v_mfma_f32_16x16x32_bf16 v[36:39], v[176:179], v[116:119], v[36:39]\n"
    "v_add_u32 v253, 147456, v1\n"
    "v_mfma_f32_16x16x32_bf16 v[36:39], v[180:183], v[124:127], v[36:39]\n"
    "ds_read_b128 v[128:131], v252 offset:0\n"
    "s_waitcnt lgkmcnt(5)\n"
    "v_mfma_f32_16x16x32_bf16 v[40:43], v[184:187], v[112:115], v[40:43]\n"
    "ds_read_b128 v[172:175], v250 offset:10240\n"
    "v_mfma_f32_16x16x32_bf16 v[40:43], v[188:191], v[120:123], v[40:43]\n"
    "ds_read_b128 v[176:179], v250 offset:11264\n"
    "v_mfma_f32_16x16x32_bf16 v[44:47], v[184:187], v[116:119], v[44:47]\n"
    "ds_read_b128 v[132:135], v252 offset:1024\n"
    "v_mfma_f32_16x16x32_bf16 v[44:47], v[188:191], v[124:127], v[44:47]\n"
    "ds_read_b128 v[136:139], v252 offset:8192\n"
    "s_waitcnt lgkmcnt(7)\n"
    "v_mfma_f32_16x16x32_bf16 v[48:51], v[192:195], v[112:115], v[48:51]\n"
    "ds_read_b128 v[180:183], v250 offset:12288\n"
    "v_mfma_f32_16x16x32_bf16 v[48:51], v[196:199], v[120:123], v[48:51]\n"
    "ds_read_b128 v[184:187], v250 offset:13312\n"
    "v_mfma_f32_16x16x32_bf16 v[52:55], v[192:195], v[116:119], v[52:55]\n"
    "ds_read_b128 v[140:143], v252 offset:9216\n"
    "v_mfma_f32_16x16x32_bf16 v[52:55], v[196:199], v[124:127], v[52:55]\n"
    "ds_read_b128 v[144:147], v252 offset:16384\n"
    "s_waitcnt lgkmcnt(9)\n"
    "v_mfma_f32_16x16x32_bf16 v[56:59], v[200:203], v[112:115], v[56:59]\n"
    "ds_read_b128 v[188:191], v250 offset:14336\n"
    "v_mfma_f32_16x16x32_bf16 v[56:59], v[168:171], v[120:123], v[56:59]\n"
    "ds_read_b128 v[192:195], v250 offset:15360\n"
    "v_mfma_f32_16x16x32_bf16 v[60:63], v[200:203], v[116:119], v[60:63]\n"
    "ds_read_b128 v[148:151], v252 offset:17408\n"
    "v_mfma_f32_16x16x32_bf16 v[60:63], v[168:171], v[124:127], v[60:63]\n"
    "ds_read_b128 v[152:155], v252 offset:24576\n"
    "s_waitcnt lgkmcnt(10)\n"
    "v_mfma_f32_16x16x32_bf16 v[64:67], v[172:175], v[112:115], v[64:67]\n"
    "ds_read_b128 v[196:199], v250 offset:16384\n"
    "v_mfma_f32_16x16x32_bf16 v[64:67], v[176:179], v[120:123], v[64:67]\n"
    "ds_read_b128 v[200:203], v250 offset:17408\n"
    "v_mfma_f32_16x16x32_bf16 v[68:71], v[172:175], v[116:119], v[68:71]\n"
    "ds_read_b128 v[156:159], v252 offset:25600\n"
    "v_mfma_f32_16x16x32_bf16 v[68:71], v[176:179], v[124:127], v[68:71]\n"
    "v_add_u32 v252, 36864, v252\n"
    "s_waitcnt lgkmcnt(9)\n"
    "v_mfma_f32_16x16x32_bf16 v[72:75], v[180:183], v[112:115], v[72:75]\n"
    "ds_read_b128 v[168:171], v253 offset:0\n"
    "v_mfma_f32_16x16x32_bf16 v[72:75], v[184:187], v[120:123], v[72:75]\n"
    "ds_read_b128 v[172:175], v253 offset:1024\n"
    "v_mfma_f32_16x16x32_bf16 v[76:79], v[180:183], v[116:119], v[76:79]\n"
    "v_add_u32 v251, 110592, v1\n"
    "v_mfma_f32_16x16x32_bf16 v[76:79], v[184:187], v[124:127], v[76:79]\n"
    "s_waitcnt lgkmcnt(7)\n"
    "v_mfma_f32_16x16x32_bf16 v[80:83], v[188:191], v[112:115], v[80:83]\n"
    "ds_read_b128 v[176:179], v253 offset:2048\n"
    "v_mfma_f32_16x16x32_bf16 v[80:83], v[192:195], v[120:123], v[80:83]\n"
    "ds_read_b128 v[180:183], v253 offset:3072\n"
    "v_mfma_f32_16x16x32_bf16 v[84:87], v[188:191], v[116:119], v[84:87]\n"
    "v_mfma_f32_16x16x32_bf16 v[84:87], v[192:195], v[124:127], v[84:87]\n"
    "s_waitcnt lgkmcnt(5)\n"
    "v_mfma_f32_16x16x32_bf16 v[88:91], v[196:199], v[112:115], v[88:91]\n"
    "ds_read_b128 v[184:187], v253 offset:8192\n"
    "s_barrier\n"
    "v_mfma_f32_16x16x32_bf16 v[88:91], v[200:203], v[120:123], v[88:91]\n"
    "v_mfma_f32_16x16x32_bf16 v[92:95], v[196:199], v[116:119], v[92:95]\n"
    "ds_read_b128 v[96:99], v252 offset:0\n"
    "ds_read_b128 v[100:103], v252 offset:1024\n"
    "v_mfma_f32_16x16x32_bf16 v[92:95], v[200:203], v[124:127], v[92:95]\n"
    "ds_read_b128 v[104:107], v252 offset:8192\n"
    "ds_read_b128 v[108:111], v252 offset:9216\n"
    "s_waitcnt lgkmcnt(7)\n"
    "v_mfma_f32_16x16x32_bf16 v[24:27], v[168:171], v[128:131], v[24:27]\n"
    "ds_read_b128 v[188:191], v253 offset:9216\n"
    "v_add_u32 v250, 0, v1\n"
    "v_mfma_f32_16x16x32_bf16 v[24:27], v[172:175], v[136:139], v[24:27]\n"
    "ds_read_b128 v[192:195], v253 offset:10240\n"
    "s_waitcnt lgkmcnt(7)\n"
    "v_mfma_f32_16x16x32_bf16 v[24:27], v[176:179], v[144:147], v[24:27]\n"
    "ds_read_b128 v[196:199], v253 offset:11264\n"
    "s_mov_b32 s48, 0\n"
    "v_mfma_f32_16x16x32_bf16 v[24:27], v[180:183], v[152:155], v[24:27]\n"
    "ds_read_b128 v[200:203], v253 offset:4096\n"
    "v_add_u32 v237, s77, v240\n"
    "v_mfma_f32_16x16x32_bf16 v[28:31], v[168:171], v[132:135], v[28:31]\n"
    "ds_read_b128 v[112:115], v252 offset:16384\n"
    "v_mfma_f32_16x16x32_bf16 v[28:31], v[172:175], v[140:143], v[28:31]\n"
    "ds_read_b128 v[116:119], v252 offset:17408\n"
    "v_mfma_f32_16x16x32_bf16 v[28:31], v[176:179], v[148:151], v[28:31]\n"
    "ds_read_b128 v[120:123], v252 offset:24576\n"
    "v_mfma_f32_16x16x32_bf16 v[28:31], v[180:183], v[156:159], v[28:31]\n"
    "ds_read_b128 v[124:127], v252 offset:25600\n"
    "s_waitcnt lgkmcnt(7)\n"
    "v_mfma_f32_16x16x32_bf16 v[32:35], v[184:187], v[128:131], v[32:35]\n"
    "ds_read_b128 v[168:171], v253 offset:5120\n"
    "v_mfma_f32_16x16x32_bf16 v[32:35], v[188:191], v[136:139], v[32:35]\n"
    "ds_read_b128 v[172:175], v253 offset:6144\n"
    "s_waitcnt lgkmcnt(7)\n"
    "v_mfma_f32_16x16x32_bf16 v[32:35], v[192:195], v[144:147], v[32:35]\n"
    "ds_read_b128 v[176:179], v253 offset:7168\n"
    "v_mfma_f32_16x16x32_bf16 v[32:35], v[196:199], v[152:155], v[32:35]\n"
    "ds_read_b128 v[180:183], v253 offset:12288\n"
    "v_mfma_f32_16x16x32_bf16 v[36:39], v[184:187], v[132:135], v[36:39]\n"
    "v_mfma_f32_16x16x32_bf16 v[36:39], v[188:191], v[140:143], v[36:39]\n"
    "v_mfma_f32_16x16x32_bf16 v[36:39], v[192:195], v[148:151], v[36:39]\n"
    "v_mfma_f32_16x16x32_bf16 v[36:39], v[196:199], v[156:159], v[36:39]\n"
    "s_waitcnt lgkmcnt(3)\n"
    "v_mfma_f32_16x16x32_bf16 v[24:27], v[200:203], v[96:99], v[24:27]\n"
    "ds_read_b128 v[184:187], v253 offset:13312\n"
    "v_mfma_f32_16x16x32_bf16 v[24:27], v[168:171], v[104:107], v[24:27]\n"
    "ds_read_b128 v[188:191], v253 offset:14336\n"
    "s_waitcnt lgkmcnt(3)\n"
    "v_mfma_f32_16x16x32_bf16 v[24:27], v[172:175], v[112:115], v[24:27]\n"
    "ds_read_b128 v[192:195], v253 offset:15360\n"
    "v_mfma_f32_16x16x32_bf16 v[24:27], v[176:179], v[120:123], v[24:27]\n"
    "s_barrier\n"
    "ds_read_b128 v[196:199], v251 offset:0\n"
    "v_mfma_f32_16x16x32_bf16 v[28:31], v[200:203], v[100:103], v[28:31]\n"
    "v_mfma_f32_16x16x32_bf16 v[28:31], v[168:171], v[108:111], v[28:31]\n"
    "v_mfma_f32_16x16x32_bf16 v[28:31], v[172:175], v[116:119], v[28:31]\n"
    "v_mfma_f32_16x16x32_bf16 v[28:31], v[176:179], v[124:127], v[28:31]\n"
    "s_waitcnt lgkmcnt(3)\n"
    "v_mfma_f32_16x16x32_bf16 v[32:35], v[180:183], v[96:99], v[32:35]\n"
    "ds_read_b128 v[200:203], v251 offset:1024\n"
    "v_mfma_f32_16x16x32_bf16 v[32:35], v[184:187], v[104:107], v[32:35]\n"
    "ds_read_b128 v[168:171], v251 offset:2048\n"
    "s_waitcnt lgkmcnt(3)\n"
    "v_mfma_f32_16x16x32_bf16 v[32:35], v[188:191], v[112:115], v[32:35]\n"
    "ds_read_b128 v[172:175], v251 offset:3072\n"
    "v_mfma_f32_16x16x32_bf16 v[32:35], v[192:195], v[120:123], v[32:35]\n"
    "ds_read_b128 v[176:179], v251 offset:4096\n"
    "v_mfma_f32_16x16x32_bf16 v[36:39], v[180:183], v[100:103], v[36:39]\n"
    "v_cvt_pk_bf16_f32 v204, v24, v25\n"
    "v_cvt_pk_bf16_f32 v205, v26, v27\n"
    "v_mfma_f32_16x16x32_bf16 v[36:39], v[184:187], v[108:111], v[36:39]\n"
    "v_cvt_pk_bf16_f32 v206, v28, v29\n"
    "v_cvt_pk_bf16_f32 v207, v30, v31\n"
    "v_mfma_f32_16x16x32_bf16 v[36:39], v[188:191], v[116:119], v[36:39]\n"
    "buffer_store_dwordx4 v[204:207], v1, s[16:19], s79 offen offset:0 sc1\n"
    "v_mfma_f32_16x16x32_bf16 v[36:39], v[192:195], v[124:127], v[36:39]\n"
    "s_waitcnt lgkmcnt(3)\n"
    "v_mfma_f32_16x16x32_bf16 v[40:43], v[196:199], v[128:131], v[40:43]\n"
    "ds_read_b128 v[180:183], v251 offset:5120\n"
    "v_mfma_f32_16x16x32_bf16 v[40:43], v[200:203], v[136:139], v[40:43]\n"
    "ds_read_b128 v[184:187], v251 offset:6144\n"
    "s_waitcnt lgkmcnt(3)\n"
    "v_mfma_f32_16x16x32_bf16 v[40:43], v[168:171], v[144:147], v[40:43]\n"
    "ds_read_b128 v[188:191], v251 offset:7168\n"
    "v_mfma_f32_16x16x32_bf16 v[40:43], v[172:175], v[152:155], v[40:43]\n"
    "ds_read_b128 v[192:195], v251 offset:8192\n"
    "v_mfma_f32_16x16x32_bf16 v[44:47], v[196:199], v[132:135], v[44:47]\n"
    "v_cvt_pk_bf16_f32 v208, v32, v33\n"
    "v_cvt_pk_bf16_f32 v209, v34, v35\n"
    "v_mfma_f32_16x16x32_bf16 v[44:47], v[200:203], v[140:143], v[44:47]\n"
    "v_cvt_pk_bf16_f32 v210, v36, v37\n"
    "v_cvt_pk_bf16_f32 v211, v38, v39\n"
    "v_mfma_f32_16x16x32_bf16 v[44:47], v[168:171], v[148:151], v[44:47]\n"
    "buffer_store_dwordx4 v[208:211], v1, s[16:19], s79 offen offset:1024 sc1\n"
    "v_mfma_f32_16x16x32_bf16 v[44:47], v[172:175], v[156:159], v[44:47]\n"
    "s_waitcnt lgkmcnt(3)\n"
    "v_mfma_f32_16x16x32_bf16 v[40:43], v[176:179], v[96:99], v[40:43]\n"
    "ds_read_b128 v[196:199], v251 offset:9216\n"
    "v_mfma_f32_16x16x32_bf16 v[40:43], v[180:183], v[104:107], v[40:43]\n"
    "ds_read_b128 v[200:203], v251 offset:10240\n"
    "s_waitcnt lgkmcnt(3)\n"
    "v_mfma_f32_16x16x32_bf16 v[40:43], v[184:187], v[112:115], v[40:43]\n"
    "ds_read_b128 v[168:171], v251 offset:11264\n"
    "v_mfma_f32_16x16x32_bf16 v[40:43], v[188:191], v[120:123], v[40:43]\n"
    "ds_read_b128 v[172:175], v251 offset:12288\n"
    "v_mfma_f32_16x16x32_bf16 v[44:47], v[176:179], v[100:103], v[44:47]\n"
    "v_mfma_f32_16x16x32_bf16 v[44:47], v[180:183], v[108:111], v[44:47]\n"
    "v_mfma_f32_16x16x32_bf16 v[44:47], v[184:187], v[116:119], v[44:47]\n"
    "v_mfma_f32_16x16x32_bf16 v[44:47], v[188:191], v[124:127], v[44:47]\n"
    "s_waitcnt lgkmcnt(3)\n"
    "v_mfma_f32_16x16x32_bf16 v[48:51], v[192:195], v[128:131], v[48:51]\n"
    "ds_read_b128 v[176:179], v251 offset:13312\n"
    "v_mfma_f32_16x16x32_bf16 v[48:51], v[196:199], v[136:139], v[48:51]\n"
    "ds_read_b128 v[180:183], v251 offset:14336\n"
    "s_waitcnt lgkmcnt(3)\n"
    "v_mfma_f32_16x16x32_bf16 v[48:51], v[200:203], v[144:147], v[48:51]\n"
    "ds_read_b128 v[184:187], v251 offset:15360\n"
    "v_mfma_f32_16x16x32_bf16 v[48:51], v[168:171], v[152:155], v[48:51]\n"
    "ds_read_b128 v[188:191], v251 offset:16384\n"
    "v_mfma_f32_16x16x32_bf16 v[52:55], v[192:195], v[132:135], v[52:55]\n"
    "v_cvt_pk_bf16_f32 v212, v40, v41\n"
    "v_cvt_pk_bf16_f32 v213, v42, v43\n"
    "v_mfma_f32_16x16x32_bf16 v[52:55], v[196:199], v[140:143], v[52:55]\n"
    "v_cvt_pk_bf16_f32 v214, v44, v45\n"
    "v_cvt_pk_bf16_f32 v215, v46, v47\n"
    "v_mfma_f32_16x16x32_bf16 v[52:55], v[200:203], v[148:151], v[52:55]\n"
    "buffer_store_dwordx4 v[212:215], v1, s[16:19], s80 offen offset:0 sc1\n"
    "v_mfma_f32_16x16x32_bf16 v[52:55], v[168:171], v[156:159], v[52:55]\n"
    "s_waitcnt lgkmcnt(3)\n"
    "v_mfma_f32_16x16x32_bf16 v[48:51], v[172:175], v[96:99], v[48:51]\n"
    "ds_read_b128 v[192:195], v251 offset:17408\n"
    "v_mfma_f32_16x16x32_bf16 v[48:51], v[176:179], v[104:107], v[48:51]\n"
    "ds_read_b128 v[196:199], v251 offset:18432\n"
    "s_waitcnt lgkmcnt(3)\n"
    "v_mfma_f32_16x16x32_bf16 v[48:51], v[180:183], v[112:115], v[48:51]\n"
    "ds_read_b128 v[200:203], v251 offset:19456\n"
    "v_mfma_f32_16x16x32_bf16 v[48:51], v[184:187], v[120:123], v[48:51]\n"
    "ds_read_b128 v[168:171], v251 offset:20480\n"
    "v_mfma_f32_16x16x32_bf16 v[52:55], v[172:175], v[100:103], v[52:55]\n"
    "v_mfma_f32_16x16x32_bf16 v[52:55], v[176:179], v[108:111], v[52:55]\n"
    "v_mfma_f32_16x16x32_bf16 v[52:55], v[180:183], v[116:119], v[52:55]\n"
    "v_mfma_f32_16x16x32_bf16 v[52:55], v[184:187], v[124:127], v[52:55]\n"
    "s_waitcnt lgkmcnt(3)\n"
    "v_mfma_f32_16x16x32_bf16 v[56:59], v[188:191], v[128:131], v[56:59]\n"
    "ds_read_b128 v[172:175], v251 offset:21504\n"
    "v_mfma_f32_16x16x32_bf16 v[56:59], v[192:195], v[136:139], v[56:59]\n"
    "ds_read_b128 v[176:179], v251 offset:22528\n"
    "s_waitcnt lgkmcnt(3)\n"
    "v_mfma_f32_16x16x32_bf16 v[56:59], v[196:199], v[144:147], v[56:59]\n"
    "ds_read_b128 v[180:183], v251 offset:23552\n"
    "v_mfma_f32_16x16x32_bf16 v[56:59], v[200:203], v[152:155], v[56:59]\n"
    "ds_read_b128 v[184:187], v251 offset:24576\n"
    "v_mfma_f32_16x16x32_bf16 v[60:63], v[188:191], v[132:135], v[60:63]\n"
    "v_cvt_pk_bf16_f32 v204, v48, v49\n"
    "v_cvt_pk_bf16_f32 v205, v50, v51\n"
    "v_mfma_f32_16x16x32_bf16 v[60:63], v[192:195], v[140:143], v[60:63]\n"
    "v_cvt_pk_bf16_f32 v206, v52, v53\n"
    "v_cvt_pk_bf16_f32 v207, v54, v55\n"
    "v_mfma_f32_16x16x32_bf16 v[60:63], v[196:199], v[148:151], v[60:63]\n"
    "buffer_store_dwordx4 v[204:207], v1, s[16:19], s80 offen offset:1024 sc1\n"
    "v_mfma_f32_16x16x32_bf16 v[60:63], v[200:203], v[156:159], v[60:63]\n"
    "s_waitcnt lgkmcnt(3)\n"
    "v_mfma_f32_16x16x32_bf16 v[56:59], v[168:171], v[96:99], v[56:59]\n"
    "ds_read_b128 v[188:191], v251 offset:25600\n"
    "v_mfma_f32_16x16x32_bf16 v[56:59], v[172:175], v[104:107], v[56:59]\n"
    "ds_read_b128 v[192:195], v251 offset:26624\n"
    "s_waitcnt lgkmcnt(3)\n"
    "v_mfma_f32_16x16x32_bf16 v[56:59], v[176:179], v[112:115], v[56:59]\n"
    "ds_read_b128 v[196:199], v251 offset:27648\n"
    "v_mfma_f32_16x16x32_bf16 v[56:59], v[180:183], v[120:123], v[56:59]\n"
    "ds_read_b128 v[200:203], v251 offset:28672\n"
    "v_mfma_f32_16x16x32_bf16 v[60:63], v[168:171], v[100:103], v[60:63]\n"
    "v_mfma_f32_16x16x32_bf16 v[60:63], v[172:175], v[108:111], v[60:63]\n"
    "v_mfma_f32_16x16x32_bf16 v[60:63], v[176:179], v[116:119], v[60:63]\n"
    "v_mfma_f32_16x16x32_bf16 v[60:63], v[180:183], v[124:127], v[60:63]\n"
    "s_waitcnt lgkmcnt(3)\n"
    "v_mfma_f32_16x16x32_bf16 v[64:67], v[184:187], v[128:131], v[64:67]\n"
    "ds_read_b128 v[168:171], v251 offset:29696\n"
    "v_mfma_f32_16x16x32_bf16 v[64:67], v[188:191], v[136:139], v[64:67]\n"
    "ds_read_b128 v[172:175], v251 offset:30720\n"
    "s_waitcnt lgkmcnt(3)\n"
    "v_mfma_f32_16x16x32_bf16 v[64:67], v[192:195], v[144:147], v[64:67]\n"
    "ds_read_b128 v[176:179], v251 offset:31744\n"
    "v_mfma_f32_16x16x32_bf16 v[64:67], v[196:199], v[152:155], v[64:67]\n"
    "s_barrier\n"
    "ds_read_b128 v[180:183], v250 offset:0\n"
    "v_mfma_f32_16x16x32_bf16 v[68:71], v[184:187], v[132:135], v[68:71]\n"
    "v_cvt_pk_bf16_f32 v208, v56, v57\n"
    "v_cvt_pk_bf16_f32 v209, v58, v59\n"
    "v_mfma_f32_16x16x32_bf16 v[68:71], v[188:191], v[140:143], v[68:71]\n"
    "v_cvt_pk_bf16_f32 v210, v60, v61\n"
    "v_cvt_pk_bf16_f32 v211, v62, v63\n"
    "v_mfma_f32_16x16x32_bf16 v[68:71], v[192:195], v[148:151], v[68:71]\n"
    "buffer_store_dwordx4 v[208:211], v1, s[16:19], s81 offen offset:0 sc1\n"
    "v_mfma_f32_16x16x32_bf16 v[68:71], v[196:199], v[156:159], v[68:71]\n"
    "s_waitcnt lgkmcnt(3)\n"
    "v_mfma_f32_16x16x32_bf16 v[64:67], v[200:203], v[96:99], v[64:67]\n"
    "ds_read_b128 v[184:187], v250 offset:1024\n"
    "v_mfma_f32_16x16x32_bf16 v[64:67], v[168:171], v[104:107], v[64:67]\n"
    "ds_read_b128 v[188:191], v250 offset:2048\n"
    "s_waitcnt lgkmcnt(3)\n"
    "v_mfma_f32_16x16x32_bf16 v[64:67], v[172:175], v[112:115], v[64:67]\n"
    "ds_read_b128 v[192:195], v250 offset:3072\n"
    "v_mfma_f32_16x16x32_bf16 v[64:67], v[176:179], v[120:123], v[64:67]\n"
    "ds_read_b128 v[196:199], v250 offset:4096\n"
    "v_mfma_f32_16x16x32_bf16 v[68:71], v[200:203], v[100:103], v[68:71]\n"
    "ds_read_b64 v[220:221], v237 offset:0\n"
    "ds_read_b64 v[222:223], v237 offset:1024\n"
    "v_mfma_f32_16x16x32_bf16 v[68:71], v[168:171], v[108:111], v[68:71]\n"
    "ds_read_b64 v[224:225], v237 offset:512\n"
    "ds_read_b64 v[226:227], v237 offset:1536\n"
    "v_mfma_f32_16x16x32_bf16 v[68:71], v[172:175], v[116:119], v[68:71]\n"
    "ds_read_b64 v[216:217], v240 offset:8192\n"
    "ds_read_b64 v[218:219], v240 offset:8704\n"
    "v_mfma_f32_16x16x32_bf16 v[68:71], v[176:179], v[124:127], v[68:71]\n"
    "ds_read2_b32 v[238:239], v241 offset0:0 offset1:16\n"
    "s_waitcnt lgkmcnt(10)\n"
    "v_mfma_f32_16x16x32_bf16 v[72:75], v[180:183], v[128:131], v[72:75]\n"
    "ds_read_b128 v[200:203], v250 offset:5120\n"
    "v_mfma_f32_16x16x32_bf16 v[72:75], v[184:187], v[136:139], v[72:75]\n"
    "ds_read_b128 v[168:171], v250 offset:6144\n"
    "s_waitcnt lgkmcnt(10)\n"
    "v_mfma_f32_16x16x32_bf16 v[72:75], v[188:191], v[144:147], v[72:75]\n"
    "ds_read_b128 v[172:175], v250 offset:7168\n"
    "v_mfma_f32_16x16x32_bf16 v[72:75], v[192:195], v[152:155], v[72:75]\n"
    "ds_read_b128 v[176:179], v250 offset:8192\n"
    "v_mfma_f32_16x16x32_bf16 v[76:79], v[180:183], v[132:135], v[76:79]\n"
    "v_cvt_pk_bf16_f32 v212, v64, v65\n"
    "v_cvt_pk_bf16_f32 v213, v66, v67\n"
    "v_mfma_f32_16x16x32_bf16 v[76:79], v[184:187], v[140:143], v[76:79]\n"
    "v_cvt_pk_bf16_f32 v214, v68, v69\n"
    "v_cvt_pk_bf16_f32 v215, v70, v71\n"
    "v_mfma_f32_16x16x32_bf16 v[76:79], v[188:191], v[148:151], v[76:79]\n"
    "buffer_store_dwordx4 v[212:215], v1, s[16:19], s81 offen offset:1024 sc1\n"
    "v_mfma_f32_16x16x32_bf16 v[76:79], v[192:195], v[156:159], v[76:79]\n"
    "s_waitcnt lgkmcnt(3)\n"
    "v_mfma_f32_16x16x32_bf16 v[72:75], v[196:199], v[96:99], v[72:75]\n"
    "ds_read_b128 v[180:183], v250 offset:9216\n"
    "v_mfma_f32_16x16x32_bf16 v[72:75], v[200:203], v[104:107], v[72:75]\n"
    "ds_read_b128 v[184:187], v250 offset:10240\n"
    "s_waitcnt lgkmcnt(3)\n"
    "v_mfma_f32_16x16x32_bf16 v[72:75], v[168:171], v[112:115], v[72:75]\n"
    "ds_read_b128 v[188:191], v250 offset:11264\n"
    "v_mfma_f32_16x16x32_bf16 v[72:75], v[172:175], v[120:123], v[72:75]\n"
    "ds_read_b128 v[192:195], v250 offset:12288\n"
    "v_mfma_f32_16x16x32_bf16 v[76:79], v[196:199], v[100:103], v[76:79]\n"
    "buffer_load_dwordx4 v[24:27], v1, s[16:19], s82 offen offset:0 sc1\n"
    "buffer_load_dwordx4 v[28:31], v1, s[16:19], s82 offen offset:1024 sc1\n"
    "v_mfma_f32_16x16x32_bf16 v[76:79], v[200:203], v[108:111], v[76:79]\n"
    "v_mfma_f32_16x16x32_bf16 v[76:79], v[168:171], v[116:119], v[76:79]\n"
    "v_mfma_f32_16x16x32_bf16 v[76:79], v[172:175], v[124:127], v[76:79]\n"
    "s_waitcnt lgkmcnt(3)\n"
    "v_mfma_f32_16x16x32_bf16 v[80:83], v[176:179], v[128:131], v[80:83]\n"
    "ds_read_b128 v[196:199], v250 offset:13312\n"
    "v_mfma_f32_16x16x32_bf16 v[80:83], v[180:183], v[136:139], v[80:83]\n"
    "ds_read_b128 v[200:203], v250 offset:14336\n"
    "s_waitcnt lgkmcnt(3)\n"
    "v_mfma_f32_16x16x32_bf16 v[80:83], v[184:187], v[144:147], v[80:83]\n"
    "ds_read_b128 v[168:171], v250 offset:15360\n"
    "v_mfma_f32_16x16x32_bf16 v[80:83], v[188:191], v[152:155], v[80:83]\n"
    "ds_read_b128 v[172:175], v250 offset:16384\n"
    "v_mfma_f32_16x16x32_bf16 v[84:87], v[176:179], v[132:135], v[84:87]\n"
    "v_cvt_pk_bf16_f32 v204, v72, v73\n"
    "v_cvt_pk_bf16_f32 v205, v74, v75\n"
    "v_mfma_f32_16x16x32_bf16 v[84:87], v[180:183], v[140:143], v[84:87]\n"
    "v_cvt_pk_bf16_f32 v206, v76, v77\n"
    "v_cvt_pk_bf16_f32 v207, v78, v79\n"
    "v_mfma_f32_16x16x32_bf16 v[84:87], v[184:187], v[148:151], v[84:87]\n"
    "buffer_store_dwordx4 v[204:207], v254, s[16:19], 0 offen sc1\n"
    "v_mfma_f32_16x16x32_bf16 v[84:87], v[188:191], v[156:159], v[84:87]\n"
    "s_waitcnt lgkmcnt(3)\n"
    "v_mfma_f32_16x16x32_bf16 v[80:83], v[192:195], v[96:99], v[80:83]\n"
    "ds_read_b128 v[176:179], v250 offset:17408\n"
    "v_mfma_f32_16x16x32_bf16 v[80:83], v[196:199], v[104:107], v[80:83]\n"
    "ds_read_b128 v[180:183], v250 offset:18432\n"
    "s_waitcnt lgkmcnt(3)\n"
    "v_mfma_f32_16x16x32_bf16 v[80:83], v[200:203], v[112:115], v[80:83]\n"
    "ds_read_b128 v[184:187], v250 offset:19456\n"
    "v_mfma_f32_16x16x32_bf16 v[80:83], v[168:171], v[120:123], v[80:83]\n"
    "ds_read_b128 v[188:191], v250 offset:20480\n"
    "v_mfma_f32_16x16x32_bf16 v[84:87], v[192:195], v[100:103], v[84:87]\n"
    "v_mfma_f32_16x16x32_bf16 v[84:87], v[196:199], v[108:111], v[84:87]\n"
    "v_mfma_f32_16x16x32_bf16 v[84:87], v[200:203], v[116:119], v[84:87]\n"
    "v_mfma_f32_16x16x32_bf16 v[84:87], v[168:171], v[124:127], v[84:87]\n"
    "s_waitcnt lgkmcnt(3)\n"
    "v_mfma_f32_16x16x32_bf16 v[88:91], v[172:175], v[128:131], v[88:91]\n"
    "ds_read_b128 v[192:195], v250 offset:21504\n"
    "v_mfma_f32_16x16x32_bf16 v[88:91], v[176:179], v[136:139], v[88:91]\n"
    "ds_read_b128 v[196:199], v250 offset:22528\n"
    "s_waitcnt lgkmcnt(3)\n"
    "v_mfma_f32_16x16x32_bf16 v[88:91], v[180:183], v[144:147], v[88:91]\n"
    "ds_read_b128 v[200:203], v250 offset:23552\n"
    "v_mfma_f32_16x16x32_bf16 v[88:91], v[184:187], v[152:155], v[88:91]\n"
    "v_mfma_f32_16x16x32_bf16 v[92:95], v[172:175], v[132:135], v[92:95]\n"
    "buffer_load_dwordx4 v[32:35], v1, s[16:19], s83 offen offset:0 sc1\n"
    "buffer_load_dwordx4 v[36:39], v1, s[16:19], s83 offen offset:1024 sc1\n"
    "v_mfma_f32_16x16x32_bf16 v[92:95], v[176:179], v[140:143], v[92:95]\n"
    "v_mfma_f32_16x16x32_bf16 v[92:95], v[180:183], v[148:151], v[92:95]\n"
    "v_mfma_f32_16x16x32_bf16 v[92:95], v[184:187], v[156:159], v[92:95]\n"
    "s_waitcnt lgkmcnt(2)\n"
    "v_mfma_f32_16x16x32_bf16 v[88:91], v[188:191], v[96:99], v[88:91]\n"
    "v_mfma_f32_16x16x32_bf16 v[88:91], v[192:195], v[104:107], v[88:91]\n"
    "s_waitcnt lgkmcnt(0)\n"
    "v_mfma_f32_16x16x32_bf16 v[88:91], v[196:199], v[112:115], v[88:91]\n"
    "v_mfma_f32_16x16x32_bf16 v[88:91], v[200:203], v[120:123], v[88:91]\n"
    "v_mfma_f32_16x16x32_bf16 v[92:95], v[188:191], v[100:103], v[92:95]\n"
    "v_mfma_f32_16x16x32_bf16 v[92:95], v[192:195], v[108:111], v[92:95]\n"
    "v_mfma_f32_16x16x32_bf16 v[92:95], v[196:199], v[116:119], v[92:95]\n"
    "v_mfma_f32_16x16x32_bf16 v[92:95], v[200:203], v[124:127], v[92:95]\n"
    "s_waitcnt vmcnt(5)\n"
    "buffer_load_dwordx4 v[40:43], v1, s[16:19], s84 offen offset:0 sc1\n"
    "buffer_load_dwordx4 v[44:47], v1, s[16:19], s84 offen offset:1024 sc1\n"
    "s_waitcnt vmcnt(4)\n"
    "buffer_load_dwordx4 v[48:51], v255, s[16:19], 0 offen offset:0 sc1\n"
    "buffer_load_dwordx4 v[52:55], v255, s[16:19], 0 offen offset:1024 sc1\n"
    "buffer_load_dwordx4 v[56:59], v255, s[16:19], 0 offen offset:2048 sc1\n"
    "v_lshlrev_b32 v128, 16, v216\n"
    "v_and_b32 v129, 0xffff0000, v216\n"
    "v_lshlrev_b32 v130, 16, v217\n"
    "v_and_b32 v131, 0xffff0000, v217\n"
    "v_lshlrev_b32 v132, 16, v218\n"
    "v_and_b32 v133, 0xffff0000, v218\n"
    "v_lshlrev_b32 v134, 16, v219\n"
    "v_and_b32 v135, 0xffff0000, v219\n"
    "v_lshlrev_b32 v136, 16, v220\n"
    "v_and_b32 v137, 0xffff0000, v220\n"
    "v_lshlrev_b32 v138, 16, v221\n"
    "v_and_b32 v139, 0xffff0000, v221\n"
    "v_lshlrev_b32 v140, 16, v222\n"
    "v_and_b32 v141, 0xffff0000, v222\n"
    "v_lshlrev_b32 v142, 16, v223\n"
    "v_and_b32 v143, 0xffff0000, v223\n"
    "v_lshlrev_b32 v144, 16, v224\n"
    "v_and_b32 v145, 0xffff0000, v224\n"
    "v_lshlrev_b32 v146, 16, v225\n"
    "v_and_b32 v147, 0xffff0000, v225\n"
    "v_lshlrev_b32 v148, 16, v226\n"
    "v_and_b32 v149, 0xffff0000, v226\n"
    "v_lshlrev_b32 v150, 16, v227\n"
    "v_and_b32 v151, 0xffff0000, v227\n"
    "kb_bk54_%=:\n"
    "v_max3_u32 v236, v24, v25, v26\n"
    "v_max3_u32 v236, v27, v28, v236\n"
    "v_max3_u32 v236, v29, v30, v236\n"
    "v_max_u32 v236, v31, v236\n"
    "v_cmp_eq_u32 vcc, -1, v236\n"
    "s_cbranch_vccnz kb_rt55_%=\n"
    "kb_bk54_%=x:\n"
    "v_mfma_f32_16x16x32_bf16 v[80:83], v[4:7], v[24:27], v[80:83]\n"
    "v_mfma_f32_16x16x32_bf16 v[84:87], v[2:5], v[24:27], v[84:87]\n"
    "v_mfma_f32_16x16x32_bf16 v[88:91], v[4:7], v[28:31], v[88:91]\n"
    "v_mfma_f32_16x16x32_bf16 v[92:95], v[2:5], v[28:31], v[92:95]\n"
    "s_waitcnt vmcnt(5)\n"
    "kb_bk60_%=:\n"
    "v_max3_u32 v236, v32, v33, v34\n"
    "v_max3_u32 v236, v35, v36, v236\n"
    "v_max3_u32 v236, v37, v38, v236\n"
    "v_max_u32 v236, v39, v236\n"
    "v_cmp_eq_u32 vcc, -1, v236\n"
    "s_cbranch_vccnz kb_rt61_%=\n"
    "kb_bk60_%=x:\n"
    "v_mfma_f32_16x16x32_bf16 v[80:83], v[4:7], v[32:35], v[80:83]\n"
    "v_mfma_f32_16x16x32_bf16 v[84:87], v[2:5], v[32:35], v[84:87]\n"
    "v_mfma_f32_16x16x32_bf16 v[88:91], v[4:7], v[36:39], v[88:91]\n"
    "v_mfma_f32_16x16x32_bf16 v[92:95], v[2:5], v[36:39], v[92:95]\n"
    "s_waitcnt vmcnt(3)\n"
    "kb_bk65_%=:\n"
    "v_max3_u32 v236, v40, v41, v42\n"
    "v_max3_u32 v236, v43, v44, v236\n"
    "v_max3_u32 v236, v45, v46, v236\n"
    "v_max_u32 v236, v47, v236\n"
    "v_cmp_eq_u32 vcc, -1, v236\n"
    "s_cbranch_vccnz kb_rt66_%=\n"
    "kb_bk65_%=x:\n"
    "v_mfma_f32_16x16x32_bf16 v[80:83], v[4:7], v[40:43], v[80:83]\n"
    "v_mfma_f32_16x16x32_bf16 v[84:87], v[2:5], v[40:43], v[84:87]\n"
    "v_mfma_f32_16x16x32_bf16 v[88:91], v[4:7], v[44:47], v[88:91]\n"
    "v_mfma_f32_16x16x32_bf16 v[92:95], v[2:5], v[44:47], v[92:95]\n"
    "s_nop 4\n"
    "v_pk_fma_f32 v[80:81], v[80:81], v[238:239], v[136:137] op_sel:[0,0,0] op_sel_hi:[1,0,1]\n"
    "v_pk_fma_f32 v[82:83], v[82:83], v[238:239], v[138:139] op_sel:[0,0,0] op_sel_hi:[1,0,1]\n"
    "v_pk_fma_f32 v[84:85], v[84:85], v[238:239], v[140:141] op_sel:[0,1,0] op_sel_hi:[1,1,1]\n"
    "v_pk_fma_f32 v[86:87], v[86:87], v[238:239], v[142:143] op_sel:[0,1,0] op_sel_hi:[1,1,1]\n"
    "v_cvt_pk_bf16_f32 v220, v80, v81\n"
    "v_cvt_pk_bf16_f32 v221, v82, v83\n"
    "v_cvt_pk_bf16_f32 v222, v84, v85\n"
    "v_cvt_pk_bf16_f32 v223, v86, v87\n"
    "s_nop 0\n"
    "v_permlane16_swap_b32 v220, v222\n"
    "v_permlane16_swap_b32 v221, v223\n"
    "buffer_store_dwordx4 v[220:223], v8, s[24:27], 0 offen offset:0\n"
    "v_pk_fma_f32 v[88:89], v[88:89], v[238:239], v[144:145] op_sel:[0,0,0] op_sel_hi:[1,0,1]\n"
    "v_pk_fma_f32 v[90:91], v[90:91], v[238:239], v[146:147] op_sel:[0,0,0] op_sel_hi:[1,0,1]\n"
    "v_pk_fma_f32 v[92:93], v[92:93], v[238:239], v[148:149] op_sel:[0,1,0] op_sel_hi:[1,1,1]\n"
    "v_pk_fma_f32 v[94:95], v[94:95], v[238:239], v[150:151] op_sel:[0,1,0] op_sel_hi:[1,1,1]\n"
    "v_cvt_pk_bf16_f32 v224, v88, v89\n"
    "v_cvt_pk_bf16_f32 v225, v90, v91\n"
    "v_cvt_pk_bf16_f32 v226, v92, v93\n"
    "v_cvt_pk_bf16_f32 v227, v94, v95\n"
    "s_nop 0\n"
    "v_permlane16_swap_b32 v224, v226\n"
    "v_permlane16_swap_b32 v225, v227\n"
    "buffer_store_dwordx4 v[224:227], v8, s[24:27], 0 offen offset:32\n"
    "s_waitcnt vmcnt(2)\n"
    "kb_bk70_%=:\n"
    "v_max3_u32 v236, v48, v49, v50\n"
    "v_max3_u32 v236, v51, v52, v236\n"
    "v_max3_u32 v236, v53, v54, v236\n"
    "v_max3_u32 v236, v55, v56, v236\n"
    "v_max3_u32 v236, v57, v58, v236\n"
    "v_max_u32 v236, v59, v236\n"
    "v_cmp_eq_u32 vcc, -1, v236\n"
    "s_cbranch_vccnz kb_rt71_%=\n"
    "kb_bk70_%=x:\n"
    "v_mfma_f32_16x16x32_bf16 v[72:75], v[4:7], v[48:51], v[72:75]\n"
    "v_mfma_f32_16x16x32_bf16 v[76:79], v[2:5], v[48:51], v[76:79]\n"
    "v_mfma_f32_16x16x32_bf16 v[72:75], v[4:7], v[52:55], v[72:75]\n"
    "v_mfma_f32_16x16x32_bf16 v[76:79], v[2:5], v[52:55], v[76:79]\n"
    "v_mfma_f32_16x16x32_bf16 v[72:75], v[4:7], v[56:59], v[72:75]\n"
    "v_mfma_f32_16x16x32_bf16 v[76:79], v[2:5], v[56:59], v[76:79]\n"
    "s_nop 6\n"
    "v_pk_fma_f32 v[72:73], v[72:73], v[238:239], v[128:129] op_sel:[0,0,0] op_sel_hi:[1,0,1]\n"
    "v_pk_fma_f32 v[74:75], v[74:75], v[238:239], v[130:131] op_sel:[0,0,0] op_sel_hi:[1,0,1]\n"
    "v_pk_fma_f32 v[76:77], v[76:77], v[238:239], v[132:133] op_sel:[0,1,0] op_sel_hi:[1,1,1]\n"
    "v_pk_fma_f32 v[78:79], v[78:79], v[238:239], v[134:135] op_sel:[0,1,0] op_sel_hi:[1,1,1]\n"
    "v_cvt_pk_bf16_f32 v216, v72, v73\n"
    "v_cvt_pk_bf16_f32 v217, v74, v75\n"
    "v_cvt_pk_bf16_f32 v218, v76, v77\n"
    "v_cvt_pk_bf16_f32 v219, v78, v79\n"
    "s_nop 0\n"
    "v_permlane16_swap_b32 v216, v218\n"
    "v_permlane16_swap_b32 v217, v219\n"
    "buffer_store_dwordx4 v[216:219], v9, s[24:27], 0 offen offset:0\n"
    "s_branch kb_fin72_%=\n"
    "kb_two32_%=:\n"
    "s_waitcnt lgkmcnt(15)\n"
    "v_mfma_f32_16x16x32_bf16 v[108:111], v[148:151], v[28:31], 0\n"
    "s_add_u32 m0, s98, 36864\n"
    "s_add_u32 s40, s98, 0\n"
    "buffer_load_dwordx4 v1, s[20:23], s40 offen offset:0 sc1 lds\n"
    "v_mfma_f32_16x16x32_bf16 v[104:107], v[148:151], v[24:27], 0\n"
    "v_mfma_f32_16x16x32_bf16 v[116:119], v[152:155], v[28:31], 0\n"
    "buffer_load_dwordx4 v1, s[20:23], s40 offen offset:1024 sc1 lds\n"
    "v_mfma_f32_16x16x32_bf16 v[112:115], v[152:155], v[24:27], 0\n"
    "s_waitcnt lgkmcnt(14)\n"
    "v_mfma_f32_16x16x32_bf16 v[108:111], v[160:163], v[36:39], v[108:111]\n"
    "buffer_load_dwordx4 v1, s[20:23], s40 offen offset:2048 sc1 lds\n"
    "v_mfma_f32_16x16x32_bf16 v[104:107], v[160:163], v[32:35], v[104:107]\n"
    "v_mfma_f32_16x16x32_bf16 v[116:119], v[164:167], v[36:39], v[116:119]\n"
    "buffer_load_dwordx4 v1, s[20:23], s40 offen offset:3072 sc1 lds\n"
    "v_mfma_f32_16x16x32_bf16 v[112:115], v[164:167], v[32:35], v[112:115]\n"
    "s_waitcnt lgkmcnt(12)\n"
    "v_mfma_f32_16x16x32_bf16 v[108:111], v[172:175], v[44:47], v[108:111]\n"
    "s_add_u32 m0, s98, 40960\n"
    "s_add_u32 s40, s98, 4096\n"
    "buffer_load_dwordx4 v1, s[20:23], s40 offen offset:0 sc1 lds\n"
    "v_mfma_f32_16x16x32_bf16 v[104:107], v[172:175], v[40:43], v[104:107]\n"
    "v_mfma_f32_16x16x32_bf16 v[116:119], v[176:179], v[44:47], v[116:119]\n"
    "buffer_load_dwordx4 v1, s[20:23], s40 offen offset:1024 sc1 lds\n"
    "v_mfma_f32_16x16x32_bf16 v[112:115], v[176:179], v[40:43], v[112:115]\n"
    "s_waitcnt lgkmcnt(10)\n"
    "v_mfma_f32_16x16x32_bf16 v[108:111], v[184:187], v[52:55], v[108:111]\n"
    "buffer_load_dwordx4 v1, s[20:23], s40 offen offset:2048 sc1 lds\n"
    "v_mfma_f32_16x16x32_bf16 v[104:107], v[184:187], v[48:51], v[104:107]\n"
    "v_mfma_f32_16x16x32_bf16 v[116:119], v[188:191], v[52:55], v[116:119]\n"
    "buffer_load_dwordx4 v1, s[20:23], s40 offen offset:3072 sc1 lds\n"
    "v_mfma_f32_16x16x32_bf16 v[112:115], v[188:191], v[48:51], v[112:115]\n"
    "s_waitcnt lgkmcnt(8)\n"
    "v_mfma_f32_16x16x32_bf16 v[108:111], v[196:199], v[60:63], v[108:111]\n"
    "s_add_u32 m0, s98, 45056\n"
    "s_add_u32 s40, s98, 8192\n"
    "buffer_load_dwordx4 v1, s[20:23], s40 offen offset:0 sc1 lds\n"
    "v_mfma_f32_16x16x32_bf16 v[104:107], v[196:199], v[56:59], v[104:107]\n"
    "v_mfma_f32_16x16x32_bf16 v[116:119], v[200:203], v[60:63], v[116:119]\n"
    "v_mfma_f32_16x16x32_bf16 v[112:115], v[200:203], v[56:59], v[112:115]\n"
    "s_waitcnt lgkmcnt(6)\n"
    "v_mfma_f32_16x16x32_bf16 v[108:111], v[208:211], v[68:71], v[108:111]\n"
    "v_mfma_f32_16x16x32_bf16 v[104:107], v[208:211], v[64:67], v[104:107]\n"
    "v_mfma_f32_16x16x32_bf16 v[116:119], v[212:215], v[68:71], v[116:119]\n"
    "v_mfma_f32_16x16x32_bf16 v[112:115], v[212:215], v[64:67], v[112:115]\n"
    "s_waitcnt lgkmcnt(4)\n"
    "v_mfma_f32_16x16x32_bf16 v[108:111], v[220:223], v[76:79], v[108:111]\n"
    "v_mfma_f32_16x16x32_bf16 v[104:107], v[220:223], v[72:75], v[104:107]\n"
    "v_mfma_f32_16x16x32_bf16 v[116:119], v[224:227], v[76:79], v[116:119]\n"
    "v_mfma_f32_16x16x32_bf16 v[112:115], v[224:227], v[72:75], v[112:115]\n"
    "s_waitcnt lgkmcnt(2)\n"
    "v_mfma_f32_16x16x32_bf16 v[108:111], v[232:235], v[84:87], v[108:111]\n"
    "v_mfma_f32_16x16x32_bf16 v[104:107], v[232:235], v[80:83], v[104:107]\n"
    "v_mfma_f32_16x16x32_bf16 v[116:119], v[236:239], v[84:87], v[116:119]\n"
    "v_mfma_f32_16x16x32_bf16 v[112:115], v[236:239], v[80:83], v[112:115]\n"
    "s_waitcnt lgkmcnt(0)\n"
    "v_mfma_f32_16x16x32_bf16 v[108:111], v[244:247], v[92:95], v[108:111]\n"
    "v_mfma_f32_16x16x32_bf16 v[104:107], v[244:247], v[88:91], v[104:107]\n"
    "v_mfma_f32_16x16x32_bf16 v[116:119], v[248:251], v[92:95], v[116:119]\n"
    "v_mfma_f32_16x16x32_bf16 v[112:115], v[248:251], v[88:91], v[112:115]\n"
    "v_lshlrev_b32 v24, 16, v140\n"
    "v_and_b32 v25, 0xffff0000, v140\n"
    "v_lshlrev_b32 v26, 16, v141\n"
    "v_and_b32 v27, 0xffff0000, v141\n"
    "v_lshlrev_b32 v28, 16, v142\n"
    "v_and_b32 v29, 0xffff0000, v142\n"
    "v_lshlrev_b32 v30, 16, v143\n"
    "v_and_b32 v31, 0xffff0000, v143\n"
    "v_xor_b32 v133, 0x1000, v3\n"
    "ds_write_b128 v3, v[108:111] offset:0\n"
    "ds_write_b128 v3, v[116:119] offset:1024\n"
    "v_add_f32 v128, v112, v113\n"
    "ds_write_b32 v16, v128\n"
    "v_add_f32 v129, v116, v117\n"
    "ds_write_b32 v17, v129\n"
    "s_waitcnt lgkmcnt(0)\n"
    "s_barrier\n"
    "s_setprio 1\n"
    "ds_read2st64_b32 v[128:129], v18 offset0:0 offset1:1\n"
    "ds_read_b128 v[108:111], v133 offset:0\n"
    "ds_read_b128 v[116:119], v133 offset:1024\n"
    "s_waitcnt lgkmcnt(2)\n"
    "v_add_f32 v128, v128, v129\n"
    "v_add_f32 v128, v128, v146\n"
    "v_cvt_pk_bf16_f32 v132, 0, v128\n"
    "s_waitcnt lgkmcnt(1)\n"
    "v_pk_add_f32 v[104:105], v[104:105], v[108:109]\n"
    "v_pk_add_f32 v[106:107], v[106:107], v[110:111]\n"
    "v_cvt_pk_bf16_f32 v104, 0, v104\n"
    "v_cvt_pk_bf16_f32 v105, 0, v105\n"
    "v_cvt_pk_bf16_f32 v106, 0, v106\n"
    "v_cvt_pk_bf16_f32 v107, 0, v107\n"
    "v_pk_add_f32 v[104:105], v[104:105], v[24:25]\n"
    "v_pk_add_f32 v[106:107], v[106:107], v[26:27]\n"
    "v_cvt_pk_bf16_f32 v242, v104, v105\n"
    "v_cvt_pk_bf16_f32 v243, v106, v107\n"
    "v_lshlrev_b32 v104, 16, v242\n"
    "v_and_b32 v105, 0xffff0000, v242\n"
    "v_lshlrev_b32 v106, 16, v243\n"
    "v_and_b32 v107, 0xffff0000, v243\n"
    "v_pk_add_f32 v[104:105], v[104:105], v[132:133] op_sel_hi:[1,0] neg_lo:[0,1] neg_hi:[0,1]\n"
    "v_pk_add_f32 v[106:107], v[106:107], v[132:133] op_sel_hi:[1,0] neg_lo:[0,1] neg_hi:[0,1]\n"
    "v_cvt_pk_bf16_f32 v108, v104, v105\n"
    "v_cvt_pk_bf16_f32 v109, v106, v107\n"
    "buffer_store_dwordx2 v[108:109], v19, s[16:19], 0 offen offset:0 sc1\n"
    "s_waitcnt lgkmcnt(0)\n"
    "v_pk_add_f32 v[112:113], v[112:113], v[116:117]\n"
    "v_pk_add_f32 v[114:115], v[114:115], v[118:119]\n"
    "v_cvt_pk_bf16_f32 v112, 0, v112\n"
    "v_cvt_pk_bf16_f32 v113, 0, v113\n"
    "v_cvt_pk_bf16_f32 v114, 0, v114\n"
    "v_cvt_pk_bf16_f32 v115, 0, v115\n"
    "v_pk_add_f32 v[112:113], v[112:113], v[28:29]\n"
    "v_pk_add_f32 v[114:115], v[114:115], v[30:31]\n"
    "v_cvt_pk_bf16_f32 v244, v112, v113\n"
    "v_cvt_pk_bf16_f32 v245, v114, v115\n"
    "v_lshlrev_b32 v112, 16, v244\n"
    "v_and_b32 v113, 0xffff0000, v244\n"
    "v_lshlrev_b32 v114, 16, v245\n"
    "v_and_b32 v115, 0xffff0000, v245\n"
    "v_pk_add_f32 v[112:113], v[112:113], v[132:133] op_sel_hi:[1,0] neg_lo:[0,1] neg_hi:[0,1]\n"
    "v_pk_add_f32 v[114:115], v[114:115], v[132:133] op_sel_hi:[1,0] neg_lo:[0,1] neg_hi:[0,1]\n"
    "v_cvt_pk_bf16_f32 v116, v112, v113\n"
    "v_cvt_pk_bf16_f32 v117, v114, v115\n"
    "buffer_store_dwordx2 v[116:117], v20, s[16:19], 0 offen offset:512 sc1\n"
    "s_setprio 0\n"
    "v_mul_f32 v112, s45, v112\n"
    "v_mul_f32 v113, s45, v113\n"
    "v_mul_f32 v114, s45, v114\n"
    "v_mul_f32 v115, s45, v115\n"
    "v_mov_b32 v248, v104\n"
    "v_mul_f32 v249, v104, v104\n"
    "v_add_f32 v248, v248, v105\n"
    "v_fmac_f32 v249, v105, v105\n"
    "v_add_f32 v248, v248, v106\n"
    "v_fmac_f32 v249, v106, v106\n"
    "v_add_f32 v248, v248, v107\n"
    "v_fmac_f32 v249, v107, v107\n"
    "v_add_f32 v248, v248, v112\n"
    "v_fmac_f32 v249, v112, v112\n"
    "v_add_f32 v248, v248, v113\n"
    "v_fmac_f32 v249, v113, v113\n"
    "v_add_f32 v248, v248, v114\n"
    "v_fmac_f32 v249, v114, v114\n"
    "v_add_f32 v248, v248, v115\n"
    "v_fmac_f32 v249, v115, v115\n"
    "v_mov_b32 v128, v248\n"
    "v_mov_b32 v129, v249\n"
    "s_nop 0\n"
    "v_permlane16_swap_b32 v248, v128\n"
    "v_permlane16_swap_b32 v249, v129\n"
    "v_add_f32 v248, v248, v128\n"
    "v_add_f32 v249, v249, v129\n"
    "v_mov_b32 v128, v248\n"
    "v_mov_b32 v129, v249\n"
    "s_nop 0\n"
    "v_permlane32_swap_b32 v248, v128\n"
    "v_permlane32_swap_b32 v249, v129\n"
    "v_add_f32 v248, v248, v128\n"
    "v_add_f32 v249, v249, v129\n"
    "s_waitcnt vmcnt(2)\n"
    "s_barrier\n"
    "s_setprio 3\n"
    "v_mov_b32 v17, s60\n"
    "s_add_u32 m0, s98, 73728\n"
    "s_add_u32 s40, s98, 36864\n"
    "buffer_load_dwordx4 v1, s[20:23], s40 offen offset:0 sc1 lds\n"
    "buffer_load_dwordx4 v1, s[20:23], s40 offen offset:1024 sc1 lds\n"
    "buffer_load_dwordx4 v1, s[20:23], s40 offen offset:2048 sc1 lds\n"
    "buffer_load_dwordx4 v1, s[20:23], s40 offen offset:3072 sc1 lds\n"
    "s_add_u32 m0, s98, 77824\n"
    "s_add_u32 s40, s98, 40960\n"
    "buffer_load_dwordx4 v1, s[20:23], s40 offen offset:0 sc1 lds\n"
    "buffer_load_dwordx4 v1, s[20:23], s40 offen offset:1024 sc1 lds\n"
    "buffer_load_dwordx4 v1, s[20:23], s40 offen offset:2048 sc1 lds\n"
    "buffer_load_dwordx4 v1, s[20:23], s40 offen offset:3072 sc1 lds\n"
    "s_add_u32 m0, s98, 81920\n"
    "s_add_u32 s40, s98, 45056\n"
    "buffer_load_dwordx4 v1, s[20:23], s40 offen offset:0 sc1 lds\n"
    "s_and_b32 s77, s3, 1\n"
    "s_lshl_b32 s77, s77, 10\n"
    "s_add_u32 s80, s94, 1\n"
    "s_and_b32 s80, s80, 3\n"
    "s_lshl_b32 s80, s80, 11\n"
    "s_add_u32 s81, s94, 2\n"
    "s_and_b32 s81, s81, 3\n"
    "s_lshl_b32 s81, s81, 11\n"
    "s_add_u32 s82, s94, 3\n"
    "s_and_b32 s82, s82, 3\n"
    "s_lshl_b32 s82, s82, 11\n"
    "s_add_u32 s83, s94, 4\n"
    "s_and_b32 s83, s83, 3\n"
    "s_lshl_b32 s83, s83, 11\n"
    "s_mul_i32 s38, s94, 36\n"
    "s_and_b32 s39, s3, 1\n"
    "s_mul_i32 s39, s39, 9\n"
    "s_add_u32 s38, s38, s39\n"
    "s_mul_i32 s38, s38, 36\n"
    "s_mul_i32 s39, s88, 9\n"
    "s_add_u32 s38, s38, s39\n"
    "s_lshl_b32 s38, s38, 10\n"
    "s_add_u32 s38, s38, 0x1a60000\n"
    "s_add_u32 s24, s4, s38\n"
    "s_addc_u32 s25, s5, 0\n"
    "s_and_b32 s25, s25, 0xffff\n"
    "s_mov_b32 s26, 967680\n"
    "s_mov_b32 s27, 0x20000\n"
    "s_cmp_ge_u32 s3, 6\n"
    "s_cbranch_scc1 kb_aux73_%=\n"
    "s_and_b32 s76, s3, 1\n"
    "s_mul_i32 s76, s76, 0x4800\n"
    "s_and_b32 s78, s3, 1\n"
    "s_lshl_b32 s78, s78, 13\n"
    "s_add_u32 s78, s78, 0x4800\n"
    "s_add_u32 s40, s76, 0x1b000\n"
    "s_mov_b32 s41, 0\n"
    "s_mov_b32 s42, 0\n"
    "s_mov_b32 s43, 1\n"
    "s_and_b32 s84, s3, 1\n"
    "s_lshl_b32 s84, s84, 1\n"
    "s_lshl2_add_u32 s84, s95, s84\n"
    "s_mul_i32 s84, s84, 0x9000\n"
    "s_mul_i32 s39, s88, 0x1200\n"
    "s_add_u32 s84, s84, s39\n"
    "s_lshl_b32 s39, s94, 10\n"
    "s_add_u32 s84, s84, s39\n"
    "s_add_u32 s84, s84, 0x201d000\n"
    "s_lshl2_add_u32 s39, s95, s94\n"
    "s_mul_i32 s39, s39, 0x9000\n"
    "s_mul_i32 s51, s88, 0x1200\n"
    "s_add_u32 s39, s39, s51\n"
    "s_add_u32 s39, s39, 0x201e000\n"
    "v_lshrrev_b32 v9, 5, v0\n"
    "v_mul_u32_u24 v9, 0x4800, v9\n"
    "v_add_u32 v9, s39, v9\n"
    "v_and_b32 v12, 31, v0\n"
    "v_lshl_add_u32 v9, v12, 4, v9\n"
    "s_mul_i32 s38, s95, 0x90\n"
    "s_mul_i32 s39, s94, 36\n"
    "s_and_b32 s51, s3, 1\n"
    "s_mul_i32 s51, s51, 9\n"
    "s_add_u32 s39, s39, s51\n"
    "s_add_u32 s38, s38, s39\n"
    "s_lshl_b32 s38, s38, 13\n"
    "s_add_u32 s38, s38, 0x225d000\n"
    "s_add_u32 s28, s4, s38\n"
    "s_addc_u32 s29, s5, 0\n"
    "s_and_b32 s29, s29, 0xffff\n"
    "s_mov_b32 s30, 221184\n"
    "s_mov_b32 s31, 0x20000\n"
    "s_waitcnt vmcnt(0)\n"
    "s_barrier\n"
    "buffer_store_dwordx2 v[242:243], v19, s[16:19], s71 offen offset:0 sc1\n"
    "buffer_store_dwordx2 v[244:245], v20, s[16:19], s71 offen offset:512 sc1\n"
    "buffer_store_dwordx2 v[248:249], v21, s[16:19], 0 offen sc1\n"
    "s_branch kb_nb75_%=\n"
    "kb_la74_%=:\n"
    "s_waitcnt vmcnt(36)\n"
    "s_barrier\n"
    "kb_nb75_%=:\n"
    "s_add_u32 m0, s76, s41\n"
    "s_add_u32 s41, s41, 36864\n"
    "s_cmp_eq_u32 s41, 147456\n"
    "s_cselect_b32 s41, 0, s41\n"
    "buffer_load_dwordx4 v1, s[20:23], s40 offen offset:0 sc1 lds\n"
    "buffer_load_dwordx4 v1, s[20:23], s40 offen offset:1024 sc1 lds\n"
    "buffer_load_dwordx4 v1, s[20:23], s40 offen offset:2048 sc1 lds\n"
    "buffer_load_dwordx4 v1, s[20:23], s40 offen offset:3072 sc1 lds\n"
    "s_add_u32 m0, m0, 4096\n"
    "s_add_u32 s40, s40, 4096\n"
    "buffer_load_dwordx4 v1, s[20:23], s40 offen offset:0 sc1 lds\n"
    "buffer_load_dwordx4 v1, s[20:23], s40 offen offset:1024 sc1 lds\n"
    "buffer_load_dwordx4 v1, s[20:23], s40 offen offset:2048 sc1 lds\n"
    "buffer_load_dwordx4 v1, s[20:23], s40 offen offset:3072 sc1 lds\n"
    "s_add_u32 m0, m0, 4096\n"
    "s_add_u32 s40, s40, 4096\n"
    "buffer_load_dwordx4 v1, s[20:23], s40 offen offset:0 sc1 lds\n"
    "buffer_load_dwordx4 v1, s[20:23], s40 offen offset:1024 sc1 lds\n"
    "buffer_load_dwordx4 v1, s[20:23], s40 offen offset:2048 sc1 lds\n"
    "buffer_load_dwordx4 v1, s[20:23], s40 offen offset:3072 sc1 lds\n"
    "s_add_u32 m0, m0, 4096\n"
    "s_add_u32 s40, s40, 4096\n"
    "buffer_load_dwordx4 v1, s[20:23], s40 offen offset:0 sc1 lds\n"
    "buffer_load_dwordx4 v1, s[20:23], s40 offen offset:1024 sc1 lds\n"
    "buffer_load_dwordx4 v1, s[20:23], s40 offen offset:2048 sc1 lds\n"
    "buffer_load_dwordx4 v1, s[20:23], s40 offen offset:3072 sc1 lds\n"
    "s_add_u32 m0, m0, 4096\n"
    "s_add_u32 s40, s40, 4096\n"
    "buffer_load_dwordx4 v1, s[20:23], s40 offen offset:0 sc1 lds\n"
    "buffer_load_dwordx4 v1, s[20:23], s40 offen offset:1024 sc1 lds\n"
    "s_add_u32 s40, s40, 20480\n"
    "s_add_u32 s43, s43, 1\n"
    "s_cmp_eq_u32 s43, 3\n"
    "s_cbranch_scc1 kb_nb75_%=\n"
    "s_cmp_lt_u32 s43, 16\n"
    "s_cbranch_scc1 kb_la74_%=\n"
    "kb_lb76_%=:\n"
    "s_mul_i32 s47, s42, 36864\n"
    "s_add_u32 m0, s77, s41\n"
    "s_add_u32 s44, s47, s80\n"
    "s_waitcnt vmcnt(34)\n"
    "s_barrier\n"
    "buffer_load_dwordx4 v1, s[24:27], s44 offen offset:0 sc1 lds\n"
    "s_add_u32 m0, m0, 1024\n"
    "s_nop 0\n"
    "buffer_load_dwordx4 v1, s[24:27], s44 offen offset:1024 sc1 lds\n"
    "s_add_u32 m0, m0, 3072\n"
    "s_add_u32 s44, s47, s81\n"
    "buffer_load_dwordx4 v1, s[24:27], s44 offen offset:0 sc1 lds\n"
    "s_add_u32 m0, m0, 1024\n"
    "s_nop 0\n"
    "buffer_load_dwordx4 v1, s[24:27], s44 offen offset:1024 sc1 lds\n"
    "s_add_u32 m0, m0, 3072\n"
    "s_add_u32 s44, s47, s82\n"
    "buffer_load_dwordx4 v1, s[24:27], s44 offen offset:0 sc1 lds\n"
    "s_add_u32 m0, m0, 1024\n"
    "s_nop 0\n"
    "buffer_load_dwordx4 v1, s[24:27], s44 offen offset:1024 sc1 lds\n"
    "s_add_u32 m0, m0, 3072\n"
    "s_add_u32 s44, s47, 8192\n"
    "buffer_load_dwordx4 v1, s[24:27], s44 offen offset:0 sc1 lds\n"
    "s_add_u32 m0, m0, 2048\n"
    "s_add_u32 s44, s47, s83\n"
    "buffer_load_dwordx4 v1, s[24:27], s44 offen offset:0 sc1 lds\n"
    "s_add_u32 m0, m0, 1024\n"
    "s_nop 0\n"
    "buffer_load_dwordx4 v1, s[24:27], s44 offen offset:1024 sc1 lds\n"
    "s_add_u32 m0, s78, s41\n"
    "s_lshl_b32 s44, s42, 13\n"
    "buffer_load_dwordx4 v1, s[28:31], s44 offen offset:0 sc1 lds\n"
    "s_add_u32 s42, s42, 18\n"
    "buffer_load_dwordx4 v1, s[28:31], s44 offen offset:1024 sc1 lds\n"
    "s_add_u32 s41, s41, 36864\n"
    "s_cmp_eq_u32 s41, 147456\n"
    "s_cselect_b32 s41, 0, s41\n"
    "buffer_load_dwordx4 v1, s[28:31], s44 offen offset:2048 sc1 lds\n"
    "buffer_load_dwordx4 v1, s[28:31], s44 offen offset:3072 sc1 lds\n"
    "s_add_u32 m0, m0, 4096\n"
    "s_add_u32 s44, s44, 4096\n"
    "buffer_load_dwordx4 v1, s[28:31], s44 offen offset:0 sc1 lds\n"
    "buffer_load_dwordx4 v1, s[28:31], s44 offen offset:1024 sc1 lds\n"
    "buffer_load_dwordx4 v1, s[28:31], s44 offen offset:2048 sc1 lds\n"
    "buffer_load_dwordx4 v1, s[28:31], s44 offen offset:3072 sc1 lds\n"
    "s_mul_i32 s47, s42, 36864\n"
    "s_add_u32 m0, s77, s41\n"
    "s_add_u32 s44, s47, s80\n"
    "s_waitcnt vmcnt(34)\n"
    "s_barrier\n"
    "buffer_load_dwordx4 v1, s[24:27], s44 offen offset:0 sc1 lds\n"
    "s_add_u32 m0, m0, 1024\n"
    "s_nop 0\n"
    "buffer_load_dwordx4 v1, s[24:27], s44 offen offset:1024 sc1 lds\n"
    "s_add_u32 m0, m0, 3072\n"
    "s_add_u32 s44, s47, s81\n"
    "buffer_load_dwordx4 v1, s[24:27], s44 offen offset:0 sc1 lds\n"
    "s_add_u32 m0, m0, 1024\n"
    "s_nop 0\n"
    "buffer_load_dwordx4 v1, s[24:27], s44 offen offset:1024 sc1 lds\n"
    "s_add_u32 m0, m0, 3072\n"
    "s_add_u32 s44, s47, s82\n"
    "buffer_load_dwordx4 v1, s[24:27], s44 offen offset:0 sc1 lds\n"
    "s_add_u32 m0, m0, 1024\n"
    "s_nop 0\n"
    "buffer_load_dwordx4 v1, s[24:27], s44 offen offset:1024 sc1 lds\n"
    "s_add_u32 m0, m0, 3072\n"
    "s_add_u32 s44, s47, 8192\n"
    "buffer_load_dwordx4 v1, s[24:27], s44 offen offset:0 sc1 lds\n"
    "s_add_u32 m0, m0, 2048\n"
    "s_add_u32 s44, s47, s83\n"
    "buffer_load_dwordx4 v1, s[24:27], s44 offen offset:0 sc1 lds\n"
    "s_add_u32 m0, m0, 1024\n"
    "s_nop 0\n"
    "buffer_load_dwordx4 v1, s[24:27], s44 offen offset:1024 sc1 lds\n"
    "s_add_u32 m0, s78, s41\n"
    "s_lshl_b32 s44, s42, 13\n"
    "buffer_load_dwordx4 v1, s[28:31], s44 offen offset:0 sc1 lds\n"
    "s_sub_u32 s42, s42, 17\n"
    "buffer_load_dwordx4 v1, s[28:31], s44 offen offset:1024 sc1 lds\n"
    "s_add_u32 s41, s41, 36864\n"
    "s_cmp_eq_u32 s41, 147456\n"
    "s_cselect_b32 s41, 0, s41\n"
    "buffer_load_dwordx4 v1, s[28:31], s44 offen offset:2048 sc1 lds\n"
    "buffer_load_dwordx4 v1, s[28:31], s44 offen offset:3072 sc1 lds\n"
    "s_add_u32 m0, m0, 4096\n"
    "s_add_u32 s44, s44, 4096\n"
    "buffer_load_dwordx4 v1, s[28:31], s44 offen offset:0 sc1 lds\n"
    "buffer_load_dwordx4 v1, s[28:31], s44 offen offset:1024 sc1 lds\n"
    "buffer_load_dwordx4 v1, s[28:31], s44 offen offset:2048 sc1 lds\n"
    "buffer_load_dwordx4 v1, s[28:31], s44 offen offset:3072 sc1 lds\n"
    "s_cmp_lt_u32 s42, 7\n"
    "s_cbranch_scc1 kb_lb76_%=\n"
    "s_waitcnt vmcnt(34)\n"
    "s_barrier\n"
    "s_add_u32 m0, s78, 18432\n"
    "s_mov_b32 s44, 57344\n"
    "buffer_load_dwordx4 v1, s[28:31], s44 offen offset:0 sc1 lds\n"
    "buffer_load_dwordx4 v1, s[28:31], s44 offen offset:1024 sc1 lds\n"
    "buffer_load_dwordx4 v1, s[28:31], s44 offen offset:2048 sc1 lds\n"
    "buffer_load_dwordx4 v1, s[28:31], s44 offen offset:3072 sc1 lds\n"
    "s_add_u32 m0, s78, 22528\n"
    "s_mov_b32 s44, 61440\n"
    "buffer_load_dwordx4 v1, s[28:31], s44 offen offset:0 sc1 lds\n"
    "buffer_load_dwordx4 v1, s[28:31], s44 offen offset:1024 sc1 lds\n"
    "buffer_load_dwordx4 v1, s[28:31], s44 offen offset:2048 sc1 lds\n"
    "buffer_load_dwordx4 v1, s[28:31], s44 offen offset:3072 sc1 lds\n"
    "s_add_u32 m0, s78, 34816\n"
    "s_mov_b32 s44, 204800\n"
    "buffer_load_dwordx4 v1, s[28:31], s44 offen offset:0 sc1 lds\n"
    "buffer_load_dwordx4 v1, s[28:31], s44 offen offset:1024 sc1 lds\n"
    "buffer_load_dwordx4 v1, s[28:31], s44 offen offset:2048 sc1 lds\n"
    "buffer_load_dwordx4 v1, s[28:31], s44 offen offset:3072 sc1 lds\n"
    "s_add_u32 m0, s78, 38912\n"
    "s_mov_b32 s44, 208896\n"
    "buffer_load_dwordx4 v1, s[28:31], s44 offen offset:0 sc1 lds\n"
    "buffer_load_dwordx4 v1, s[28:31], s44 offen offset:1024 sc1 lds\n"
    "buffer_load_dwordx4 v1, s[28:31], s44 offen offset:2048 sc1 lds\n"
    "buffer_load_dwordx4 v1, s[28:31], s44 offen offset:3072 sc1 lds\n"
    "s_waitcnt vmcnt(33)\n"
    "s_barrier\n"
    "s_add_u32 m0, s78, 55296\n"
    "s_mov_b32 s44, 65536\n"
    "buffer_load_dwordx4 v1, s[28:31], s44 offen offset:0 sc1 lds\n"
    "buffer_load_dwordx4 v1, s[28:31], s44 offen offset:1024 sc1 lds\n"
    "buffer_load_dwordx4 v1, s[28:31], s44 offen offset:2048 sc1 lds\n"
    "buffer_load_dwordx4 v1, s[28:31], s44 offen offset:3072 sc1 lds\n"
    "s_add_u32 m0, s78, 59392\n"
    "s_mov_b32 s44, 69632\n"
    "buffer_load_dwordx4 v1, s[28:31], s44 offen offset:0 sc1 lds\n"
    "buffer_load_dwordx4 v1, s[28:31], s44 offen offset:1024 sc1 lds\n"
    "buffer_load_dwordx4 v1, s[28:31], s44 offen offset:2048 sc1 lds\n"
    "buffer_load_dwordx4 v1, s[28:31], s44 offen offset:3072 sc1 lds\n"
    "s_add_u32 m0, s78, 71680\n"
    "s_mov_b32 s44, 212992\n"
    "buffer_load_dwordx4 v1, s[28:31], s44 offen offset:0 sc1 lds\n"
    "buffer_load_dwordx4 v1, s[28:31], s44 offen offset:1024 sc1 lds\n"
    "buffer_load_dwordx4 v1, s[28:31], s44 offen offset:2048 sc1 lds\n"
    "buffer_load_dwordx4 v1, s[28:31], s44 offen offset:3072 sc1 lds\n"
    "s_add_u32 m0, s78, 75776\n"
    "s_mov_b32 s44, 217088\n"
    "buffer_load_dwordx4 v1, s[28:31], s44 offen offset:0 sc1 lds\n"
    "buffer_load_dwordx4 v1, s[28:31], s44 offen offset:1024 sc1 lds\n"
    "buffer_load_dwordx4 v1, s[28:31], s44 offen offset:2048 sc1 lds\n"
    "buffer_load_dwordx4 v1, s[28:31], s44 offen offset:3072 sc1 lds\n"
    "s_waitcnt vmcnt(16)\n"
    "s_barrier\n"
    "s_add_u32 m0, s77, 110592\n"
    "s_add_u32 s44, s81, 258048\n"
    "buffer_load_dwordx4 v1, s[24:27], s44 offen sc1 lds\n"
    "s_add_u32 m0, s77, 112640\n"
    "s_add_u32 s44, s81, 921600\n"
    "buffer_load_dwordx4 v1, s[24:27], s44 offen sc1 lds\n"
    "s_add_u32 m0, s77, 114688\n"
    "s_add_u32 s44, s81, 294912\n"
    "buffer_load_dwordx4 v1, s[24:27], s44 offen sc1 lds\n"
    "s_add_u32 m0, s77, 116736\n"
    "s_add_u32 s44, s81, 958464\n"
    "buffer_load_dwordx4 v1, s[24:27], s44 offen sc1 lds\n"
    "s_add_u32 m0, s77, 118784\n"
    "s_add_u32 s44, s81, 259072\n"
    "buffer_load_dwordx4 v1, s[24:27], s44 offen sc1 lds\n"
    "s_add_u32 m0, s77, 120832\n"
    "s_add_u32 s44, s81, 922624\n"
    "buffer_load_dwordx4 v1, s[24:27], s44 offen sc1 lds\n"
    "s_add_u32 m0, s77, 122880\n"
    "s_add_u32 s44, s81, 295936\n"
    "buffer_load_dwordx4 v1, s[24:27], s44 offen sc1 lds\n"
    "s_add_u32 m0, s77, 124928\n"
    "s_add_u32 s44, s81, 959488\n"
    "buffer_load_dwordx4 v1, s[24:27], s44 offen sc1 lds\n"
    "s_add_u32 m0, s77, 126976\n"
    "s_add_u32 s44, s82, 258048\n"
    "buffer_load_dwordx4 v1, s[24:27], s44 offen sc1 lds\n"
    "s_add_u32 m0, s77, 129024\n"
    "s_add_u32 s44, s82, 921600\n"
    "buffer_load_dwordx4 v1, s[24:27], s44 offen sc1 lds\n"
    "s_add_u32 m0, s77, 131072\n"
    "s_add_u32 s44, s82, 294912\n"
    "buffer_load_dwordx4 v1, s[24:27], s44 offen sc1 lds\n"
    "s_add_u32 m0, s77, 133120\n"
    "s_add_u32 s44, s82, 958464\n"
    "buffer_load_dwordx4 v1, s[24:27], s44 offen sc1 lds\n"
    "s_add_u32 m0, s77, 135168\n"
    "s_add_u32 s44, s82, 259072\n"
    "buffer_load_dwordx4 v1, s[24:27], s44 offen sc1 lds\n"
    "s_add_u32 m0, s77, 137216\n"
    "s_add_u32 s44, s82, 922624\n"
    "buffer_load_dwordx4 v1, s[24:27], s44 offen sc1 lds\n"
    "s_add_u32 m0, s77, 139264\n"
    "s_add_u32 s44, s82, 295936\n"
    "buffer_load_dwordx4 v1, s[24:27], s44 offen sc1 lds\n"
    "s_add_u32 m0, s77, 141312\n"
    "s_add_u32 s44, s82, 959488\n"
    "buffer_load_dwordx4 v1, s[24:27], s44 offen sc1 lds\n"
    "s_waitcnt vmcnt(16)\n"
    "s_barrier\n"
    "s_add_u32 m0, s77, 0\n"
    "s_mov_b32 s44, 266240\n"
    "buffer_load_dwordx4 v1, s[24:27], s44 offen sc1 lds\n"
    "s_add_u32 m0, s77, 2048\n"
    "s_mov_b32 s44, 929792\n"
    "buffer_load_dwordx4 v1, s[24:27], s44 offen sc1 lds\n"
    "s_add_u32 m0, s77, 4096\n"
    "s_mov_b32 s44, 303104\n"
    "buffer_load_dwordx4 v1, s[24:27], s44 offen sc1 lds\n"
    "s_add_u32 m0, s77, 6144\n"
    "s_mov_b32 s44, 966656\n"
    "buffer_load_dwordx4 v1, s[24:27], s44 offen sc1 lds\n"
    "s_add_u32 m0, s77, 8192\n"
    "s_add_u32 s44, s83, 258048\n"
    "buffer_load_dwordx4 v1, s[24:27], s44 offen sc1 lds\n"
    "s_add_u32 m0, s77, 10240\n"
    "s_add_u32 s44, s83, 921600\n"
    "buffer_load_dwordx4 v1, s[24:27], s44 offen sc1 lds\n"
    "s_add_u32 m0, s77, 12288\n"
    "s_add_u32 s44, s83, 294912\n"
    "buffer_load_dwordx4 v1, s[24:27], s44 offen sc1 lds\n"
    "s_add_u32 m0, s77, 14336\n"
    "s_add_u32 s44, s83, 958464\n"
    "buffer_load_dwordx4 v1, s[24:27], s44 offen sc1 lds\n"
    "s_add_u32 m0, s77, 16384\n"
    "s_add_u32 s44, s83, 259072\n"
    "buffer_load_dwordx4 v1, s[24:27], s44 offen sc1 lds\n"
    "s_add_u32 m0, s77, 18432\n"
    "s_add_u32 s44, s83, 922624\n"
    "buffer_load_dwordx4 v1, s[24:27], s44 offen sc1 lds\n"
    "s_add_u32 m0, s77, 20480\n"
    "s_add_u32 s44, s83, 295936\n"
    "buffer_load_dwordx4 v1, s[24:27], s44 offen sc1 lds\n"
    "s_add_u32 m0, s77, 22528\n"
    "s_add_u32 s44, s83, 959488\n"
    "buffer_load_dwordx4 v1, s[24:27], s44 offen sc1 lds\n"
    "s_lshl2_add_u32 m0, s77, 24576\n"
    "s_add_u32 s44, s84, 0\n"
    "buffer_load_dwordx4 v1, s[16:19], s44 offen sc1 lds\n"
    "s_lshl2_add_u32 m0, s77, 25600\n"
    "s_add_u32 s44, s84, 18432\n"
    "buffer_load_dwordx4 v1, s[16:19], s44 offen sc1 lds\n"
    "s_lshl2_add_u32 m0, s77, 26624\n"
    "s_add_u32 s44, s84, 36864\n"
    "buffer_load_dwordx4 v1, s[16:19], s44 offen sc1 lds\n"
    "s_lshl2_add_u32 m0, s77, 27648\n"
    "s_add_u32 s44, s84, 55296\n"
    "buffer_load_dwordx4 v1, s[16:19], s44 offen sc1 lds\n"
    "s_mov_b32 m0, 32768\n"
    "s_nop 0\n"
    "buffer_load_dwordx4 v9, s[16:19], 0 offen sc1 lds\n"
    "s_waitcnt vmcnt(17)\n"
    "s_barrier\n"
    "s_waitcnt vmcnt(0)\n"
    "s_barrier\n"
    "s_branch kb_fin72_%=\n"
    "kb_aux73_%=:\n"
    "s_lshl_b32 s39, s94, 2\n"
    "s_lshl4_add_u32 s39, s95, s39\n"
    "s_lshl_b32 s39, s39, 7\n"
    "v_and_b32 v2, 3, v0\n"
    "v_lshl_add_u32 v2, v2, 7, s39\n"
    "s_lshl4_add_u32 s39, s95, s96\n"
    "s_lshl_b32 s39, s39, 7\n"
    "v_mov_b32 v4, s39\n"
    "v_mov_b32 v12, 0x80000000\n"
    "s_and_b32 s39, s3, 1\n"
    "s_lshl_b32 s39, s39, 6\n"
    "v_add_u32 v11, s39, v0\n"
    "v_cmp_eq_u32 vcc, 0, v11\n"
    "s_nop 1\n"
    "v_cndmask_b32 v4, v12, v4, vcc\n"
    "s_and_b32 s39, s3, 1\n"
    "s_lshl_b32 s39, s39, 1\n"
    "s_lshl2_add_u32 s39, s95, s39\n"
    "s_lshl_b32 s39, s39, 9\n"
    "s_add_u32 s39, s39, 0x10000\n"
    "v_and_b32 v6, 7, v0\n"
    "v_lshl_add_u32 v6, v6, 7, s39\n"
    "s_and_b32 s39, s3, 1\n"
    "s_lshl_b32 s39, s39, 1\n"
    "s_lshl2_add_u32 s39, s95, s39\n"
    "s_lshl_b32 s39, s39, 12\n"
    "s_add_u32 s39, s39, 0x1000000\n"
    "v_lshrrev_b32 v7, 4, v0\n"
    "v_lshl_add_u32 v7, v7, 11, s39\n"
    "v_and_b32 v11, 15, v0\n"
    "v_lshl_add_u32 v7, v11, 3, v7\n"
    "s_and_b32 s39, s3, 1\n"
    "s_lshl_b32 s39, s39, 8\n"
    "s_add_u32 s39, s39, 0x8800\n"
    "v_lshl_add_u32 v8, v0, 2, s39\n"
    "s_lshl2_add_u32 s85, s95, s88\n"
    "s_and_b32 s39, s3, 1\n"
    "s_lshl_b32 s39, s39, 1\n"
    "s_lshl2_add_u32 s85, s85, s39\n"
    "s_mul_i32 s79, s85, 0x6000\n"
    "s_mul_i32 s39, s94, 0x1800\n"
    "s_add_u32 s79, s79, s39\n"
    "s_add_u32 s79, s79, 0x40000\n"
    "s_mul_i32 s39, s85, 0xc00\n"
    "s_and_b32 s51, s3, 1\n"
    "s_lshl_b32 s51, s51, 1\n"
    "s_sub_u32 s51, s51, s94\n"
    "s_and_b32 s51, s51, 3\n"
    "s_lshl_b32 s51, s51, 10\n"
    "s_add_u32 s39, s39, s51\n"
    "s_add_u32 s39, s39, 0x63fc00\n"
    "s_and_b32 s51, s3, 1\n"
    "s_lshl_b32 s51, s51, 1\n"
    "s_cmp_eq_u32 s51, s94\n"
    "s_cselect_b32 s39, 0x80000000, s39\n"
    "v_add_u32 v56, s39, v1\n"
    "s_add_u32 s39, s85, 1\n"
    "s_mul_i32 s39, s39, 0xc00\n"
    "s_and_b32 s51, s3, 1\n"
    "s_lshl1_add_u32 s51, s51, 1\n"
    "s_sub_u32 s51, s51, s94\n"
    "s_and_b32 s51, s51, 3\n"
    "s_lshl_b32 s51, s51, 10\n"
    "s_add_u32 s39, s39, s51\n"
    "s_add_u32 s39, s39, 0x63fc00\n"
    "s_and_b32 s51, s3, 1\n"
    "s_lshl1_add_u32 s51, s51, 1\n"
    "s_cmp_eq_u32 s51, s94\n"
    "s_cselect_b32 s39, 0x80000000, s39\n"
    "v_add_u32 v57, s39, v1\n"
    "s_lshl2_add_u32 s39, s95, s88\n"
    "s_lshl_b32 s39, s39, 8\n"
    "s_add_u32 s39, s39, 0x8000\n"
    "v_and_b32 v3, 3, v0\n"
    "v_lshl_add_u32 v3, v3, 6, s39\n"
    "v_add_u32 v11, 64, v2\n"
    "v_cmp_gt_u32 vcc, 4, v0\n"
    "s_nop 1\n"
    "v_cndmask_b32 v3, v11, v3, vcc\n"
    "s_lshl2_add_u32 s39, s95, s88\n"
    "s_lshl2_add_u32 s39, s39, s94\n"
    "s_lshl_b32 s39, s39, 6\n"
    "s_add_u32 s39, s39, 0x8000\n"
    "v_mov_b32 v5, s39\n"
    "v_mov_b32 v11, 0x80000000\n"
    "s_and_b32 s39, s3, 1\n"
    "s_lshl_b32 s39, s39, 6\n"
    "v_add_u32 v12, s39, v0\n"
    "v_cmp_eq_u32 vcc, 0, v12\n"
    "s_nop 1\n"
    "v_cndmask_b32 v5, v11, v5, vcc\n"
    "v_mov_b32 v60, -1\n"
    "v_mov_b32 v61, -1\n"
    "v_mov_b32 v62, -1\n"
    "v_mov_b32 v63, -1\n"
    "v_mov_b32 v16, s60\n"
    "v_mov_b32 v58, 0x38000\n"
    "v_mov_b32 v12, 0x80000000\n"
    "s_and_b32 s39, s3, 1\n"
    "s_add_u32 s39, s39, s2\n"
    "v_add_u32 v11, s39, v0\n"
    "v_cmp_eq_u32 vcc, 0, v11\n"
    "s_nop 1\n"
    "v_cndmask_b32 v58, v12, v58, vcc\n"
    "buffer_store_dword v16, v58, s[16:19], 0 offen sc1\n"
    "s_waitcnt vmcnt(0)\n"
    "s_setprio 0\n"
    "s_barrier\n"
    "buffer_store_dwordx2 v[242:243], v19, s[16:19], s71 offen offset:0 sc1\n"
    "buffer_store_dwordx2 v[244:245], v20, s[16:19], s71 offen offset:512 sc1\n"
    "buffer_store_dwordx2 v[248:249], v21, s[16:19], 0 offen sc1\n"
    "s_barrier\n"
    "s_waitcnt vmcnt(0)\n"
    "s_barrier\n"
    "buffer_store_dword v16, v13, s[16:19], 0 offen sc1\n"
    "s_barrier\n"
    "s_add_u32 s45, s79, 0\n"
    "buffer_store_dwordx4 v[60:63], v1, s[16:19], s45 offen sc1\n"
    "s_add_u32 s45, s79, 24576\n"
    "buffer_store_dwordx4 v[60:63], v1, s[16:19], s45 offen sc1\n"
    "s_barrier\n"
    "s_add_u32 s45, s79, 1024\n"
    "buffer_store_dwordx4 v[60:63], v1, s[16:19], s45 offen sc1\n"
    "s_add_u32 s45, s79, 25600\n"
    "buffer_store_dwordx4 v[60:63], v1, s[16:19], s45 offen sc1\n"
    "s_barrier\n"
    "buffer_load_dword v18, v6, s[16:19], 0 offen sc1\n"
    "s_add_u32 s45, s79, 2048\n"
    "buffer_store_dwordx4 v[60:63], v1, s[16:19], s45 offen sc1\n"
    "s_add_u32 s45, s79, 26624\n"
    "buffer_store_dwordx4 v[60:63], v1, s[16:19], s45 offen sc1\n"
    "s_barrier\n"
    "s_add_u32 s45, s79, 3072\n"
    "buffer_store_dwordx4 v[60:63], v1, s[16:19], s45 offen sc1\n"
    "s_add_u32 s45, s79, 27648\n"
    "buffer_store_dwordx4 v[60:63], v1, s[16:19], s45 offen sc1\n"
    "s_barrier\n"
    "s_waitcnt vmcnt(4)\n"
    "v_cmp_ne_u32 vcc, s60, v18\n"
    "s_cbranch_vccnz kb_sp77_%=\n"
    "kb_ok78_%=:\n"
    "buffer_load_dwordx2 v[24:25], v7, s[16:19], 0 offen offset:0 sc1\n"
    "buffer_load_dwordx2 v[26:27], v7, s[16:19], 0 offen offset:128 sc1\n"
    "buffer_load_dwordx2 v[28:29], v7, s[16:19], 0 offen offset:256 sc1\n"
    "buffer_load_dwordx2 v[30:31], v7, s[16:19], 0 offen offset:384 sc1\n"
    "buffer_load_dwordx2 v[32:33], v7, s[16:19], 0 offen offset:512 sc1\n"
    "buffer_load_dwordx2 v[34:35], v7, s[16:19], 0 offen offset:640 sc1\n"
    "buffer_load_dwordx2 v[36:37], v7, s[16:19], 0 offen offset:768 sc1\n"
    "buffer_load_dwordx2 v[38:39], v7, s[16:19], 0 offen offset:896 sc1\n"
    "s_add_u32 s45, s79, 4096\n"
    "buffer_store_dwordx4 v[60:63], v1, s[16:19], s45 offen sc1\n"
    "s_add_u32 s45, s79, 28672\n"
    "buffer_store_dwordx4 v[60:63], v1, s[16:19], s45 offen sc1\n"
    "s_barrier\n"
    "buffer_load_dwordx2 v[40:41], v7, s[16:19], 0 offen offset:1024 sc1\n"
    "buffer_load_dwordx2 v[42:43], v7, s[16:19], 0 offen offset:1152 sc1\n"
    "buffer_load_dwordx2 v[44:45], v7, s[16:19], 0 offen offset:1280 sc1\n"
    "buffer_load_dwordx2 v[46:47], v7, s[16:19], 0 offen offset:1408 sc1\n"
    "buffer_load_dwordx2 v[48:49], v7, s[16:19], 0 offen offset:1536 sc1\n"
    "buffer_load_dwordx2 v[50:51], v7, s[16:19], 0 offen offset:1664 sc1\n"
    "buffer_load_dwordx2 v[52:53], v7, s[16:19], 0 offen offset:1792 sc1\n"
    "buffer_load_dwordx2 v[54:55], v7, s[16:19], 0 offen offset:1920 sc1\n"
    "s_add_u32 s45, s79, 5120\n"
    "buffer_store_dwordx4 v[60:63], v1, s[16:19], s45 offen sc1\n"
    "s_add_u32 s45, s79, 29696\n"
    "buffer_store_dwordx4 v[60:63], v1, s[16:19], s45 offen sc1\n"
    "s_barrier\n"
    "buffer_store_dwordx4 v[60:63], v56, s[16:19], 0 offen sc1\n"
    "buffer_store_dwordx4 v[60:63], v57, s[16:19], 0 offen sc1\n"
    "s_barrier\n"
    "buffer_store_dword v16, v4, s[16:19], 0 offen offset:0 sc1\n"
    "s_lshr_b32 s39, s2, 3\n"
    "s_and_b32 s51, s3, 1\n"
    "s_lshl_b32 s51, s51, 2\n"
    "s_lshl3_add_u32 s39, s39, s51\n"
    "v_and_b32 v19, 3, v0\n"
    "v_add_u32 v19, s39, v19\n"
    "v_lshlrev_b32 v19, 7, v19\n"
    "v_min_u32 v19, kb_code_end-kb_epi17_%=, v19\n"
    "s_getpc_b64 s[36:37]\n"
    "kb_pc79_%=:\n"
    "s_add_u32 s36, s36, kb_epi17_%=-kb_pc79_%=\n"
    "s_addc_u32 s37, s37, -1\n"
    "global_load_dword v19, v19, s[36:37]\n"
    "s_waitcnt vmcnt(6)\n"
    "v_add_f32 v24, v24, v40\n"
    "v_add_f32 v25, v25, v41\n"
    "v_add_f32 v26, v26, v42\n"
    "v_add_f32 v27, v27, v43\n"
    "v_add_f32 v28, v28, v44\n"
    "v_add_f32 v29, v29, v45\n"
    "v_add_f32 v30, v30, v46\n"
    "v_add_f32 v31, v31, v47\n"
    "v_add_f32 v32, v32, v48\n"
    "s_barrier\n"
    "s_add_u32 m0, s77, 147456\n"
    "s_add_u32 s44, s80, 258048\n"
    "buffer_load_dwordx4 v1, s[24:27], s44 offen sc1 lds\n"
    "s_add_u32 m0, s77, 149504\n"
    "s_add_u32 s44, s80, 921600\n"
    "buffer_load_dwordx4 v1, s[24:27], s44 offen sc1 lds\n"
    "s_add_u32 m0, s77, 151552\n"
    "s_add_u32 s44, s80, 294912\n"
    "buffer_load_dwordx4 v1, s[24:27], s44 offen sc1 lds\n"
    "s_add_u32 m0, s77, 153600\n"
    "s_add_u32 s44, s80, 958464\n"
    "buffer_load_dwordx4 v1, s[24:27], s44 offen sc1 lds\n"
    "v_add_f32 v33, v33, v49\n"
    "v_add_f32 v34, v34, v50\n"
    "v_add_f32 v35, v35, v51\n"
    "v_add_f32 v36, v36, v52\n"
    "v_add_f32 v37, v37, v53\n"
    "v_add_f32 v38, v38, v54\n"
    "v_add_f32 v39, v39, v55\n"
    "v_add_f32 v24, v24, v32\n"
    "v_add_f32 v25, v25, v33\n"
    "s_barrier\n"
    "buffer_load_dword v17, v2, s[16:19], 0 offen sc1\n"
    "s_add_u32 m0, s77, 155648\n"
    "s_add_u32 s44, s80, 259072\n"
    "buffer_load_dwordx4 v1, s[24:27], s44 offen sc1 lds\n"
    "s_add_u32 m0, s77, 157696\n"
    "s_add_u32 s44, s80, 922624\n"
    "buffer_load_dwordx4 v1, s[24:27], s44 offen sc1 lds\n"
    "s_add_u32 m0, s77, 159744\n"
    "s_add_u32 s44, s80, 295936\n"
    "buffer_load_dwordx4 v1, s[24:27], s44 offen sc1 lds\n"
    "s_add_u32 m0, s77, 161792\n"
    "s_add_u32 s44, s80, 959488\n"
    "buffer_load_dwordx4 v1, s[24:27], s44 offen sc1 lds\n"
    "v_add_f32 v26, v26, v34\n"
    "v_add_f32 v27, v27, v35\n"
    "v_add_f32 v28, v28, v36\n"
    "v_add_f32 v29, v29, v37\n"
    "v_add_f32 v30, v30, v38\n"
    "v_add_f32 v31, v31, v39\n"
    "v_add_f32 v24, v24, v28\n"
    "v_add_f32 v25, v25, v29\n"
    "v_add_f32 v26, v26, v30\n"
    "s_barrier\n"
    "v_add_f32 v27, v27, v31\n"
    "v_add_f32 v24, v24, v26\n"
    "v_add_f32 v25, v25, v27\n"
    "v_mul_f32 v24, 0x3ae38e39, v24\n"
    "v_mul_f32 v25, 0x3ae38e39, v25\n"
    "v_fma_f32 v24, -v24, v24, v25\n"
    "v_max_f32 v24, 0, v24\n"
    "v_cvt_pk_bf16_f32 v24, 0, v24\n"
    "v_add_f32 v24, 0x3727c5ac, v24\n"
    "s_waitcnt vmcnt(4)\n"
    "v_cmp_ne_u32 vcc, s60, v17\n"
    "s_cbranch_vccnz kb_sp80_%=\n"
    "kb_ok81_%=:\n"
    "v_add_u32 v2, 32, v2\n"
    "s_barrier\n"
    "v_cvt_pk_bf16_f32 v24, 0, v24\n"
    "v_sqrt_f32 v24, v24\n"
    "s_nop 0\n"
    "v_cvt_pk_bf16_f32 v24, 0, v24\n"
    "v_rcp_f32 v24, v24\n"
    "s_nop 0\n"
    "v_mul_f32 v24, v24, v24\n"
    "s_waitcnt vmcnt(0)\n"
    "s_barrier\n"
    "buffer_store_dword v16, v5, s[16:19], 0 offen sc1\n"
    "ds_write_b32 v8, v24\n"
    "s_waitcnt lgkmcnt(0)\n"
    "s_barrier\n"
    "buffer_store_dword v16, v4, s[16:19], 0 offen offset:32 sc1\n"
    "s_barrier\n"
    "s_barrier\n"
    "s_barrier\n"
    "s_barrier\n"
    "buffer_load_dword v17, v2, s[16:19], 0 offen sc1\n"
    "s_barrier\n"
    "buffer_store_dword v16, v4, s[16:19], 0 offen offset:64 sc1\n"
    "s_waitcnt vmcnt(1)\n"
    "v_cmp_ne_u32 vcc, s60, v17\n"
    "s_cbranch_vccnz kb_sp82_%=\n"
    "kb_ok83_%=:\n"
    "v_add_u32 v2, 32, v2\n"
    "s_barrier\n"
    "s_barrier\n"
    "s_barrier\n"
    "s_barrier\n"
    "s_barrier\n"
    "buffer_load_dword v17, v3, s[16:19], 0 offen sc1\n"
    "s_barrier\n"
    "s_waitcnt vmcnt(0)\n"
    "v_cmp_ne_u32 vcc, s60, v17\n"
    "s_cbranch_vccnz kb_sp84_%=\n"
    "kb_ok85_%=:\n"
    "v_add_u32 v2, 32, v2\n"
    "s_barrier\n"
    "s_branch kb_fin72_%=\n"
    "kb_fb22_%=:\n"
    "s_add_u32 s48, s48, 1\n"
    "s_cmp_lt_u32 s48, 131072\n"
    "s_cbranch_scc0 kb_fa21_%=x\n"
    "s_sleep 8\n"
    "s_mov_b32 s45, 212992\n"
    "buffer_load_dwordx4 v[140:143], v1, s[16:19], s45 offen sc1\n"
    "s_waitcnt vmcnt(0)\n"
    "s_branch kb_fa21_%=\n"
    "kb_fb25_%=:\n"
    "s_add_u32 s48, s48, 1\n"
    "s_cmp_lt_u32 s48, 131072\n"
    "s_cbranch_scc0 kb_fa24_%=x\n"
    "s_sleep 8\n"
    "buffer_load_dwordx2 v[4:5], v1, s[16:19], s45 offen sc1\n"
    "buffer_load_dwordx2 v[8:9], v1, s[16:19], s45 offen offset:8 sc1\n"
    "s_waitcnt vmcnt(0)\n"
    "s_branch kb_fa24_%=\n"
    "kb_rt31_%=:\n"
    "s_add_u32 s48, s48, 1\n"
    "s_cmp_lt_u32 s48, 131072\n"
    "s_cbranch_scc0 kb_bk30_%=x\n"
    "buffer_load_dwordx4 v[24:27], v1, s[16:19], s97 offen offset:0 sc1\n"
    "buffer_load_dwordx4 v[28:31], v1, s[16:19], s97 offen offset:1024 sc1\n"
    "buffer_load_dwordx4 v[32:35], v1, s[16:19], s97 offen offset:2048 sc1\n"
    "buffer_load_dwordx4 v[36:39], v1, s[16:19], s97 offen offset:3072 sc1\n"
    "buffer_load_dword v40, v4, s[16:19], 0 offen sc1\n"
    "buffer_load_dword v41, v4, s[16:19], 0 offen offset:64 sc1\n"
    "buffer_load_dwordx4 v[44:47], v1, s[16:19], s43 offen sc1\n"
    "s_waitcnt vmcnt(0)\n"
    "s_branch kb_bk30_%=\n"
    "kb_rt35_%=:\n"
    "s_add_u32 s48, s48, 1\n"
    "s_cmp_lt_u32 s48, 131072\n"
    "s_cbranch_scc0 kb_bk34_%=x\n"
    "s_add_u32 s45, s76, 0\n"
    "s_add_u32 s46, s76, 18432\n"
    "buffer_load_dwordx4 v[24:27], v1, s[16:19], s45 offen offset:0 sc1\n"
    "buffer_load_dwordx4 v[28:31], v1, s[16:19], s46 offen offset:0 sc1\n"
    "buffer_load_dwordx4 v[32:35], v1, s[16:19], s45 offen offset:1024 sc1\n"
    "buffer_load_dwordx4 v[36:39], v1, s[16:19], s46 offen offset:1024 sc1\n"
    "buffer_load_dwordx4 v[40:43], v1, s[16:19], s45 offen offset:2048 sc1\n"
    "buffer_load_dwordx4 v[44:47], v1, s[16:19], s46 offen offset:2048 sc1\n"
    "s_waitcnt vmcnt(0)\n"
    "s_branch kb_bk34_%=\n"
    "kb_rt37_%=:\n"
    "s_add_u32 s48, s48, 1\n"
    "s_cmp_lt_u32 s48, 131072\n"
    "s_cbranch_scc0 kb_bk36_%=x\n"
    "s_add_u32 s45, s76, 3072\n"
    "s_add_u32 s46, s76, 21504\n"
    "buffer_load_dwordx4 v[48:51], v1, s[16:19], s45 offen offset:0 sc1\n"
    "buffer_load_dwordx4 v[52:55], v1, s[16:19], s46 offen offset:0 sc1\n"
    "buffer_load_dwordx4 v[56:59], v1, s[16:19], s45 offen offset:1024 sc1\n"
    "buffer_load_dwordx4 v[60:63], v1, s[16:19], s46 offen offset:1024 sc1\n"
    "buffer_load_dwordx4 v[64:67], v1, s[16:19], s45 offen offset:2048 sc1\n"
    "buffer_load_dwordx4 v[68:71], v1, s[16:19], s46 offen offset:2048 sc1\n"
    "s_waitcnt vmcnt(0)\n"
    "s_branch kb_bk36_%=\n"
    "kb_rt39_%=:\n"
    "s_add_u32 s48, s48, 1\n"
    "s_cmp_lt_u32 s48, 131072\n"
    "s_cbranch_scc0 kb_bk38_%=x\n"
    "s_add_u32 s45, s76, 6144\n"
    "s_add_u32 s46, s76, 24576\n"
    "buffer_load_dwordx4 v[72:75], v1, s[16:19], s45 offen offset:0 sc1\n"
    "buffer_load_dwordx4 v[76:79], v1, s[16:19], s46 offen offset:0 sc1\n"
    "buffer_load_dwordx4 v[80:83], v1, s[16:19], s45 offen offset:1024 sc1\n"
    "buffer_load_dwordx4 v[84:87], v1, s[16:19], s46 offen offset:1024 sc1\n"
    "buffer_load_dwordx4 v[88:91], v1, s[16:19], s45 offen offset:2048 sc1\n"
    "buffer_load_dwordx4 v[92:95], v1, s[16:19], s46 offen offset:2048 sc1\n"
    "s_waitcnt vmcnt(0)\n"
    "s_branch kb_bk38_%=\n"
    "kb_rt41_%=:\n"
    "s_add_u32 s48, s48, 1\n"
    "s_cmp_lt_u32 s48, 131072\n"
    "s_cbranch_scc0 kb_bk40_%=x\n"
    "s_add_u32 s45, s76, 9216\n"
    "s_add_u32 s46, s76, 27648\n"
    "buffer_load_dwordx4 v[96:99], v1, s[16:19], s45 offen offset:0 sc1\n"
    "buffer_load_dwordx4 v[100:103], v1, s[16:19], s46 offen offset:0 sc1\n"
    "buffer_load_dwordx4 v[104:107], v1, s[16:19], s45 offen offset:1024 sc1\n"
    "buffer_load_dwordx4 v[108:111], v1, s[16:19], s46 offen offset:1024 sc1\n"
    "buffer_load_dwordx4 v[112:115], v1, s[16:19], s45 offen offset:2048 sc1\n"
    "buffer_load_dwordx4 v[116:119], v1, s[16:19], s46 offen offset:2048 sc1\n"
    "s_waitcnt vmcnt(0)\n"
    "s_branch kb_bk40_%=\n"
    "kb_rt43_%=:\n"
    "s_add_u32 s48, s48, 1\n"
    "s_cmp_lt_u32 s48, 131072\n"
    "s_cbranch_scc0 kb_bk42_%=x\n"
    "s_add_u32 s45, s76, 12288\n"
    "s_add_u32 s46, s76, 30720\n"
    "buffer_load_dwordx4 v[120:123], v1, s[16:19], s45 offen offset:0 sc1\n"
    "buffer_load_dwordx4 v[124:127], v1, s[16:19], s46 offen offset:0 sc1\n"
    "buffer_load_dwordx4 v[128:131], v1, s[16:19], s45 offen offset:1024 sc1\n"
    "buffer_load_dwordx4 v[132:135], v1, s[16:19], s46 offen offset:1024 sc1\n"
    "buffer_load_dwordx4 v[136:139], v1, s[16:19], s45 offen offset:2048 sc1\n"
    "buffer_load_dwordx4 v[140:143], v1, s[16:19], s46 offen offset:2048 sc1\n"
    "s_waitcnt vmcnt(0)\n"
    "s_branch kb_bk42_%=\n"
    "kb_rt45_%=:\n"
    "s_add_u32 s48, s48, 1\n"
    "s_cmp_lt_u32 s48, 131072\n"
    "s_cbranch_scc0 kb_bk44_%=x\n"
    "s_add_u32 s45, s76, 15360\n"
    "s_add_u32 s46, s76, 33792\n"
    "buffer_load_dwordx4 v[144:147], v1, s[16:19], s45 offen offset:0 sc1\n"
    "buffer_load_dwordx4 v[148:151], v1, s[16:19], s46 offen offset:0 sc1\n"
    "buffer_load_dwordx4 v[152:155], v1, s[16:19], s45 offen offset:1024 sc1\n"
    "buffer_load_dwordx4 v[156:159], v1, s[16:19], s46 offen offset:1024 sc1\n"
    "buffer_load_dwordx4 v[160:163], v1, s[16:19], s45 offen offset:2048 sc1\n"
    "buffer_load_dwordx4 v[164:167], v1, s[16:19], s46 offen offset:2048 sc1\n"
    "s_waitcnt vmcnt(0)\n"
    "s_branch kb_bk44_%=\n"
    "kb_rt55_%=:\n"
    "s_add_u32 s48, s48, 1\n"
    "s_cmp_lt_u32 s48, 131072\n"
    "s_cbranch_scc0 kb_bk54_%=x\n"
    "s_waitcnt vmcnt(0)\n"
    "buffer_load_dwordx4 v[24:27], v1, s[16:19], s82 offen offset:0 sc1\n"
    "buffer_load_dwordx4 v[28:31], v1, s[16:19], s82 offen offset:1024 sc1\n"
    "v_max3_u32 v236, v32, v33, v34\n"
    "v_max3_u32 v236, v35, v36, v236\n"
    "v_max3_u32 v236, v37, v38, v236\n"
    "v_max_u32 v236, v39, v236\n"
    "v_cmp_eq_u32 vcc, -1, v236\n"
    "s_cbranch_vccz kb_sk49_%=\n"
    "buffer_load_dwordx4 v[32:35], v1, s[16:19], s83 offen offset:0 sc1\n"
    "buffer_load_dwordx4 v[36:39], v1, s[16:19], s83 offen offset:1024 sc1\n"
    "kb_sk49_%=:\n"
    "v_max3_u32 v236, v40, v41, v42\n"
    "v_max3_u32 v236, v43, v44, v236\n"
    "v_max3_u32 v236, v45, v46, v236\n"
    "v_max_u32 v236, v47, v236\n"
    "v_cmp_eq_u32 vcc, -1, v236\n"
    "s_cbranch_vccz kb_sk50_%=\n"
    "buffer_load_dwordx4 v[40:43], v1, s[16:19], s84 offen offset:0 sc1\n"
    "buffer_load_dwordx4 v[44:47], v1, s[16:19], s84 offen offset:1024 sc1\n"
    "kb_sk50_%=:\n"
    "v_max3_u32 v236, v48, v49, v50\n"
    "v_max_u32 v236, v51, v236\n"
    "v_cmp_eq_u32 vcc, -1, v236\n"
    "s_cbranch_vccz kb_sk51_%=\n"
    "buffer_load_dwordx4 v[48:51], v255, s[16:19], 0 offen offset:0 sc1\n"
    "kb_sk51_%=:\n"
    "v_max3_u32 v236, v52, v53, v54\n"
    "v_max_u32 v236, v55, v236\n"
    "v_cmp_eq_u32 vcc, -1, v236\n"
    "s_cbranch_vccz kb_sk52_%=\n"
    "buffer_load_dwordx4 v[52:55], v255, s[16:19], 0 offen offset:1024 sc1\n"
    "kb_sk52_%=:\n"
    "v_max3_u32 v236, v56, v57, v58\n"
    "v_max_u32 v236, v59, v236\n"
    "v_cmp_eq_u32 vcc, -1, v236\n"
    "s_cbranch_vccz kb_sk53_%=\n"
    "buffer_load_dwordx4 v[56:59], v255, s[16:19], 0 offen offset:2048 sc1\n"
    "kb_sk53_%=:\n"
    "s_waitcnt vmcnt(0)\n"
    "s_branch kb_bk54_%=\n"
    "kb_rt61_%=:\n"
    "s_add_u32 s48, s48, 1\n"
    "s_cmp_lt_u32 s48, 131072\n"
    "s_cbranch_scc0 kb_bk60_%=x\n"
    "s_waitcnt vmcnt(0)\n"
    "buffer_load_dwordx4 v[32:35], v1, s[16:19], s83 offen offset:0 sc1\n"
    "buffer_load_dwordx4 v[36:39], v1, s[16:19], s83 offen offset:1024 sc1\n"
    "v_max3_u32 v236, v40, v41, v42\n"
    "v_max3_u32 v236, v43, v44, v236\n"
    "v_max3_u32 v236, v45, v46, v236\n"
    "v_max_u32 v236, v47, v236\n"
    "v_cmp_eq_u32 vcc, -1, v236\n"
    "s_cbranch_vccz kb_sk56_%=\n"
    "buffer_load_dwordx4 v[40:43], v1, s[16:19], s84 offen offset:0 sc1\n"
    "buffer_load_dwordx4 v[44:47], v1, s[16:19], s84 offen offset:1024 sc1\n"
    "kb_sk56_%=:\n"
    "v_max3_u32 v236, v48, v49, v50\n"
    "v_max_u32 v236, v51, v236\n"
    "v_cmp_eq_u32 vcc, -1, v236\n"
    "s_cbranch_vccz kb_sk57_%=\n"
    "buffer_load_dwordx4 v[48:51], v255, s[16:19], 0 offen offset:0 sc1\n"
    "kb_sk57_%=:\n"
    "v_max3_u32 v236, v52, v53, v54\n"
    "v_max_u32 v236, v55, v236\n"
    "v_cmp_eq_u32 vcc, -1, v236\n"
    "s_cbranch_vccz kb_sk58_%=\n"
    "buffer_load_dwordx4 v[52:55], v255, s[16:19], 0 offen offset:1024 sc1\n"
    "kb_sk58_%=:\n"
    "v_max3_u32 v236, v56, v57, v58\n"
    "v_max_u32 v236, v59, v236\n"
    "v_cmp_eq_u32 vcc, -1, v236\n"
    "s_cbranch_vccz kb_sk59_%=\n"
    "buffer_load_dwordx4 v[56:59], v255, s[16:19], 0 offen offset:2048 sc1\n"
    "kb_sk59_%=:\n"
    "s_waitcnt vmcnt(0)\n"
    "s_branch kb_bk60_%=\n"
    "kb_rt66_%=:\n"
    "s_add_u32 s48, s48, 1\n"
    "s_cmp_lt_u32 s48, 131072\n"
    "s_cbranch_scc0 kb_bk65_%=x\n"
    "s_waitcnt vmcnt(0)\n"
    "buffer_load_dwordx4 v[40:43], v1, s[16:19], s84 offen offset:0 sc1\n"
    "buffer_load_dwordx4 v[44:47], v1, s[16:19], s84 offen offset:1024 sc1\n"
    "v_max3_u32 v236, v48, v49, v50\n"
    "v_max_u32 v236, v51, v236\n"
    "v_cmp_eq_u32 vcc, -1, v236\n"
    "s_cbranch_vccz kb_sk62_%=\n"
    "buffer_load_dwordx4 v[48:51], v255, s[16:19], 0 offen offset:0 sc1\n"
    "kb_sk62_%=:\n"
    "v_max3_u32 v236, v52, v53, v54\n"
    "v_max_u32 v236, v55, v236\n"
    "v_cmp_eq_u32 vcc, -1, v236\n"
    "s_cbranch_vccz kb_sk63_%=\n"
    "buffer_load_dwordx4 v[52:55], v255, s[16:19], 0 offen offset:1024 sc1\n"
    "kb_sk63_%=:\n"
    "v_max3_u32 v236, v56, v57, v58\n"
    "v_max_u32 v236, v59, v236\n"
    "v_cmp_eq_u32 vcc, -1, v236\n"
    "s_cbranch_vccz kb_sk64_%=\n"
    "buffer_load_dwordx4 v[56:59], v255, s[16:19], 0 offen offset:2048 sc1\n"
    "kb_sk64_%=:\n"
    "s_waitcnt vmcnt(0)\n"
    "s_branch kb_bk65_%=\n"
    "kb_rt71_%=:\n"
    "s_add_u32 s48, s48, 1\n"
    "s_cmp_lt_u32 s48, 131072\n"
    "s_cbranch_scc0 kb_bk70_%=x\n"
    "v_max3_u32 v236, v48, v49, v50\n"
    "v_max_u32 v236, v51, v236\n"
    "v_cmp_eq_u32 vcc, -1, v236\n"
    "s_cbranch_vccz kb_sk67_%=\n"
    "buffer_load_dwordx4 v[48:51], v255, s[16:19], 0 offen offset:0 sc1\n"
    "kb_sk67_%=:\n"
    "v_max3_u32 v236, v52, v53, v54\n"
    "v_max_u32 v236, v55, v236\n"
    "v_cmp_eq_u32 vcc, -1, v236\n"
    "s_cbranch_vccz kb_sk68_%=\n"
    "buffer_load_dwordx4 v[52:55], v255, s[16:19], 0 offen offset:1024 sc1\n"
    "kb_sk68_%=:\n"
    "v_max3_u32 v236, v56, v57, v58\n"
    "v_max_u32 v236, v59, v236\n"
    "v_cmp_eq_u32 vcc, -1, v236\n"
    "s_cbranch_vccz kb_sk69_%=\n"
    "buffer_load_dwordx4 v[56:59], v255, s[16:19], 0 offen offset:2048 sc1\n"
    "kb_sk69_%=:\n"
    "s_waitcnt vmcnt(0)\n"
    "s_branch kb_bk70_%=\n"
    "kb_sp77_%=:\n"
    "s_mov_b32 s48, 0\n"
    "kb_sp77_%=l:\n"
    "buffer_load_dword v18, v6, s[16:19], 0 offen sc1\n"
    "s_waitcnt vmcnt(0)\n"
    "v_cmp_ne_u32 vcc, s60, v18\n"
    "s_cbranch_vccz kb_ok78_%=\n"
    "s_add_u32 s48, s48, 1\n"
    "s_cmp_lt_u32 s48, 131072\n"
    "s_cbranch_scc1 kb_sp77_%=l\n"
    "s_branch kb_ok78_%=\n"
    "kb_sp80_%=:\n"
    "s_mov_b32 s48, 0\n"
    "kb_sp80_%=l:\n"
    "buffer_load_dword v17, v2, s[16:19], 0 offen sc1\n"
    "s_waitcnt vmcnt(0)\n"
    "v_cmp_ne_u32 vcc, s60, v17\n"
    "s_cbranch_vccz kb_ok81_%=\n"
    "s_add_u32 s48, s48, 1\n"
    "s_cmp_lt_u32 s48, 131072\n"
    "s_cbranch_scc1 kb_sp80_%=l\n"
    "s_branch kb_ok81_%=\n"
    "kb_sp82_%=:\n"
    "s_mov_b32 s48, 0\n"
    "kb_sp82_%=l:\n"
    "buffer_load_dword v17, v2, s[16:19], 0 offen sc1\n"
    "s_waitcnt vmcnt(0)\n"
    "v_cmp_ne_u32 vcc, s60, v17\n"
    "s_cbranch_vccz kb_ok83_%=\n"
    "s_add_u32 s48, s48, 1\n"
    "s_cmp_lt_u32 s48, 131072\n"
    "s_cbranch_scc1 kb_sp82_%=l\n"
    "s_branch kb_ok83_%=\n"
    "kb_sp84_%=:\n"
    "s_mov_b32 s48, 0\n"
    "kb_sp84_%=l:\n"
    "buffer_load_dword v17, v3, s[16:19], 0 offen sc1\n"
    "s_waitcnt vmcnt(0)\n"
    "v_cmp_ne_u32 vcc, s60, v17\n"
    "s_cbranch_vccz kb_ok85_%=\n"
    "s_add_u32 s48, s48, 1\n"
    "s_cmp_lt_u32 s48, 131072\n"
    "s_cbranch_scc1 kb_sp84_%=l\n"
    "s_branch kb_ok85_%=\n"
    "kb_code_end:\n"
    "kb_redo26_%=:\n"
    "s_waitcnt vmcnt(0) lgkmcnt(0)\n"
    "s_barrier\n"
    "s_mov_b32 s41, 1\n"
    "s_mov_b32 m0, s75\n"
    "v_and_b32 v48, 31, v0\n"
    "v_lshlrev_b32 v48, 2, v48\n"
    "kb_rl27_%=:\n"
    "buffer_load_dwordx4 v1, s[20:23], m0 offen sc1 lds\n"
    "s_add_u32 m0, m0, 8192\n"
    "s_cmp_lt_u32 m0, 147456\n"
    "s_cbranch_scc1 kb_rl27_%=\n"
    "s_branch kb_start16_%=\n"
    "kb_slow18_%=:\n"
    "v_mov_b32 v50, v48\n"
    "v_mov_b32 v51, v1\n"
    "v_mov_b32 v48, 0xff800000\n"
    "v_mov_b32 v49, 0xff800000\n"
    "s_mov_b32 s40, 0\n"
    "s_waitcnt vmcnt(0)\n"
    "s_barrier\n"
    "kb_p128_%=:\n"
    "ds_read_b32 v126, v50 offset:2176\n"
    "ds_read_b128 v[122:125], v51 offset:0\n"
    "s_waitcnt lgkmcnt(0)\n"
    "v_mfma_f32_32x32x4_2b_bf16 v[16:47], v[126:127], v[120:121], 0\n"
    "s_nop 15\n"
    "s_nop 1\n"
    "v_mfma_f32_32x32x16_bf16 v[16:31], v[122:125], v[112:115], v[16:31]\n"
    "v_mfma_f32_32x32x16_bf16 v[32:47], v[122:125], v[116:119], v[32:47]\n"
    "v_add_u32 v50, 2304, v50\n"
    "v_add_u32 v51, 2304, v51\n"
    "s_nop 8\n"
    "v_max3_f32 v48, v16, v17, v48\n"
    "v_max3_f32 v48, v18, v19, v48\n"
    "v_max3_f32 v48, v20, v21, v48\n"
    "v_max3_f32 v48, v22, v23, v48\n"
    "v_max3_f32 v48, v24, v25, v48\n"
    "v_max3_f32 v48, v26, v27, v48\n"
    "v_max3_f32 v48, v28, v29, v48\n"
    "v_max3_f32 v48, v30, v31, v48\n"
    "v_max3_f32 v49, v32, v33, v49\n"
    "v_max3_f32 v49, v34, v35, v49\n"
    "v_max3_f32 v49, v36, v37, v49\n"
    "v_max3_f32 v49, v38, v39, v49\n"
    "v_max3_f32 v49, v40, v41, v49\n"
    "v_max3_f32 v49, v42, v43, v49\n"
    "v_max3_f32 v49, v44, v45, v49\n"
    "v_max3_f32 v49, v46, v47, v49\n"
    "s_add_u32 s40, s40, 1\n"
    "s_cmp_lt_u32 s40, 64\n"
    "s_cbranch_scc1 kb_p128_%=\n"
    "v_mov_b32 v50, v48\n"
    "s_nop 1\n"
    "v_permlane32_swap_b32 v48, v50\n"
    "v_max_f32 v48, v48, v50\n"
    "v_mov_b32 v50, v49\n"
    "s_nop 1\n"
    "v_permlane32_swap_b32 v49, v50\n"
    "v_max_f32 v49, v49, v50\n"
    "s_mov_b32 s40, 0\n"
    "kb_p229_%=:\n"
    "ds_read_b32 v126, v139 offset:2176\n"
    "ds_read_b128 v[122:125], v137 offset:0\n"
    "s_mov_b64 exec, s[42:43]\n"
    "ds_read_b128 v[128:131], v138 offset:0\n"
    "s_mov_b64 exec, -1\n"
    "s_mov_b64 exec, s[42:43]\n"
    "ds_read_b128 v[132:135], v2 offset:0\n"
    "s_mov_b64 exec, -1\n"
    "s_waitcnt lgkmcnt(0)\n"
    "v_mfma_f32_32x32x4_2b_bf16 v[16:47], v[126:127], v[120:121], 0\n"
    "s_nop 15\n"
    "s_nop 1\n"
    "v_mfma_f32_32x32x16_bf16 v[16:31], v[122:125], v[112:115], v[16:31]\n"
    "v_mfma_f32_32x32x16_bf16 v[32:47], v[122:125], v[116:119], v[32:47]\n"
    "v_add_u32 v137, 4608, v137\n"
    "v_add_u32 v138, 4608, v138\n"
    "v_add_u32 v2, 4608, v2\n"
    "v_add_u32 v139, 4608, v139\n"
    "s_nop 6\n"
    "v_sub_f32 v16, v16, v48\n"
    "v_sub_f32 v17, v17, v48\n"
    "v_sub_f32 v18, v18, v48\n"
    "v_sub_f32 v19, v19, v48\n"
    "v_sub_f32 v20, v20, v48\n"
    "v_sub_f32 v21, v21, v48\n"
    "v_sub_f32 v22, v22, v48\n"
    "v_sub_f32 v23, v23, v48\n"
    "v_sub_f32 v24, v24, v48\n"
    "v_sub_f32 v25, v25, v48\n"
    "v_sub_f32 v26, v26, v48\n"
    "v_sub_f32 v27, v27, v48\n"
    "v_sub_f32 v28, v28, v48\n"
    "v_sub_f32 v29, v29, v48\n"
    "v_sub_f32 v30, v30, v48\n"
    "v_sub_f32 v31, v31, v48\n"
    "v_sub_f32 v32, v32, v49\n"
    "v_sub_f32 v33, v33, v49\n"
    "v_sub_f32 v34, v34, v49\n"
    "v_sub_f32 v35, v35, v49\n"
    "v_sub_f32 v36, v36, v49\n"
    "v_sub_f32 v37, v37, v49\n"
    "v_sub_f32 v38, v38, v49\n"
    "v_sub_f32 v39, v39, v49\n"
    "v_sub_f32 v40, v40, v49\n"
    "v_sub_f32 v41, v41, v49\n"
    "v_sub_f32 v42, v42, v49\n"
    "v_sub_f32 v43, v43, v49\n"
    "v_sub_f32 v44, v44, v49\n"
    "v_sub_f32 v45, v45, v49\n"
    "v_sub_f32 v46, v46, v49\n"
    "v_sub_f32 v47, v47, v49\n"
    "v_exp_f32 v16, v16\n"
    "v_exp_f32 v17, v17\n"
    "v_exp_f32 v18, v18\n"
    "v_exp_f32 v19, v19\n"
    "v_exp_f32 v20, v20\n"
    "v_exp_f32 v21, v21\n"
    "v_exp_f32 v22, v22\n"
    "v_exp_f32 v23, v23\n"
    "v_exp_f32 v24, v24\n"
    "v_exp_f32 v25, v25\n"
    "v_exp_f32 v26, v26\n"
    "v_exp_f32 v27, v27\n"
    "v_exp_f32 v28, v28\n"
    "v_exp_f32 v29, v29\n"
    "v_exp_f32 v30, v30\n"
    "v_exp_f32 v31, v31\n"
    "v_exp_f32 v32, v32\n"
    "v_exp_f32 v33, v33\n"
    "v_exp_f32 v34, v34\n"
    "v_exp_f32 v35, v35\n"
    "v_exp_f32 v36, v36\n"
    "v_exp_f32 v37, v37\n"
    "v_exp_f32 v38, v38\n"
    "v_exp_f32 v39, v39\n"
    "v_exp_f32 v40, v40\n"
    "v_exp_f32 v41, v41\n"
    "v_exp_f32 v42, v42\n"
    "v_exp_f32 v43, v43\n"
    "v_exp_f32 v44, v44\n"
    "v_exp_f32 v45, v45\n"
    "v_exp_f32 v46, v46\n"
    "v_exp_f32 v47, v47\n"
    "v_smfmac_f32_32x32x32_bf16 v[80:95], v[128:131], v[16:23], v136\n"
    "v_smfmac_f32_32x32x32_bf16 v[80:95], v[132:135], v[24:31], v136\n"
    "v_smfmac_f32_32x32x32_bf16 v[96:111], v[128:131], v[32:39], v136\n"
    "v_smfmac_f32_32x32x32_bf16 v[96:111], v[132:135], v[40:47], v136\n"
    "s_add_u32 s40, s40, 1\n"
    "s_cmp_lt_u32 s40, 32\n"
    "s_cbranch_scc1 kb_p229_%=\n"
    "s_barrier\n"
    "s_branch kb_epi17_%=\n"
    "kb_fin72_%=:\n"
    : "+{v0}"(tid) : "{s[0:1]}"(__builtin_amdgcn_kernarg_segment_ptr()), "{s2}"(blockIdx.x) : "v1", "v2", "v3", "v4", "v5", "v6", "v7", "v8", "v9", "v10", "v11", "v12", "v13", "v14", "v15", "v16", "v17", "v18", "v19", "v20", "v21", "v22", "v23", "v24", "v25", "v26", "v27", "v28", "v29", "v30", "v31", "v32", "v33", "v34", "v35", "v36", "v37", "v38", "v39", "v40", "v41", "v42", "v43", "v44", "v45", "v46", "v47", "v48", "v49", "v50", "v51", "v52", "v53", "v54", "v55", "v56", "v57", "v58", "v59", "v60", "v61", "v62", "v63", "v64", "v65", "v66", "v67", "v68", "v69", "v70", "v71", "v72", "v73", "v74", "v75", "v76", "v77", "v78", "v79", "v80", "v81", "v82", "v83", "v84", "v85", "v86", "v87", "v88", "v89", "v90", "v91", "v92", "v93", "v94", "v95", "v96", "v97", "v98", "v99", "v100", "v101", "v102", "v103", "v104", "v105", "v106", "v107", "v108", "v109", "v110", "v111", "v112", "v113", "v114", "v115", "v116", "v117", "v118", "v119", "v120", "v121", "v122", "v123", "v124", "v125", "v126", "v127", "v128", "v129", "v130", "v131", "v132", "v133", "v134", "v135", "v136", "v137", "v138", "v139", "v140", "v141", "v142", "v143", "v144", "v145", "v146", "v147", "v148", "v149", "v150", "v151", "v152", "v153", "v154", "v155", "v156", "v157", "v158", "v159", "v160", "v161", "v162", "v163", "v164", "v165", "v166", "v167", "v168", "v169", "v170", "v171", "v172", "v173", "v174", "v175", "v176", "v177", "v178", "v179", "v180", "v181", "v182", "v183", "v184", "v185", "v186", "v187", "v188", "v189", "v190", "v191", "v192", "v193", "v194", "v195", "v196", "v197", "v198", "v199", "v200", "v201", "v202", "v203", "v204", "v205", "v206", "v207", "v208", "v209", "v210", "v211", "v212", "v213", "v214", "v215", "v216", "v217", "v218", "v219", "v220", "v221", "v222", "v223", "v224", "v225", "v226", "v227", "v228", "v229", "v230", "v231", "v232", "v233", "v234", "v235", "v236", "v237", "v238", "v239", "v240", "v241", "v242", "v243", "v244", "v245", "v246", "v247", "v248", "v249", "v250", "v251", "v252", "v253", "v254", "v255", "s3", "s4", "s5", "s6", "s7", "s8", "s9", "s10", "s11", "s12", "s13", "s14", "s15", "s16", "s17", "s18", "s19", "s20", "s21", "s22", "s23", "s24", "s25", "s26", "s27", "s28", "s29", "s30", "s31", "s33", "s34", "s35", "s36", "s37", "s38", "s39", "s40", "s41", "s42", "s43", "s44", "s45", "s46", "s47", "s48", "s49", "s50", "s51", "s52", "s53", "s54", "s55", "s56", "s57", "s58", "s59", "s60", "s61", "s62", "s63", "s64", "s65", "s66", "s67", "s68", "s69", "s70", "s71", "s72", "s73", "s74", "s75", "s76", "s77", "s78", "s79", "s80", "s81", "s82", "s83", "s84", "s85", "s86", "s87", "s88", "s89", "s90", "s91", "s92", "s93", "s94", "s95", "s96", "s97", "s98", "s99", "vcc", "scc", "memory");
}

extern "C" void kernel_launch(void* const* d_in, const int* in_sizes, int n_in, void* d_out, int out_size, void* d_ws, size_t ws_size, hipStream_t stream) {
  const char* x = (const char*)d_in[0]; const char* wqkv = (const char*)d_in[1]; const char* wo = (const char*)d_in[2];
  const char* wg = (const char*)d_in[4]; const char* wu = (const char*)d_in[5]; const char* wd = (const char*)d_in[6];
  char* ws = (char*)d_ws; char* out = (char*)d_out;
  k_main<<<256, 512, 0, stream>>>(ws, x, wg, wu, wd, out, wqkv, wo);
}
```
